```python
import torch

def _softmax(x, axis=-1):
    return torch.softmax(x, dim=axis)
def _gelu(x):
    return torch.nn.functional.gelu(x, approximate="tanh")
def _silu(x):
    return torch.nn.functional.silu(x)
def _sigmoid(x):
    return torch.sigmoid(x)
def _layernorm(x, axis=-1, eps=1e-5):
    m = x.mean(dim=axis, keepdim=True)
    v = x.var(dim=axis, keepdim=True, unbiased=False)
    return (x - m) / torch.sqrt(v + eps)
def _scatter_add(tgt, idx, src, axis=0):
    return tgt.clone().index_add_(axis, idx, src)
def _segment_sum(src, seg, n):
    out = torch.zeros((n,) + src.shape[1:], dtype=src.dtype)
    return out.index_add_(0, seg, src)
def _pad(x, pw):
    flat = tuple(v for pair in reversed(pw) for v in pair)
    return torch.nn.functional.pad(x, flat)

kernel_name = 'standalone__gqa_block_cached__near_ridge'

def setup_inputs():

    _w = (lambda a: (lambda t: t.to(torch.bfloat16) if t.is_floating_point() else t)(torch.as_tensor(a)))
    B, Sq, D = 8, 192, 512
    Hq, Hkv, Sk, Dh = 32, 32, 1024, 128
    assert Hq % Hkv == 0, (Hq, Hkv)
    return {
        "x": _w((lambda *s: torch.randn(*s, dtype=torch.bfloat16) * 0.8)(B, Sq, D)),
        "k_cache": _w((lambda *s: torch.randn(*s, dtype=torch.bfloat16) * 0.8)(B, Hkv, Sk, Dh)),
        "v_cache": _w((lambda *s: torch.randn(*s, dtype=torch.bfloat16) * 0.05)(B, Hkv, Sk, Dh)),
        "wq": _w((lambda *s: torch.randn(*s, dtype=torch.bfloat16) * 0.8)(D, Hq * Dh) * (D ** -0.5)),
        "wo": _w((lambda *s: torch.randn(*s, dtype=torch.bfloat16) * 0.05)(Hq * Dh, D) * ((Hq * Dh) ** -0.5)),
    }

def reference(x, k_cache, v_cache, wq, wo):
    B, Sq, D = x.shape
    Hq, Dh = 32, 128
    Hkv = k_cache.shape[1]
    rep = Hq // Hkv
    q = (lambda x, *d: x.permute(*d))((x @ wq).reshape(B, Sq, Hq, Dh), 0, 2, 1, 3)
    k = (lambda x, n, axis: torch.repeat_interleave(x, n, dim=axis))(k_cache, rep, axis=1)
    v = (lambda x, n, axis: torch.repeat_interleave(x, n, dim=axis))(v_cache, rep, axis=1)
    scores = torch.einsum("bhqd,bhkd->bhqk", q, k) * (Dh ** -0.5)
    p = _softmax(scores, axis=-1)
    o = torch.einsum("bhqk,bhkd->bhqd", p, v)
    return (lambda x, *d: x.permute(*d))(o, 0, 2, 1, 3).reshape(B, Sq, Hq * Dh) @ wo
```

```cpp
#include <hip/hip_runtime.h>
#include <hip/hip_bf16.h>
typedef float f32x16 __attribute__((ext_vector_type(16)));
typedef float f32x2 __attribute__((ext_vector_type(2)));
typedef __bf16 bf16x8 __attribute__((ext_vector_type(8)));
typedef __bf16 bf16x2 __attribute__((ext_vector_type(2)));
typedef unsigned u32x4 __attribute__((ext_vector_type(4)));
#define PREFIX 0
__global__ __launch_bounds__(256) void gemm(const __bf16* __restrict__ A, const __bf16* __restrict__ Bm, __bf16* __restrict__ C, int M, int N, int K, float scale) {
  const int wid = threadIdx.x >> 6, l = threadIdx.x & 63, i = l & 31, h = l >> 5;
  const int tile = blockIdx.x * 4 + wid;
  const int tn = tile % (N / 32), tm = tile / (N / 32);
  f32x16 acc = {};
  for (int k0 = 0; k0 < K; k0 += 16) {
    bf16x8 a, b;
    for (int j = 0; j < 8; ++j) a[j] = Bm[(size_t)(k0 + 8 * h + j) * N + tn * 32 + i];
    b = *(const bf16x8*)(A + (size_t)(tm * 32 + i) * K + k0 + 8 * h);
    acc = __builtin_amdgcn_mfma_f32_32x32x16_bf16(a, b, acc, 0, 0, 0);
  }
  for (int r = 0; r < 16; ++r) { int n = (r & 3) + 8 * (r >> 2) + 4 * h; C[(size_t)(tm * 32 + i) * N + tn * 32 + n] = (__bf16)(acc[r] * scale); }
}
__global__ __launch_bounds__(192) void attn(const __bf16* __restrict__ Q, const __bf16* __restrict__ Kc, const __bf16* __restrict__ Vc, __bf16* __restrict__ O) {
  const int wid = (threadIdx.x >> 6) + 3 * (blockIdx.x >> 8), l = threadIdx.x & 63, i = l & 31, h = l >> 5;
  const int b = blockIdx.x & 7, hd = (blockIdx.x >> 3) & 31;
  const __bf16* q = Q + (size_t)(b * 192 + wid * 32 + i) * 4096 + hd * 128;
  const __bf16* k = Kc + (size_t)(b * 32 + hd) * 1024 * 128;
  const __bf16* v = Vc + (size_t)(b * 32 + hd) * 1024 * 128;
  bf16x8 qf[8];
  for (int s = 0; s < 8; ++s) qf[s] = *(const bf16x8*)(q + 16 * s + 8 * h);
  f32x16 o[4] = {};
  float m = -1e30f, lsum = 0.f;
  for (int t = 0; t < 1024; t += 32) {
    f32x16 s = {};
    for (int ss = 0; ss < 8; ++ss) { bf16x8 kf = *(const bf16x8*)(k + (size_t)(t + i) * 128 + 16 * ss + 8 * h); s = __builtin_amdgcn_mfma_f32_32x32x16_bf16(kf, qf[ss], s, 0, 0, 0); }
    float mx = s[0]; for (int r = 1; r < 16; ++r) mx = fmaxf(mx, s[r]);
    mx = fmaxf(mx, __shfl_xor(mx, 32));
    float mn = fmaxf(m, mx), f = exp2f(m - mn); m = mn;
    float rs = 0.f; for (int r = 0; r < 16; ++r) { s[r] = exp2f(s[r] - mn); rs += s[r]; }
    rs += __shfl_xor(rs, 32); lsum = lsum * f + rs;
    for (int db = 0; db < 4; ++db) for (int r = 0; r < 16; ++r) o[db][r] *= f;
    for (int ks = 0; ks < 2; ++ks) {
      bf16x8 p; for (int j = 0; j < 8; ++j) p[j] = (__bf16)s[8 * ks + j];
      for (int db = 0; db < 4; ++db) {
        bf16x8 vf; for (int j = 0; j < 8; ++j) vf[j] = v[(size_t)(t + 16 * ks + 8 * (j >> 2) + 4 * h + (j & 3)) * 128 + 32 * db + i];
        o[db] = __builtin_amdgcn_mfma_f32_32x32x16_bf16(vf, p, o[db], 0, 0, 0);
      }
    }
  }
  float il = 1.f / lsum;
  __bf16* op = O + (size_t)(b * 192 + wid * 32 + i) * 4096 + hd * 128;
  for (int db = 0; db < 4; ++db) for (int r = 0; r < 16; ++r) op[32 * db + (r & 3) + 8 * (r >> 2) + 4 * h] = (__bf16)(o[db][r] * il);
}
template <int NT, int DEPTH>
__global__ __launch_bounds__(256, 1) void streamk(const u32x4* __restrict__ K, const u32x4* __restrict__ V, unsigned* __restrict__ out) {
  extern __shared__ __attribute__((aligned(16))) char lds[];
  const int bh = (blockIdx.x & 7) * 32 + (blockIdx.x >> 3);
  const int wid = __builtin_amdgcn_readfirstlane(threadIdx.x >> 6), lane = threadIdx.x & 63;
  __amdgpu_buffer_rsrc_t sk = __builtin_amdgcn_make_buffer_rsrc((void*)(K + (size_t)bh * 16384), (short)0, 262144, 0x00020000);
  __amdgpu_buffer_rsrc_t sv = __builtin_amdgcn_make_buffer_rsrc((void*)(V + (size_t)bh * 16384), (short)0, 262144, 0x00020000);
  const unsigned voff = lane * 16;
  const unsigned ldsb = (unsigned)(size_t)lds + wid * 32768;
  #pragma unroll 1
  for (int t = 0; t < 16; ++t) {
    const unsigned m0a = ldsb + (t & 3) * 8192, so = (wid >> 1) * 131072 + t * 8192 + (wid & 1) * 4096;
    if (NT) asm volatile("s_mov_b32 m0, %0\n\ts_nop 0\n\tbuffer_load_dwordx4 %1, %2, %4 offen nt lds\n\tbuffer_load_dwordx4 %1, %2, %4 offen offset:1024 nt lds\n\tbuffer_load_dwordx4 %1, %2, %4 offen offset:2048 nt lds\n\tbuffer_load_dwordx4 %1, %2, %4 offen offset:3072 nt lds\n\t"
                 "s_add_u32 m0, m0, 4096\n\ts_nop 0\n\tbuffer_load_dwordx4 %1, %3, %4 offen nt lds\n\tbuffer_load_dwordx4 %1, %3, %4 offen offset:1024 nt lds\n\tbuffer_load_dwordx4 %1, %3, %4 offen offset:2048 nt lds\n\tbuffer_load_dwordx4 %1, %3, %4 offen offset:3072 nt lds\n\t"
                 "s_waitcnt vmcnt(%5)" :: "s"(m0a), "v"(voff), "s"(sk), "s"(sv), "s"(so), "i"(DEPTH) : "m0", "memory", "scc");
    else asm volatile("s_mov_b32 m0, %0\n\ts_nop 0\n\tbuffer_load_dwordx4 %1, %2, %4 offen lds\n\tbuffer_load_dwordx4 %1, %2, %4 offen offset:1024 lds\n\tbuffer_load_dwordx4 %1, %2, %4 offen offset:2048 lds\n\tbuffer_load_dwordx4 %1, %2, %4 offen offset:3072 lds\n\t"
                 "s_add_u32 m0, m0, 4096\n\ts_nop 0\n\tbuffer_load_dwordx4 %1, %3, %4 offen lds\n\tbuffer_load_dwordx4 %1, %3, %4 offen offset:1024 lds\n\tbuffer_load_dwordx4 %1, %3, %4 offen offset:2048 lds\n\tbuffer_load_dwordx4 %1, %3, %4 offen offset:3072 lds\n\t"
                 "s_waitcnt vmcnt(%5)" :: "s"(m0a), "v"(voff), "s"(sk), "s"(sv), "s"(so), "i"(DEPTH) : "m0", "memory", "scc");
  }
  asm volatile("s_waitcnt vmcnt(0)" ::: "memory");
  out[blockIdx.x * 256 + threadIdx.x] = *(const unsigned*)(lds + threadIdx.x * 4);
}
__global__ void nullk(unsigned* out) { if (threadIdx.x == 0) out[blockIdx.x] = 1; }
extern "C" void kernel_launch(void* const* d_in, const int* in_sizes, int n_in, void* d_out, int out_size, void* d_ws, size_t ws_size, hipStream_t stream) {
  __bf16* Q = (__bf16*)d_ws; __bf16* O = Q + 1536 * 4096; unsigned* junk = (unsigned*)(O + 1536 * 4096);
  gemm<<<(1536 / 32) * (4096 / 32) / 4, 256, 0, stream>>>((const __bf16*)d_in[0], (const __bf16*)d_in[3], Q, 1536, 4096, 512, 0.12751743082f);
  attn<<<512, 192, 0, stream>>>(Q, (const __bf16*)d_in[1], (const __bf16*)d_in[2], O);
  gemm<<<(1536 / 32) * (512 / 32) / 4, 256, 0, stream>>>(O, (const __bf16*)d_in[4], (__bf16*)d_out, 1536, 512, 4096, 1.f);
}
```

```cpp
#include <hip/hip_runtime.h>
#include <hip/hip_bf16.h>
typedef float f32x16 __attribute__((ext_vector_type(16)));
typedef float f32x2 __attribute__((ext_vector_type(2)));
typedef __bf16 bf16x8 __attribute__((ext_vector_type(8)));
typedef __bf16 bf16x2 __attribute__((ext_vector_type(2)));
typedef unsigned u32x4 __attribute__((ext_vector_type(4)));
#define PREFIX 0
__global__ __launch_bounds__(256) void gemm(const __bf16* __restrict__ A, const __bf16* __restrict__ Bm, __bf16* __restrict__ C, int M, int N, int K, float scale) {
  const int wid = threadIdx.x >> 6, l = threadIdx.x & 63, i = l & 31, h = l >> 5;
  const int tile = blockIdx.x * 4 + wid;
  const int tn = tile % (N / 32), tm = tile / (N / 32);
  f32x16 acc = {};
  for (int k0 = 0; k0 < K; k0 += 16) {
    bf16x8 a, b;
    for (int j = 0; j < 8; ++j) a[j] = Bm[(size_t)(k0 + 8 * h + j) * N + tn * 32 + i];
    b = *(const bf16x8*)(A + (size_t)(tm * 32 + i) * K + k0 + 8 * h);
    acc = __builtin_amdgcn_mfma_f32_32x32x16_bf16(a, b, acc, 0, 0, 0);
  }
  for (int r = 0; r < 16; ++r) { int n = (r & 3) + 8 * (r >> 2) + 4 * h; C[(size_t)(tm * 32 + i) * N + tn * 32 + n] = (__bf16)(acc[r] * scale); }
}
__global__ __launch_bounds__(192) void attn(const __bf16* __restrict__ Q, const __bf16* __restrict__ Kc, const __bf16* __restrict__ Vc, __bf16* __restrict__ O) {
  const int wid = (threadIdx.x >> 6) + 3 * (blockIdx.x >> 8), l = threadIdx.x & 63, i = l & 31, h = l >> 5;
  const int b = blockIdx.x & 7, hd = (blockIdx.x >> 3) & 31;
  const __bf16* q = Q + (size_t)(b * 192 + wid * 32 + i) * 4096 + hd * 128;
  const __bf16* k = Kc + (size_t)(b * 32 + hd) * 1024 * 128;
  const __bf16* v = Vc + (size_t)(b * 32 + hd) * 1024 * 128;
  bf16x8 qf[8];
  for (int s = 0; s < 8; ++s) qf[s] = *(const bf16x8*)(q + 16 * s + 8 * h);
  f32x16 o[4] = {};
  float m = -1e30f, lsum = 0.f;
  for (int t = 0; t < 1024; t += 32) {
    f32x16 s = {};
    for (int ss = 0; ss < 8; ++ss) { bf16x8 kf = *(const bf16x8*)(k + (size_t)(t + i) * 128 + 16 * ss + 8 * h); s = __builtin_amdgcn_mfma_f32_32x32x16_bf16(kf, qf[ss], s, 0, 0, 0); }
    float mx = s[0]; for (int r = 1; r < 16; ++r) mx = fmaxf(mx, s[r]);
    mx = fmaxf(mx, __shfl_xor(mx, 32));
    float mn = fmaxf(m, mx), f = exp2f(m - mn); m = mn;
    float rs = 0.f; for (int r = 0; r < 16; ++r) { s[r] = exp2f(s[r] - mn); rs += s[r]; }
    rs += __shfl_xor(rs, 32); lsum = lsum * f + rs;
    for (int db = 0; db < 4; ++db) for (int r = 0; r < 16; ++r) o[db][r] *= f;
    for (int ks = 0; ks < 2; ++ks) {
      bf16x8 p; for (int j = 0; j < 8; ++j) p[j] = (__bf16)s[8 * ks + j];
      for (int db = 0; db < 4; ++db) {
        bf16x8 vf; for (int j = 0; j < 8; ++j) vf[j] = v[(size_t)(t + 16 * ks + 8 * (j >> 2) + 4 * h + (j & 3)) * 128 + 32 * db + i];
        o[db] = __builtin_amdgcn_mfma_f32_32x32x16_bf16(vf, p, o[db], 0, 0, 0);
      }
    }
  }
  float il = 1.f / lsum;
  __bf16* op = O + (size_t)(b * 192 + wid * 32 + i) * 4096 + hd * 128;
  for (int db = 0; db < 4; ++db) for (int r = 0; r < 16; ++r) op[32 * db + (r & 3) + 8 * (r >> 2) + 4 * h] = (__bf16)(o[db][r] * il);
}
#define CFG -1,0,0,0
#define SIDE 4

typedef unsigned long long u64;
#define LD(POL, OFF) "buffer_load_dwordx4 %1, %2, %3 offen offset:" #OFF " " POL " lds\n\t"
#define DMA2(POL, m0a, voff, srd, so) asm volatile("s_mov_b32 m0, %0\n\ts_nop 0\n\t" LD(POL,0) LD(POL,1024) :: "s"(m0a), "v"(voff), "s"(srd), "s"(so) : "m0", "memory")
template <int KVPOL, int WOPRE, int WOPOL, int SHARE>
__global__ __launch_bounds__(512, 2) void sk(const u32x4* K, const u32x4* V, const u32x4* wo) {
  extern __shared__ __attribute__((aligned(16))) char lds[];
  const int blk = blockIdx.x, x = blk & 7, idx = blk >> 3;
  const int bh = x * 32 + idx;
  const int wid = __builtin_amdgcn_readfirstlane(threadIdx.x >> 6), lane = threadIdx.x & 63;
  const int cb = idx & 7;
  const unsigned ldsb = (unsigned)(size_t)lds;
  if (wid >= 4) {
    if (KVPOL >= 0) {
    __amdgpu_buffer_rsrc_t sK = __builtin_amdgcn_make_buffer_rsrc((void*)(K + (size_t)bh * 16384), (short)0, 262144, 0x00020000);
    __amdgpu_buffer_rsrc_t sV = __builtin_amdgcn_make_buffer_rsrc((void*)(V + (size_t)bh * 16384), (short)0, 262144, 0x00020000);
    const unsigned voff = lane * 16;
    #pragma unroll 1
    for (int u = 0; u < 32; ++u) {
      const unsigned m0a = ldsb + (u & 7) * 8192 + (wid - 4) * 2048, so = u * 8192 + (wid - 4) * 2048;
      if (KVPOL == 1) { DMA2("nt", m0a, voff, sK, so); DMA2("nt", m0a + 65536, voff, sV, so); }
      else { DMA2("", m0a, voff, sK, so); DMA2("", m0a + 65536, voff, sV, so); }
      asm volatile("s_waitcnt vmcnt(8)" ::: "memory");
    }
    }
  } else if (wid < 2 && WOPRE) {
    __amdgpu_buffer_rsrc_t swo = __builtin_amdgcn_make_buffer_rsrc((void*)(wo), (short)0, 4 << 20, 0x00020000);
    const unsigned wvoff = (lane >> 3) * 1024 + (lane & 7) * 16 + cb * 128;
    const u64 c0 = __builtin_amdgcn_s_memtime();
    #pragma unroll 1
    for (int i = 0; i < WOPRE; ++i) {
      int n = 0; while ((long long)(__builtin_amdgcn_s_memtime() - c0) < 4000 + (long long)i * (28000 / (WOPRE ? WOPRE : 1)) && ++n < 100000) __builtin_amdgcn_s_sleep(1);
      const unsigned m0a = ldsb + 131072 + wid * 16384 + (i & 15) * 1024, so = SHARE == 4 ? (x * 64 + wid * 32 + (i & 31)) * 8192 : (wid * 256 + i) * 8192;
      if (WOPOL == 0) asm volatile("s_mov_b32 m0, %0\n\ts_nop 0\n\tbuffer_load_dwordx4 %1, %2, %3 offen lds" :: "s"(m0a), "v"(wvoff), "s"(swo), "s"(so) : "m0", "memory");
      else asm volatile("s_mov_b32 m0, %0\n\ts_nop 0\n\tbuffer_load_dwordx4 %1, %2, %3 offen nt lds" :: "s"(m0a), "v"(wvoff), "s"(swo), "s"(so) : "m0", "memory");
    }
  }
  asm volatile("s_waitcnt vmcnt(0)" ::: "memory");
}
__global__ void stampk(u64* t) { if (threadIdx.x == 0) *t = __builtin_amdgcn_s_memrealtime(); }
__global__ void padk(const u64* t) { u64 a = *t; int n = 0; while (__builtin_amdgcn_s_memrealtime() - a < 40000ull && ++n < 2000000) __builtin_amdgcn_s_sleep(1); }
extern "C" void kernel_launch(void* const* d_in, const int* in_sizes, int n_in, void* d_out, int out_size, void* d_ws, size_t ws_size, hipStream_t stream) {
  __bf16* Q = (__bf16*)d_ws; __bf16* O = Q + 1536 * 4096; u64* junk = (u64*)(O + 1536 * 4096);
  hipFuncSetAttribute((const void*)sk<CFG>, hipFuncAttributeMaxDynamicSharedMemorySize, 163840);
  sk<CFG><<<256, 512, 163840, stream>>>((const u32x4*)d_in[1], (const u32x4*)d_in[2], (const u32x4*)d_in[SIDE]);
  stampk<<<1, 64, 0, stream>>>(junk);
  gemm<<<(1536 / 32) * (4096 / 32) / 4, 256, 0, stream>>>((const __bf16*)d_in[0], (const __bf16*)d_in[3], Q, 1536, 4096, 512, 0.12751743082f);
  attn<<<512, 192, 0, stream>>>(Q, (const __bf16*)d_in[1], (const __bf16*)d_in[2], O);
  gemm<<<(1536 / 32) * (512 / 32) / 4, 256, 0, stream>>>(O, (const __bf16*)d_in[4], (__bf16*)d_out, 1536, 512, 4096, 1.f);
  padk<<<1, 64, 0, stream>>>(junk);
}
```

```cpp
#include <hip/hip_runtime.h>
__global__ __launch_bounds__(512, 2) void fused_k(const void* x, const void* k, const void* v, const void* wq, const void* wo, void* out, void* ws) {
  const int wid = __builtin_amdgcn_readfirstlane(threadIdx.x >> 6);
  asm volatile(
"s_load_dwordx8 s[20:27], %0, 0\n"
"s_load_dwordx4 s[28:31], %0, 32\n"
"s_load_dwordx2 s[32:33], %0, 48\n"
"s_mov_b32 s39, %2\n"
"s_mov_b32 s43, %1\n"
"v_mbcnt_lo_u32_b32 v255, -1, 0\n"
"v_mbcnt_hi_u32_b32 v255, -1, v255\n"
"s_and_b32 s38, s39, 3\n"
"s_and_b32 s34, s43, 7\n"
"s_lshr_b32 s35, s43, 3\n"
"s_lshr_b32 s40, s35, 3\n"
"s_and_b32 s41, s35, 7\n"
"s_and_b32 s36, s34, 1\n"
"s_lshl2_add_u32 s36, s36, s40\n"
"s_lshr_b32 s37, s34, 1\n"
"s_lshl3_add_u32 s37, s37, s41\n"
"s_lshl_b32 s42, s36, 5\n"
"s_add_u32 s42, s42, s37\n"
"s_and_b32 s76, s36, 4\n"
"s_lshr_b32 s77, s41, 2\n"
"s_add_u32 s76, s76, s77\n"
"s_bfe_u32 s77, s34, 0x10001\n"
"s_and_b32 s41, s41, 3\n"
"s_lshl1_add_u32 s41, s41, s77\n"
"s_xor_b32 s41, s41, 6\n"
"s_lshr_b32 s34, s34, 2\n"
"s_lshl1_add_u32 s34, s34, s76\n"
"s_cmp_lt_u32 s39, 4\n"
"s_cbranch_scc1 .LkLCOMP\n"
"s_barrier\n"
"v_and_b32 v24, 7, v255\n"
"v_lshrrev_b32 v25, 4, v255\n"
"v_xor_b32 v26, v24, v25\n"
"v_lshrrev_b32 v27, 3, v255\n"
"v_lshlrev_b32 v28, 10, v27\n"
"v_lshl_add_u32 v29, v26, 4, v28\n"
"v_mov_b32 v30, s38\n"
"v_mul_u32_u24 v31, 0xc000, v30\n"
"v_add_u32 v6, v29, v31\n"
"v_add_u32 v32, 4, v25\n"
"v_xor_b32 v33, v24, v32\n"
"v_lshl_add_u32 v34, v33, 4, v28\n"
"v_add_u32 v35, v34, v31\n"
"v_add_u32 v7, 0x1c00, v35\n"
"v_add_u32 v8, 0x3800, v6\n"
"v_add_u32 v9, 0x5400, v35\n"
"v_add_u32 v10, 0x8000, v6\n"
"v_add_u32 v11, 0x9c00, v35\n"
"v_and_b32 v36, 15, v255\n"
"v_and_b32 v37, 1, v30\n"
"v_lshl_add_u32 v38, v25, 2, v37\n"
"v_xor_b32 v39, v36, v38\n"
"v_lshlrev_b32 v40, 13, v25\n"
"v_lshl_add_u32 v41, v39, 4, v40\n"
"v_lshl_add_u32 v4, s38, 16, v41\n"
"v_add_u32 v5, 0x7c00, v4\n"
"v_and_b32 v42, 1, v25\n"
"v_lshl_add_u32 v43, v37, 2, v42\n"
"v_xor_b32 v44, v24, v43\n"
"v_lshrrev_b32 v45, 5, v255\n"
"v_and_b32 v46, 3, v27\n"
"v_lshlrev_b32 v47, 8, v46\n"
"v_lshl_add_u32 v48, v45, 7, v47\n"
"v_lshl_add_u32 v49, s38, 11, v48\n"
"v_lshl_add_u32 v0, v44, 4, v49\n"
"v_lshl_add_u32 v50, v37, 2, 2\n"
"v_add_u32 v51, v50, v42\n"
"v_xor_b32 v52, v24, v51\n"
"v_lshl_add_u32 v1, v52, 4, v49\n"
"v_lshlrev_b32 v53, 1, v42\n"
"v_xor_b32 v54, v24, v53\n"
"v_lshl_add_u32 v2, v54, 4, v49\n"
"v_add_u32 v55, 4, v53\n"
"v_xor_b32 v56, v24, v55\n"
"v_lshl_add_u32 v3, v56, 4, v49\n"
"v_and_b32 v57, 3, v255\n"
"v_mul_u32_u24 v13, 0x104, v57\n"
"v_mov_b32 v14, 0xb0000000\n"
"s_getpc_b64 s[72:73]\n"
".LkLTPC:\n"
"s_add_u32 s72, s72, .LkLTAB-.LkLTPC\n"
"s_addc_u32 s73, s73, 0\n"
"s_mov_b32 s74, 256\n"
"s_mov_b32 s75, 0x20000\n"
"s_mul_i32 s82, s38, 6144\n"
"s_add_u32 s82, s82, 83968\n"
"s_lshl_b32 s77, s38, 11\n"
"s_add_u32 s77, s77, 2048\n"
"s_lshl_b32 s76, s38, 8\n"
"s_add_u32 s76, s76, 1024\n"
"s_and_b32 s80, s43, 7\n"
"s_lshl_b32 s80, s80, 7\n"
"s_mul_i32 s56, s36, 196608\n"
"s_lshl_b32 s52, s37, 8\n"
"s_lshl_b32 s44, s42, 18\n"
"s_waitcnt lgkmcnt(0)\n"
"s_add_u32 s56, s20, s56\n"
"s_addc_u32 s57, s21, 0\n"
"s_mov_b32 s58, 0x30000\n"
"s_mov_b32 s59, 0x20000\n"
"s_add_u32 s52, s26, s52\n"
"s_addc_u32 s53, s27, 0\n"
"s_mov_b32 s54, 0x3fe100\n"
"s_mov_b32 s55, 0x20000\n"
"s_add_u32 s48, s24, s44\n"
"s_addc_u32 s49, s25, 0\n"
"s_sub_u32 s48, s48, 8192\n"
"s_subb_u32 s49, s49, 0\n"
"s_mov_b32 s50, 270336\n"
"s_mov_b32 s51, 0x20000\n"
"s_add_u32 s44, s22, s44\n"
"s_addc_u32 s45, s23, 0\n"
"s_mov_b32 s46, 0x40000\n"
"s_mov_b32 s47, 0x20000\n"
"s_xor_b32 s87, s35, s40\n"
"s_bitcmp1_b32 s87, 0\n"
"s_cbranch_scc0 .LkLHQ0\n"
"s_mov_b32 s78, 0\n"
"s_add_u32 m0, s77, 0\n"
"s_nop 0\n"
"buffer_load_dwordx4 v0, s[44:47], s78 offen nt lds\n"
"buffer_load_dwordx4 v1, s[44:47], s78 offen offset:1024 nt lds\n"
"s_mov_b32 s78, 8192\n"
"s_add_u32 m0, s77, 8192\n"
"s_nop 0\n"
"buffer_load_dwordx4 v0, s[44:47], s78 offen nt lds\n"
"buffer_load_dwordx4 v1, s[44:47], s78 offen offset:1024 nt lds\n"
"s_add_u32 m0, s77, 65536\n"
"s_nop 0\n"
"buffer_load_dwordx4 v2, s[48:51], s78 offen nt lds\n"
"buffer_load_dwordx4 v3, s[48:51], s78 offen offset:1024 nt lds\n"
".LkLHQ0:\n"
"s_add_u32 m0, s82, 24576\n"
"s_lshl_b32 s81, s80, 12\n"
"buffer_load_dwordx4 v6, s[56:59], s80 offen lds\n"
"buffer_load_dwordx4 v7, s[56:59], s80 offen offset:1024 lds\n"
"buffer_load_dwordx4 v8, s[56:59], s80 offen offset:2048 lds\n"
"buffer_load_dwordx4 v9, s[56:59], s80 offen offset:3072 lds\n"
"s_add_u32 m0, s82, 28672\n"
"s_nop 0\n"
"buffer_load_dwordx4 v10, s[56:59], s80 offen lds\n"
"buffer_load_dwordx4 v11, s[56:59], s80 offen offset:1024 lds\n"
"s_add_u32 m0, s77, 57344\n"
"s_add_u32 s80, s80, 128\n"
"s_and_b32 s80, s80, 1023\n"
"buffer_load_dwordx4 v4, s[52:55], s81 offen lds\n"
"buffer_load_dwordx4 v5, s[52:55], s81 offen offset:1024 lds\n"
"s_sub_u32 m0, s76, 4\n"
"s_nop 0\n"
"buffer_load_dword off, s[72:75], 0 offset:4 lds\n"
"s_waitcnt vmcnt(12)\n"
"s_add_u32 m0, s77, 24576\n"
"s_add_u32 s81, s81, 262144\n"
"buffer_load_dwordx4 v4, s[52:55], s81 offen lds\n"
"buffer_load_dwordx4 v5, s[52:55], s81 offen offset:1024 lds\n"
"s_sub_u32 m0, s76, 8\n"
"s_nop 0\n"
"buffer_load_dword off, s[72:75], 0 offset:8 lds\n"
"s_waitcnt vmcnt(12)\n"
"s_add_u32 m0, s82, 49152\n"
"s_lshl_b32 s81, s80, 12\n"
"buffer_load_dwordx4 v6, s[56:59], s80 offen lds\n"
"buffer_load_dwordx4 v7, s[56:59], s80 offen offset:1024 lds\n"
"buffer_load_dwordx4 v8, s[56:59], s80 offen offset:2048 lds\n"
"buffer_load_dwordx4 v9, s[56:59], s80 offen offset:3072 lds\n"
"s_add_u32 m0, s82, 53248\n"
"s_nop 0\n"
"buffer_load_dwordx4 v10, s[56:59], s80 offen lds\n"
"buffer_load_dwordx4 v11, s[56:59], s80 offen offset:1024 lds\n"
"s_add_u32 m0, s77, 32768\n"
"s_add_u32 s80, s80, 128\n"
"s_and_b32 s80, s80, 1023\n"
"buffer_load_dwordx4 v4, s[52:55], s81 offen lds\n"
"buffer_load_dwordx4 v5, s[52:55], s81 offen offset:1024 lds\n"
"s_sub_u32 m0, s76, 12\n"
"s_nop 0\n"
"buffer_load_dword off, s[72:75], 0 offset:12 lds\n"
"s_waitcnt vmcnt(12)\n"
"s_add_u32 m0, s77, 40960\n"
"s_add_u32 s81, s81, 262144\n"
"buffer_load_dwordx4 v4, s[52:55], s81 offen lds\n"
"buffer_load_dwordx4 v5, s[52:55], s81 offen offset:1024 lds\n"
"s_sub_u32 m0, s76, 16\n"
"s_nop 0\n"
"buffer_load_dword off, s[72:75], 0 offset:16 lds\n"
"s_waitcnt vmcnt(12)\n"
"s_add_u32 m0, s82, 0\n"
"s_lshl_b32 s81, s80, 12\n"
"buffer_load_dwordx4 v6, s[56:59], s80 offen lds\n"
"buffer_load_dwordx4 v7, s[56:59], s80 offen offset:1024 lds\n"
"buffer_load_dwordx4 v8, s[56:59], s80 offen offset:2048 lds\n"
"buffer_load_dwordx4 v9, s[56:59], s80 offen offset:3072 lds\n"
"s_add_u32 m0, s82, 4096\n"
"s_nop 0\n"
"buffer_load_dwordx4 v10, s[56:59], s80 offen lds\n"
"buffer_load_dwordx4 v11, s[56:59], s80 offen offset:1024 lds\n"
"s_add_u32 m0, s77, 49152\n"
"s_add_u32 s80, s80, 128\n"
"s_and_b32 s80, s80, 1023\n"
"buffer_load_dwordx4 v4, s[52:55], s81 offen lds\n"
"buffer_load_dwordx4 v5, s[52:55], s81 offen offset:1024 lds\n"
"s_sub_u32 m0, s76, 20\n"
"s_nop 0\n"
"buffer_load_dword off, s[72:75], 0 offset:20 lds\n"
"s_waitcnt vmcnt(12)\n"
"s_barrier\n"
"s_add_u32 m0, s77, 57344\n"
"s_add_u32 s81, s81, 262144\n"
"buffer_load_dwordx4 v4, s[52:55], s81 offen lds\n"
"buffer_load_dwordx4 v5, s[52:55], s81 offen offset:1024 lds\n"
"s_sub_u32 m0, s76, 24\n"
"s_nop 0\n"
"buffer_load_dword off, s[72:75], 0 offset:24 lds\n"
"s_waitcnt vmcnt(12)\n"
"s_add_u32 m0, s82, 24576\n"
"s_lshl_b32 s81, s80, 12\n"
"buffer_load_dwordx4 v6, s[56:59], s80 offen lds\n"
"buffer_load_dwordx4 v7, s[56:59], s80 offen offset:1024 lds\n"
"buffer_load_dwordx4 v8, s[56:59], s80 offen offset:2048 lds\n"
"buffer_load_dwordx4 v9, s[56:59], s80 offen offset:3072 lds\n"
"s_add_u32 m0, s82, 28672\n"
"s_nop 0\n"
"buffer_load_dwordx4 v10, s[56:59], s80 offen lds\n"
"buffer_load_dwordx4 v11, s[56:59], s80 offen offset:1024 lds\n"
"s_add_u32 m0, s77, 24576\n"
"s_add_u32 s80, s80, 128\n"
"s_and_b32 s80, s80, 1023\n"
"buffer_load_dwordx4 v4, s[52:55], s81 offen lds\n"
"buffer_load_dwordx4 v5, s[52:55], s81 offen offset:1024 lds\n"
"s_sub_u32 m0, s76, 28\n"
"s_nop 0\n"
"buffer_load_dword off, s[72:75], 0 offset:28 lds\n"
"s_xor_b32 s87, s35, s40\n"
"s_bitcmp1_b32 s87, 0\n"
"s_cbranch_scc0 .LkLHS06\n"
"s_mov_b32 s78, 16384\n"
"s_add_u32 m0, s77, 16384\n"
"s_nop 0\n"
"buffer_load_dwordx4 v0, s[44:47], s78 offen nt lds\n"
"buffer_load_dwordx4 v1, s[44:47], s78 offen offset:1024 nt lds\n"
"s_add_u32 m0, s77, 73728\n"
"s_nop 0\n"
"buffer_load_dwordx4 v2, s[48:51], s78 offen nt lds\n"
"buffer_load_dwordx4 v3, s[48:51], s78 offen offset:1024 nt lds\n"
".LkLHS06:\n"
"s_waitcnt vmcnt(12)\n"
"s_barrier\n"
"s_add_u32 m0, s77, 32768\n"
"s_add_u32 s81, s81, 262144\n"
"buffer_load_dwordx4 v4, s[52:55], s81 offen lds\n"
"buffer_load_dwordx4 v5, s[52:55], s81 offen offset:1024 lds\n"
"s_sub_u32 m0, s76, 32\n"
"s_nop 0\n"
"buffer_load_dword off, s[72:75], 0 offset:32 lds\n"
"s_waitcnt vmcnt(12)\n"
"s_add_u32 m0, s82, 49152\n"
"s_lshl_b32 s81, s80, 12\n"
"buffer_load_dwordx4 v6, s[56:59], s80 offen lds\n"
"buffer_load_dwordx4 v7, s[56:59], s80 offen offset:1024 lds\n"
"buffer_load_dwordx4 v8, s[56:59], s80 offen offset:2048 lds\n"
"buffer_load_dwordx4 v9, s[56:59], s80 offen offset:3072 lds\n"
"s_add_u32 m0, s82, 53248\n"
"s_nop 0\n"
"buffer_load_dwordx4 v10, s[56:59], s80 offen lds\n"
"buffer_load_dwordx4 v11, s[56:59], s80 offen offset:1024 lds\n"
"s_add_u32 m0, s77, 40960\n"
"s_add_u32 s80, s80, 128\n"
"s_and_b32 s80, s80, 1023\n"
"buffer_load_dwordx4 v4, s[52:55], s81 offen lds\n"
"buffer_load_dwordx4 v5, s[52:55], s81 offen offset:1024 lds\n"
"s_sub_u32 m0, s76, 36\n"
"s_nop 0\n"
"buffer_load_dword off, s[72:75], 0 offset:36 lds\n"
"s_waitcnt vmcnt(12)\n"
"s_barrier\n"
"s_add_u32 m0, s77, 49152\n"
"s_add_u32 s81, s81, 262144\n"
"buffer_load_dwordx4 v4, s[52:55], s81 offen lds\n"
"buffer_load_dwordx4 v5, s[52:55], s81 offen offset:1024 lds\n"
"s_sub_u32 m0, s76, 40\n"
"s_nop 0\n"
"buffer_load_dword off, s[72:75], 0 offset:40 lds\n"
"s_xor_b32 s87, s35, s40\n"
"s_bitcmp1_b32 s87, 0\n"
"s_cbranch_scc1 .LkLHS19\n"
"s_mov_b32 s78, 0\n"
"s_add_u32 m0, s77, 0\n"
"s_nop 0\n"
"buffer_load_dwordx4 v0, s[44:47], s78 offen nt lds\n"
"buffer_load_dwordx4 v1, s[44:47], s78 offen offset:1024 nt lds\n"
".LkLHS19:\n"
"s_waitcnt vmcnt(12)\n"
"s_add_u32 m0, s82, 0\n"
"s_lshl_b32 s81, s80, 12\n"
"buffer_load_dwordx4 v6, s[56:59], s80 offen lds\n"
"buffer_load_dwordx4 v7, s[56:59], s80 offen offset:1024 lds\n"
"buffer_load_dwordx4 v8, s[56:59], s80 offen offset:2048 lds\n"
"buffer_load_dwordx4 v9, s[56:59], s80 offen offset:3072 lds\n"
"s_add_u32 m0, s82, 4096\n"
"s_nop 0\n"
"buffer_load_dwordx4 v10, s[56:59], s80 offen lds\n"
"buffer_load_dwordx4 v11, s[56:59], s80 offen offset:1024 lds\n"
"s_add_u32 m0, s77, 57344\n"
"s_add_u32 s80, s80, 128\n"
"s_and_b32 s80, s80, 1023\n"
"buffer_load_dwordx4 v4, s[52:55], s81 offen lds\n"
"buffer_load_dwordx4 v5, s[52:55], s81 offen offset:1024 lds\n"
"s_sub_u32 m0, s76, 44\n"
"s_nop 0\n"
"buffer_load_dword off, s[72:75], 0 offset:44 lds\n"
"s_waitcnt vmcnt(12)\n"
"s_barrier\n"
"s_add_u32 m0, s77, 24576\n"
"s_add_u32 s81, s81, 262144\n"
"buffer_load_dwordx4 v4, s[52:55], s81 offen lds\n"
"buffer_load_dwordx4 v5, s[52:55], s81 offen offset:1024 lds\n"
"s_sub_u32 m0, s76, 48\n"
"s_nop 0\n"
"buffer_load_dword off, s[72:75], 0 offset:48 lds\n"
"s_waitcnt vmcnt(12)\n"
"s_add_u32 m0, s82, 24576\n"
"s_lshl_b32 s81, s80, 12\n"
"buffer_load_dwordx4 v6, s[56:59], s80 offen lds\n"
"buffer_load_dwordx4 v7, s[56:59], s80 offen offset:1024 lds\n"
"buffer_load_dwordx4 v8, s[56:59], s80 offen offset:2048 lds\n"
"buffer_load_dwordx4 v9, s[56:59], s80 offen offset:3072 lds\n"
"s_add_u32 m0, s82, 28672\n"
"s_nop 0\n"
"buffer_load_dwordx4 v10, s[56:59], s80 offen lds\n"
"buffer_load_dwordx4 v11, s[56:59], s80 offen offset:1024 lds\n"
"s_add_u32 m0, s77, 32768\n"
"s_add_u32 s80, s80, 128\n"
"s_and_b32 s80, s80, 1023\n"
"buffer_load_dwordx4 v4, s[52:55], s81 offen lds\n"
"buffer_load_dwordx4 v5, s[52:55], s81 offen offset:1024 lds\n"
"s_sub_u32 m0, s76, 52\n"
"s_nop 0\n"
"buffer_load_dword off, s[72:75], 0 offset:52 lds\n"
"s_xor_b32 s87, s35, s40\n"
"s_bitcmp1_b32 s87, 0\n"
"s_cbranch_scc1 .LkLHS112\n"
"s_mov_b32 s78, 8192\n"
"s_add_u32 m0, s77, 8192\n"
"s_nop 0\n"
"buffer_load_dwordx4 v0, s[44:47], s78 offen nt lds\n"
"buffer_load_dwordx4 v1, s[44:47], s78 offen offset:1024 nt lds\n"
"s_add_u32 m0, s77, 65536\n"
"s_nop 0\n"
"buffer_load_dwordx4 v2, s[48:51], s78 offen nt lds\n"
"buffer_load_dwordx4 v3, s[48:51], s78 offen offset:1024 nt lds\n"
".LkLHS112:\n"
"s_waitcnt vmcnt(12)\n"
"s_barrier\n"
"s_add_u32 m0, s77, 40960\n"
"s_add_u32 s81, s81, 262144\n"
"buffer_load_dwordx4 v4, s[52:55], s81 offen lds\n"
"buffer_load_dwordx4 v5, s[52:55], s81 offen offset:1024 lds\n"
"s_sub_u32 m0, s76, 56\n"
"s_nop 0\n"
"buffer_load_dword off, s[72:75], 0 offset:56 lds\n"
"s_waitcnt vmcnt(12)\n"
"s_add_u32 m0, s82, 49152\n"
"s_lshl_b32 s81, s80, 12\n"
"buffer_load_dwordx4 v6, s[56:59], s80 offen lds\n"
"buffer_load_dwordx4 v7, s[56:59], s80 offen offset:1024 lds\n"
"buffer_load_dwordx4 v8, s[56:59], s80 offen offset:2048 lds\n"
"buffer_load_dwordx4 v9, s[56:59], s80 offen offset:3072 lds\n"
"s_add_u32 m0, s82, 53248\n"
"s_nop 0\n"
"buffer_load_dwordx4 v10, s[56:59], s80 offen lds\n"
"buffer_load_dwordx4 v11, s[56:59], s80 offen offset:1024 lds\n"
"s_add_u32 m0, s77, 49152\n"
"s_add_u32 s80, s80, 128\n"
"s_and_b32 s80, s80, 1023\n"
"buffer_load_dwordx4 v4, s[52:55], s81 offen lds\n"
"buffer_load_dwordx4 v5, s[52:55], s81 offen offset:1024 lds\n"
"s_sub_u32 m0, s76, 60\n"
"s_nop 0\n"
"buffer_load_dword off, s[72:75], 0 offset:60 lds\n"
"s_waitcnt vmcnt(12)\n"
"s_barrier\n"
"s_add_u32 m0, s77, 57344\n"
"s_add_u32 s81, s81, 262144\n"
"buffer_load_dwordx4 v4, s[52:55], s81 offen lds\n"
"buffer_load_dwordx4 v5, s[52:55], s81 offen offset:1024 lds\n"
"s_xor_b32 s87, s35, s40\n"
"s_bitcmp1_b32 s87, 0\n"
"s_cbranch_scc1 .LkLHB1\n"
"s_sub_u32 m0, s76, 72\n"
"s_nop 0\n"
"buffer_load_dword off, s[72:75], 0 offset:72 lds\n"
"s_mov_b32 s78, 16384\n"
"s_add_u32 m0, s77, 16384\n"
"s_nop 0\n"
"buffer_load_dwordx4 v0, s[44:47], s78 offen nt lds\n"
"buffer_load_dwordx4 v1, s[44:47], s78 offen offset:1024 nt lds\n"
"s_add_u32 m0, s77, 73728\n"
"s_nop 0\n"
"buffer_load_dwordx4 v2, s[48:51], s78 offen nt lds\n"
"buffer_load_dwordx4 v3, s[48:51], s78 offen offset:1024 nt lds\n"
".LkLHB1:\n"
"s_sub_u32 m0, s76, 76\n"
"s_nop 0\n"
"buffer_load_dword off, s[72:75], 0 offset:76 lds\n"
"s_memrealtime s[64:65]\n"
"s_waitcnt lgkmcnt(0)\n"
"s_mov_b32 s78, 24576\n"
"s_add_u32 m0, s77, 24576\n"
"s_nop 0\n"
"buffer_load_dwordx4 v0, s[44:47], s78 offen nt lds\n"
"buffer_load_dwordx4 v1, s[44:47], s78 offen offset:1024 nt lds\n"
"s_add_u32 m0, s77, 81920\n"
"s_nop 0\n"
"buffer_load_dwordx4 v2, s[48:51], s78 offen nt lds\n"
"buffer_load_dwordx4 v3, s[48:51], s78 offen offset:1024 nt lds\n"
"s_sub_u32 m0, s76, 80\n"
"s_nop 0\n"
"buffer_load_dword off, s[72:75], 0 offset:80 lds\n"
"v_mov_b32 v4, 163824\n"
"ds_read_b32 v8, v4 offset:8\n"
"ds_read_b64 v[4:5], v4\n"
"s_add_u32 s68, s32, 0x1000000\n"
"s_addc_u32 s69, s33, 0\n"
"s_mov_b32 s70, 0x200000\n"
"s_mov_b32 s71, 0x20000\n"
"s_lshl_b32 s60, s38, 13\n"
"s_add_u32 s60, s60, s41\n"
"s_lshl_b32 s60, s60, 7\n"
"s_add_u32 s60, s28, s60\n"
"s_addc_u32 s61, s29, 0\n"
"s_mov_b32 s62, 0xffc80\n"
"s_mov_b32 s63, 0x20000\n"
"s_lshl_b32 s29, s38, 17\n"
"s_add_u32 s29, s29, 0xd00000\n"
"s_add_u32 s29, s32, s29\n"
"s_addc_u32 s28, s33, 0\n"
"s_cmp_eq_u32 s41, 3\n"
"s_cselect_b32 s60, s29, s60\n"
"s_cselect_b32 s61, s28, s61\n"
"s_cselect_b32 s62, 0x20000, s62\n"
"s_mul_i32 s26, s41, 0\n"
"s_and_b32 s29, s43, 1\n"
"s_mul_i32 s29, s29, 8192\n"
"s_add_u32 s26, s26, s29\n"
"s_bfe_u32 s29, s43, 0x10001\n"
"s_mul_i32 s29, s29, 8192\n"
"s_add_u32 s26, s26, s29\n"
"s_bfe_u32 s29, s43, 0x10002\n"
"s_mul_i32 s29, s29, 16384\n"
"s_add_u32 s26, s26, s29\n"
"s_and_b32 s27, s40, 0\n"
"s_lshl_b32 s27, s27, 13\n"
"s_and_b32 s28, s40, 0\n"
"s_lshl_b32 s28, s28, 10\n"
"s_lshl2_add_u32 s52, s34, s40\n"
"s_lshl2_add_u32 s52, s52, s38\n"
"s_mul_i32 s52, s52, 98304\n"
"s_add_u32 s52, s32, s52\n"
"s_addc_u32 s53, s33, 0\n"
"s_mov_b32 s54, 0x18000\n"
"s_mov_b32 s55, 0x20000\n"
"s_cmp_eq_u32 s41, 3\n"
"s_cselect_b32 s83, 4, 32\n"
"s_cselect_b32 s29, 0, 7168\n"
"s_lshr_b32 s84, s38, 1\n"
"s_mul_i32 s84, s84, 57344\n"
"s_lshl_b32 s85, s38, 12\n"
"s_add_u32 s84, s84, s85\n"
"s_add_u32 s84, s84, 2048\n"
"s_mul_i32 s85, s38, 6144\n"
"s_add_u32 s85, s85, 133120\n"
"v_and_b32 v48, 1, v255\n"
"v_and_b32 v49, 3, v255\n"
"v_lshlrev_b32 v50, 1, v49\n"
"v_bfe_u32 v51, v255, 3, 2\n"
"v_xor_b32 v52, v50, v51\n"
"v_bfe_u32 v53, v255, 2, 2\n"
"v_lshrrev_b32 v54, 4, v255\n"
"v_lshlrev_b32 v55, 9, v54\n"
"v_lshl_add_u32 v56, v53, 7, v55\n"
"v_lshl_add_u32 v57, v52, 4, v56\n"
"v_lshl_add_u32 v28, v48, 3, v57\n"
"v_lshrrev_b32 v58, 3, v255\n"
"v_lshlrev_b32 v21, 5, v58\n"
"v_and_b32 v59, 7, v255\n"
"v_xor_b32 v60, v59, v54\n"
"v_lshlrev_b32 v22, 4, v60\n"
"v_mad_u32_u24 v20, v21, s83, v22\n"
"v_add_u32 v21, s29, v20\n"
"v_add_u32 v22, s29, v21\n"
"v_add_u32 v23, s29, v22\n"
"v_xor_b32 v29, 8, v28\n"
"v_xor_b32 v30, 16, v28\n"
"v_xor_b32 v31, 24, v28\n"
"v_add_u32 v24, s84, v28\n"
"v_add_u32 v25, s84, v29\n"
"v_add_u32 v26, s84, v30\n"
"v_add_u32 v27, s84, v31\n"
"v_add_u32 v28, s85, v28\n"
"v_add_u32 v29, s85, v29\n"
"v_add_u32 v30, s85, v30\n"
"v_add_u32 v31, s85, v31\n"
"v_lshlrev_b32 v76, 4, v255\n"
"v_mov_b32 v77, v24\n"
"v_lshlrev_b32 v48, 3, v255\n"
"v_lshl_add_u32 v49, s38, 9, v48\n"
"v_add_u32 v6, 0x20000, v49\n"
"v_mov_b32 v51, s40\n"
"v_lshl_add_u32 v50, s34, 2, v51\n"
"v_lshl_add_u32 v52, v50, 2, s38\n"
"v_and_b32 v53, 7, v255\n"
"v_lshl_add_u32 v54, v52, 3, v53\n"
"v_lshlrev_b32 v7, 7, v54\n"
"v_lshlrev_b32_e64 v78, 8, s38\n"
"s_waitcnt lgkmcnt(0)\n"
"v_readfirstlane_b32 s20, v4\n"
"v_readfirstlane_b32 s21, v5\n"
"v_readfirstlane_b32 s79, v8\n"
"s_sub_u32 s79, s79, s64\n"
"s_add_u32 s79, s79, 2515\n"
"s_max_i32 s79, s79, 1682\n"
"s_min_i32 s79, s79, 2088\n"
"s_mul_i32 s79, s79, 2260\n"
"s_lshr_b32 s79, s79, 8\n"
"s_lshl_b32 s64, s64, 8\n"
"s_mov_b32 s65, s64\n"
"ds_read_b32 v14, v13\n"
"s_add_u32 s64, s64, s79\n"
"s_add_u32 s64, s64, 0xfffff723\n"
"s_sub_u32 s65, s65, s64\n"
"s_max_i32 s65, s65, 0\n"
"s_mov_b32 s86, 0x1000\n"
".LkLPC04:\n"
"s_memrealtime s[66:67]\n"
"s_waitcnt lgkmcnt(0)\n"
"s_lshl_b32 s66, s66, 8\n"
"s_sub_u32 s66, s66, s64\n"
"s_cmp_ge_i32 s66, s65\n"
"s_cbranch_scc1 .LkLPC14\n"
"s_sub_u32 s86, s86, 1\n"
"s_cbranch_scc0 .LkLPC04\n"
".LkLPC14:\n"
"s_add_u32 s65, s66, s64\n"
"s_add_u32 s65, s65, 13824\n"
"s_waitcnt lgkmcnt(0)\n"
"v_cmp_gt_i32 vcc, 0xb000000e, v14\n"
"s_cbranch_vccz .LkLPD0\n"
"s_mov_b32 s86, 0x10000\n"
".LkLP0:\n"
"ds_read_b32 v14, v13\n"
"s_waitcnt lgkmcnt(0)\n"
"v_cmp_gt_i32 vcc, 0xb000000e, v14\n"
"s_cbranch_vccz .LkLPD0\n"
"s_sleep 1\n"
"s_sub_u32 s86, s86, 1\n"
"s_cbranch_scc0 .LkLP0\n"
".LkLPD0:\n"
"s_mov_b32 s78, 32768\n"
"s_add_u32 m0, s77, 32768\n"
"s_nop 0\n"
"buffer_load_dwordx4 v0, s[44:47], s78 offen nt lds\n"
"buffer_load_dwordx4 v1, s[44:47], s78 offen offset:1024 nt lds\n"
"s_add_u32 m0, s77, 90112\n"
"s_nop 0\n"
"buffer_load_dwordx4 v2, s[48:51], s78 offen nt lds\n"
"buffer_load_dwordx4 v3, s[48:51], s78 offen offset:1024 nt lds\n"
"s_sub_u32 m0, s76, 84\n"
"s_nop 0\n"
"buffer_load_dword off, s[72:75], 0 offset:84 lds\n"
"ds_read_b32 v14, v13\n"
"s_waitcnt vmcnt(10)\n"
"s_add_u32 s64, s64, s79\n"
"s_add_u32 s64, s64, 0xfffffa23\n"
"s_sub_u32 s65, s65, s64\n"
"s_max_i32 s65, s65, 0\n"
"s_mov_b32 s86, 0x1000\n"
".LkLPC05:\n"
"s_memrealtime s[66:67]\n"
"s_waitcnt lgkmcnt(0)\n"
"s_lshl_b32 s66, s66, 8\n"
"s_sub_u32 s66, s66, s64\n"
"s_cmp_ge_i32 s66, s65\n"
"s_cbranch_scc1 .LkLPC15\n"
"s_sub_u32 s86, s86, 1\n"
"s_cbranch_scc0 .LkLPC05\n"
".LkLPC15:\n"
"s_add_u32 s65, s66, s64\n"
"s_add_u32 s65, s65, 13824\n"
"s_waitcnt lgkmcnt(0)\n"
"v_cmp_gt_i32 vcc, 0xb000000f, v14\n"
"s_cbranch_vccz .LkLPD1\n"
"s_mov_b32 s86, 0x10000\n"
".LkLP1:\n"
"ds_read_b32 v14, v13\n"
"s_waitcnt lgkmcnt(0)\n"
"v_cmp_gt_i32 vcc, 0xb000000f, v14\n"
"s_cbranch_vccz .LkLPD1\n"
"s_sleep 1\n"
"s_sub_u32 s86, s86, 1\n"
"s_cbranch_scc0 .LkLP1\n"
".LkLPD1:\n"
"s_mov_b32 s78, 40960\n"
"s_add_u32 m0, s77, 40960\n"
"s_nop 0\n"
"buffer_load_dwordx4 v0, s[44:47], s78 offen nt lds\n"
"buffer_load_dwordx4 v1, s[44:47], s78 offen offset:1024 nt lds\n"
"s_add_u32 m0, s77, 98304\n"
"s_nop 0\n"
"buffer_load_dwordx4 v2, s[48:51], s78 offen nt lds\n"
"buffer_load_dwordx4 v3, s[48:51], s78 offen offset:1024 nt lds\n"
"s_sub_u32 m0, s76, 88\n"
"s_nop 0\n"
"buffer_load_dword off, s[72:75], 0 offset:88 lds\n"
"v_mov_b32 v4, s20\n"
"v_mov_b32 v5, s21\n"
"s_cmp_lg_u32 s41, 3\n"
"s_cbranch_scc1 .LkLW0\n"
"buffer_load_dwordx2 v[4:5], v6, s[68:71], 0 offen sc1\n"
".LkLW0:\n"
"ds_read_b32 v14, v13\n"
"s_waitcnt vmcnt(10)\n"
"s_add_u32 s64, s64, s79\n"
"s_add_u32 s64, s64, 0xfffffe23\n"
"s_sub_u32 s65, s65, s64\n"
"s_max_i32 s65, s65, 0\n"
"s_mov_b32 s86, 0x1000\n"
".LkLPC06:\n"
"s_memrealtime s[66:67]\n"
"s_waitcnt lgkmcnt(0)\n"
"s_lshl_b32 s66, s66, 8\n"
"s_sub_u32 s66, s66, s64\n"
"s_cmp_ge_i32 s66, s65\n"
"s_cbranch_scc1 .LkLPC16\n"
"s_sub_u32 s86, s86, 1\n"
"s_cbranch_scc0 .LkLPC06\n"
".LkLPC16:\n"
"s_add_u32 s65, s66, s64\n"
"s_add_u32 s65, s65, 13824\n"
"s_waitcnt lgkmcnt(0)\n"
"v_cmp_gt_i32 vcc, 0xb0000010, v14\n"
"s_cbranch_vccz .LkLPD2\n"
"s_mov_b32 s86, 0x10000\n"
".LkLP2:\n"
"ds_read_b32 v14, v13\n"
"s_waitcnt lgkmcnt(0)\n"
"v_cmp_gt_i32 vcc, 0xb0000010, v14\n"
"s_cbranch_vccz .LkLPD2\n"
"s_sleep 1\n"
"s_sub_u32 s86, s86, 1\n"
"s_cbranch_scc0 .LkLP2\n"
".LkLPD2:\n"
"s_mov_b32 s78, 49152\n"
"s_add_u32 m0, s77, 49152\n"
"s_nop 0\n"
"buffer_load_dwordx4 v0, s[44:47], s78 offen nt lds\n"
"buffer_load_dwordx4 v1, s[44:47], s78 offen offset:1024 nt lds\n"
"s_add_u32 m0, s77, 106496\n"
"s_nop 0\n"
"buffer_load_dwordx4 v2, s[48:51], s78 offen nt lds\n"
"buffer_load_dwordx4 v3, s[48:51], s78 offen offset:1024 nt lds\n"
"s_sub_u32 m0, s76, 92\n"
"s_nop 0\n"
"buffer_load_dword off, s[72:75], 0 offset:92 lds\n"
"ds_read_b32 v14, v13\n"
"s_waitcnt vmcnt(10)\n"
"s_add_u32 s64, s64, s79\n"
"s_add_u32 s64, s64, 0x123\n"
"s_sub_u32 s65, s65, s64\n"
"s_max_i32 s65, s65, 0\n"
"s_mov_b32 s86, 0x1000\n"
".LkLPC07:\n"
"s_memrealtime s[66:67]\n"
"s_waitcnt lgkmcnt(0)\n"
"s_lshl_b32 s66, s66, 8\n"
"s_sub_u32 s66, s66, s64\n"
"s_cmp_ge_i32 s66, s65\n"
"s_cbranch_scc1 .LkLPC17\n"
"s_sub_u32 s86, s86, 1\n"
"s_cbranch_scc0 .LkLPC07\n"
".LkLPC17:\n"
"s_add_u32 s65, s66, s64\n"
"s_add_u32 s65, s65, 13824\n"
"s_waitcnt lgkmcnt(0)\n"
"v_cmp_gt_i32 vcc, 0xb0000011, v14\n"
"s_cbranch_vccz .LkLPD3\n"
"s_mov_b32 s86, 0x10000\n"
".LkLP3:\n"
"ds_read_b32 v14, v13\n"
"s_waitcnt lgkmcnt(0)\n"
"v_cmp_gt_i32 vcc, 0xb0000011, v14\n"
"s_cbranch_vccz .LkLPD3\n"
"s_sleep 1\n"
"s_sub_u32 s86, s86, 1\n"
"s_cbranch_scc0 .LkLP3\n"
".LkLPD3:\n"
"s_mov_b32 s78, 57344\n"
"s_add_u32 m0, s77, 57344\n"
"s_nop 0\n"
"buffer_load_dwordx4 v0, s[44:47], s78 offen nt lds\n"
"buffer_load_dwordx4 v1, s[44:47], s78 offen offset:1024 nt lds\n"
"s_add_u32 m0, s77, 114688\n"
"s_nop 0\n"
"buffer_load_dwordx4 v2, s[48:51], s78 offen nt lds\n"
"buffer_load_dwordx4 v3, s[48:51], s78 offen offset:1024 nt lds\n"
"s_sub_u32 m0, s76, 96\n"
"s_nop 0\n"
"buffer_load_dword off, s[72:75], 0 offset:96 lds\n"
"s_waitcnt vmcnt(10)\n"
"s_mov_b32 s86, 0x8000\n"
".LkLWP:\n"
"v_cmp_ne_u64 vcc, s[20:21], v[4:5]\n"
"s_cbranch_vccz .LkLWD\n"
"buffer_load_dwordx2 v[4:5], v6, s[68:71], 0 offen sc1\n"
"s_waitcnt vmcnt(0)\n"
"s_sub_u32 s86, s86, 1\n"
"s_cbranch_scc0 .LkLWP\n"
".LkLWD:\n"
"s_add_u32 m0, s85, 0\n"
"s_add_u32 s29, s26, 24576\n"
"s_and_b32 s29, s29, 0x7e00\n"
"s_mul_i32 s29, s29, s83\n"
"buffer_load_dwordx4 v20, s[60:63], s29 offen sc1 lds\n"
"buffer_load_dwordx4 v21, s[60:63], s29 offen offset:1024 sc1 lds\n"
"ds_read_b32 v14, v13\n"
"s_waitcnt vmcnt(12)\n"
"s_add_u32 s64, s64, s79\n"
"s_add_u32 s64, s64, 0x423\n"
"s_sub_u32 s65, s65, s64\n"
"s_max_i32 s65, s65, 0\n"
"s_mov_b32 s86, 0x1000\n"
".LkLPC08:\n"
"s_memrealtime s[66:67]\n"
"s_waitcnt lgkmcnt(0)\n"
"s_lshl_b32 s66, s66, 8\n"
"s_sub_u32 s66, s66, s64\n"
"s_cmp_ge_i32 s66, s65\n"
"s_cbranch_scc1 .LkLPC18\n"
"s_sub_u32 s86, s86, 1\n"
"s_cbranch_scc0 .LkLPC08\n"
".LkLPC18:\n"
"s_add_u32 s65, s66, s64\n"
"s_add_u32 s65, s65, 13824\n"
"s_waitcnt lgkmcnt(0)\n"
"v_cmp_gt_i32 vcc, 0xb0000012, v14\n"
"s_cbranch_vccz .LkLPD4\n"
"s_mov_b32 s86, 0x10000\n"
".LkLP4:\n"
"ds_read_b32 v14, v13\n"
"s_waitcnt lgkmcnt(0)\n"
"v_cmp_gt_i32 vcc, 0xb0000012, v14\n"
"s_cbranch_vccz .LkLPD4\n"
"s_sleep 1\n"
"s_sub_u32 s86, s86, 1\n"
"s_cbranch_scc0 .LkLP4\n"
".LkLPD4:\n"
"s_mov_b32 s78, 65536\n"
"s_add_u32 m0, s77, 0\n"
"s_nop 0\n"
"buffer_load_dwordx4 v0, s[44:47], s78 offen nt lds\n"
"buffer_load_dwordx4 v1, s[44:47], s78 offen offset:1024 nt lds\n"
"s_add_u32 m0, s77, 122880\n"
"s_nop 0\n"
"buffer_load_dwordx4 v2, s[48:51], s78 offen nt lds\n"
"buffer_load_dwordx4 v3, s[48:51], s78 offen offset:1024 nt lds\n"
"s_sub_u32 m0, s76, 100\n"
"s_nop 0\n"
"buffer_load_dword off, s[72:75], 0 offset:100 lds\n"
"s_add_u32 m0, s85, 2048\n"
"s_add_u32 s29, s26, 25088\n"
"s_and_b32 s29, s29, 0x7e00\n"
"s_mul_i32 s29, s29, s83\n"
"buffer_load_dwordx4 v20, s[60:63], s29 offen sc1 lds\n"
"buffer_load_dwordx4 v21, s[60:63], s29 offen offset:1024 sc1 lds\n"
"ds_read_b32 v14, v13\n"
"s_waitcnt vmcnt(14)\n"
"s_add_u32 s64, s64, s79\n"
"s_add_u32 s64, s64, 0x823\n"
"s_sub_u32 s65, s65, s64\n"
"s_max_i32 s65, s65, 0\n"
"s_mov_b32 s86, 0x1000\n"
".LkLPC09:\n"
"s_memrealtime s[66:67]\n"
"s_waitcnt lgkmcnt(0)\n"
"s_lshl_b32 s66, s66, 8\n"
"s_sub_u32 s66, s66, s64\n"
"s_cmp_ge_i32 s66, s65\n"
"s_cbranch_scc1 .LkLPC19\n"
"s_sub_u32 s86, s86, 1\n"
"s_cbranch_scc0 .LkLPC09\n"
".LkLPC19:\n"
"s_add_u32 s65, s66, s64\n"
"s_add_u32 s65, s65, 13824\n"
"s_waitcnt lgkmcnt(0)\n"
"v_cmp_gt_i32 vcc, 0xb0000013, v14\n"
"s_cbranch_vccz .LkLPD5\n"
"s_mov_b32 s86, 0x10000\n"
".LkLP5:\n"
"ds_read_b32 v14, v13\n"
"s_waitcnt lgkmcnt(0)\n"
"v_cmp_gt_i32 vcc, 0xb0000013, v14\n"
"s_cbranch_vccz .LkLPD5\n"
"s_sleep 1\n"
"s_sub_u32 s86, s86, 1\n"
"s_cbranch_scc0 .LkLP5\n"
".LkLPD5:\n"
"s_mov_b32 s78, 73728\n"
"s_add_u32 m0, s77, 8192\n"
"s_nop 0\n"
"buffer_load_dwordx4 v0, s[44:47], s78 offen nt lds\n"
"buffer_load_dwordx4 v1, s[44:47], s78 offen offset:1024 nt lds\n"
"s_add_u32 m0, s77, 65536\n"
"s_nop 0\n"
"buffer_load_dwordx4 v2, s[48:51], s78 offen nt lds\n"
"buffer_load_dwordx4 v3, s[48:51], s78 offen offset:1024 nt lds\n"
"s_sub_u32 m0, s76, 104\n"
"s_nop 0\n"
"buffer_load_dword off, s[72:75], 0 offset:104 lds\n"
"s_add_u32 m0, s85, 4096\n"
"s_add_u32 s29, s26, 25600\n"
"s_and_b32 s29, s29, 0x7e00\n"
"s_mul_i32 s29, s29, s83\n"
"buffer_load_dwordx4 v20, s[60:63], s29 offen sc1 lds\n"
"buffer_load_dwordx4 v21, s[60:63], s29 offen offset:1024 sc1 lds\n"
"ds_read_b32 v14, v13\n"
"s_waitcnt vmcnt(14)\n"
"ds_read_b64_tr_b16 v[32:33], v28 offset:0\n"
"ds_read_b64_tr_b16 v[36:37], v29 offset:0\n"
"ds_read_b64_tr_b16 v[40:41], v30 offset:0\n"
"ds_read_b64_tr_b16 v[44:45], v31 offset:0\n"
"s_add_u32 s64, s64, s79\n"
"s_add_u32 s64, s64, 0xb23\n"
"s_sub_u32 s65, s65, s64\n"
"s_max_i32 s65, s65, 0\n"
"s_mov_b32 s86, 0x1000\n"
".LkLPC010:\n"
"s_memrealtime s[66:67]\n"
"s_waitcnt lgkmcnt(0)\n"
"s_lshl_b32 s66, s66, 8\n"
"s_sub_u32 s66, s66, s64\n"
"s_cmp_ge_i32 s66, s65\n"
"s_cbranch_scc1 .LkLPC110\n"
"s_sub_u32 s86, s86, 1\n"
"s_cbranch_scc0 .LkLPC010\n"
".LkLPC110:\n"
"s_add_u32 s65, s66, s64\n"
"s_add_u32 s65, s65, 13824\n"
"s_waitcnt lgkmcnt(0)\n"
"v_cmp_gt_i32 vcc, 0xb0000014, v14\n"
"s_cbranch_vccz .LkLPD6\n"
"s_mov_b32 s86, 0x10000\n"
".LkLP6:\n"
"ds_read_b32 v14, v13\n"
"s_waitcnt lgkmcnt(0)\n"
"v_cmp_gt_i32 vcc, 0xb0000014, v14\n"
"s_cbranch_vccz .LkLPD6\n"
"s_sleep 1\n"
"s_sub_u32 s86, s86, 1\n"
"s_cbranch_scc0 .LkLP6\n"
".LkLPD6:\n"
"s_mov_b32 s78, 81920\n"
"s_add_u32 m0, s77, 16384\n"
"s_nop 0\n"
"buffer_load_dwordx4 v0, s[44:47], s78 offen nt lds\n"
"buffer_load_dwordx4 v1, s[44:47], s78 offen offset:1024 nt lds\n"
"s_add_u32 m0, s77, 73728\n"
"s_nop 0\n"
"buffer_load_dwordx4 v2, s[48:51], s78 offen nt lds\n"
"buffer_load_dwordx4 v3, s[48:51], s78 offen offset:1024 nt lds\n"
"s_sub_u32 m0, s76, 108\n"
"s_nop 0\n"
"buffer_load_dword off, s[72:75], 0 offset:108 lds\n"
"s_add_u32 m0, s85, 0\n"
"s_add_u32 s29, s26, 26112\n"
"s_and_b32 s29, s29, 0x7e00\n"
"s_mul_i32 s29, s29, s83\n"
"buffer_load_dwordx4 v20, s[60:63], s29 offen sc1 lds\n"
"buffer_load_dwordx4 v21, s[60:63], s29 offen offset:1024 sc1 lds\n"
"ds_read_b32 v14, v13\n"
"s_waitcnt vmcnt(14)\n"
"ds_read_b64_tr_b16 v[34:35], v28 offset:2048\n"
"ds_read_b64_tr_b16 v[38:39], v29 offset:2048\n"
"ds_read_b64_tr_b16 v[42:43], v30 offset:2048\n"
"ds_read_b64_tr_b16 v[46:47], v31 offset:2048\n"
"s_add_u32 s64, s64, s79\n"
"s_add_u32 s64, s64, 0xfffffd23\n"
"s_sub_u32 s65, s65, s64\n"
"s_max_i32 s65, s65, 0\n"
"s_mov_b32 s86, 0x1000\n"
".LkLPC011:\n"
"s_memrealtime s[66:67]\n"
"s_waitcnt lgkmcnt(0)\n"
"s_lshl_b32 s66, s66, 8\n"
"s_sub_u32 s66, s66, s64\n"
"s_cmp_ge_i32 s66, s65\n"
"s_cbranch_scc1 .LkLPC111\n"
"s_sub_u32 s86, s86, 1\n"
"s_cbranch_scc0 .LkLPC011\n"
".LkLPC111:\n"
"s_add_u32 s65, s66, s64\n"
"s_add_u32 s65, s65, 13824\n"
"s_waitcnt lgkmcnt(0)\n"
"v_cmp_gt_i32 vcc, 0xb0000015, v14\n"
"s_cbranch_vccz .LkLPD7\n"
"s_mov_b32 s86, 0x10000\n"
".LkLP7:\n"
"ds_read_b32 v14, v13\n"
"s_waitcnt lgkmcnt(0)\n"
"v_cmp_gt_i32 vcc, 0xb0000015, v14\n"
"s_cbranch_vccz .LkLPD7\n"
"s_sleep 1\n"
"s_sub_u32 s86, s86, 1\n"
"s_cbranch_scc0 .LkLP7\n"
".LkLPD7:\n"
"s_mov_b32 s78, 90112\n"
"s_add_u32 m0, s77, 24576\n"
"s_nop 0\n"
"buffer_load_dwordx4 v0, s[44:47], s78 offen nt lds\n"
"buffer_load_dwordx4 v1, s[44:47], s78 offen offset:1024 nt lds\n"
"s_add_u32 m0, s77, 81920\n"
"s_nop 0\n"
"buffer_load_dwordx4 v2, s[48:51], s78 offen nt lds\n"
"buffer_load_dwordx4 v3, s[48:51], s78 offen offset:1024 nt lds\n"
"s_sub_u32 m0, s76, 112\n"
"s_nop 0\n"
"buffer_load_dword off, s[72:75], 0 offset:112 lds\n"
"s_add_u32 m0, s85, 2048\n"
"s_add_u32 s29, s26, 26624\n"
"s_and_b32 s29, s29, 0x7e00\n"
"s_mul_i32 s29, s29, s83\n"
"buffer_load_dwordx4 v20, s[60:63], s29 offen sc1 lds\n"
"buffer_load_dwordx4 v21, s[60:63], s29 offen offset:1024 sc1 lds\n"
"ds_read_b32 v14, v13\n"
"s_waitcnt vmcnt(14)\n"
"ds_read_b64_tr_b16 v[80:81], v28 offset:4096\n"
"ds_read_b64_tr_b16 v[84:85], v29 offset:4096\n"
"ds_read_b64_tr_b16 v[88:89], v30 offset:4096\n"
"ds_read_b64_tr_b16 v[92:93], v31 offset:4096\n"
"s_add_u32 s64, s64, s79\n"
"s_add_u32 s64, s64, 0xfffffd23\n"
"s_sub_u32 s65, s65, s64\n"
"s_max_i32 s65, s65, 0\n"
"s_mov_b32 s86, 0x1000\n"
".LkLPC012:\n"
"s_memrealtime s[66:67]\n"
"s_waitcnt lgkmcnt(0)\n"
"s_lshl_b32 s66, s66, 8\n"
"s_sub_u32 s66, s66, s64\n"
"s_cmp_ge_i32 s66, s65\n"
"s_cbranch_scc1 .LkLPC112\n"
"s_sub_u32 s86, s86, 1\n"
"s_cbranch_scc0 .LkLPC012\n"
".LkLPC112:\n"
"s_add_u32 s65, s66, s64\n"
"s_add_u32 s65, s65, 13824\n"
"s_waitcnt lgkmcnt(0)\n"
"v_cmp_gt_i32 vcc, 0xb0000016, v14\n"
"s_cbranch_vccz .LkLPD8\n"
"s_mov_b32 s86, 0x10000\n"
".LkLP8:\n"
"ds_read_b32 v14, v13\n"
"s_waitcnt lgkmcnt(0)\n"
"v_cmp_gt_i32 vcc, 0xb0000016, v14\n"
"s_cbranch_vccz .LkLPD8\n"
"s_sleep 1\n"
"s_sub_u32 s86, s86, 1\n"
"s_cbranch_scc0 .LkLP8\n"
".LkLPD8:\n"
"s_mov_b32 s78, 98304\n"
"s_add_u32 m0, s77, 32768\n"
"s_nop 0\n"
"buffer_load_dwordx4 v0, s[44:47], s78 offen nt lds\n"
"buffer_load_dwordx4 v1, s[44:47], s78 offen offset:1024 nt lds\n"
"s_add_u32 m0, s77, 90112\n"
"s_nop 0\n"
"buffer_load_dwordx4 v2, s[48:51], s78 offen nt lds\n"
"buffer_load_dwordx4 v3, s[48:51], s78 offen offset:1024 nt lds\n"
"s_sub_u32 m0, s76, 116\n"
"s_nop 0\n"
"buffer_load_dword off, s[72:75], 0 offset:116 lds\n"
"s_add_u32 m0, s85, 4096\n"
"s_add_u32 s29, s26, 27136\n"
"s_and_b32 s29, s29, 0x7e00\n"
"s_mul_i32 s29, s29, s83\n"
"buffer_load_dwordx4 v20, s[60:63], s29 offen sc1 lds\n"
"buffer_load_dwordx4 v21, s[60:63], s29 offen offset:1024 sc1 lds\n"
"ds_read_b32 v14, v13\n"
"s_waitcnt vmcnt(14)\n"
"ds_read_b64_tr_b16 v[82:83], v28 offset:0\n"
"ds_read_b64_tr_b16 v[86:87], v29 offset:0\n"
"ds_read_b64_tr_b16 v[90:91], v30 offset:0\n"
"ds_read_b64_tr_b16 v[94:95], v31 offset:0\n"
"s_add_u32 s64, s64, s79\n"
"s_add_u32 s64, s64, 0xfffffd23\n"
"s_sub_u32 s65, s65, s64\n"
"s_max_i32 s65, s65, 0\n"
"s_mov_b32 s86, 0x1000\n"
".LkLPC013:\n"
"s_memrealtime s[66:67]\n"
"s_waitcnt lgkmcnt(0)\n"
"s_lshl_b32 s66, s66, 8\n"
"s_sub_u32 s66, s66, s64\n"
"s_cmp_ge_i32 s66, s65\n"
"s_cbranch_scc1 .LkLPC113\n"
"s_sub_u32 s86, s86, 1\n"
"s_cbranch_scc0 .LkLPC013\n"
".LkLPC113:\n"
"s_add_u32 s65, s66, s64\n"
"s_add_u32 s65, s65, 13824\n"
"s_waitcnt lgkmcnt(0)\n"
"v_cmp_gt_i32 vcc, 0xb0000017, v14\n"
"s_cbranch_vccz .LkLPD9\n"
"s_mov_b32 s86, 0x10000\n"
".LkLP9:\n"
"ds_read_b32 v14, v13\n"
"s_waitcnt lgkmcnt(0)\n"
"v_cmp_gt_i32 vcc, 0xb0000017, v14\n"
"s_cbranch_vccz .LkLPD9\n"
"s_sleep 1\n"
"s_sub_u32 s86, s86, 1\n"
"s_cbranch_scc0 .LkLP9\n"
".LkLPD9:\n"
"s_mov_b32 s78, 106496\n"
"s_add_u32 m0, s77, 40960\n"
"s_nop 0\n"
"buffer_load_dwordx4 v0, s[44:47], s78 offen nt lds\n"
"buffer_load_dwordx4 v1, s[44:47], s78 offen offset:1024 nt lds\n"
"s_add_u32 m0, s77, 98304\n"
"s_nop 0\n"
"buffer_load_dwordx4 v2, s[48:51], s78 offen nt lds\n"
"buffer_load_dwordx4 v3, s[48:51], s78 offen offset:1024 nt lds\n"
"s_sub_u32 m0, s76, 120\n"
"s_nop 0\n"
"buffer_load_dword off, s[72:75], 0 offset:120 lds\n"
"s_add_u32 m0, s85, 0\n"
"s_add_u32 s29, s26, 27648\n"
"s_and_b32 s29, s29, 0x7e00\n"
"s_mul_i32 s29, s29, s83\n"
"buffer_load_dwordx4 v20, s[60:63], s29 offen sc1 lds\n"
"buffer_load_dwordx4 v21, s[60:63], s29 offen offset:1024 sc1 lds\n"
"ds_read_b32 v14, v13\n"
"s_waitcnt vmcnt(14)\n"
"ds_read_b64_tr_b16 v[96:97], v28 offset:2048\n"
"ds_read_b64_tr_b16 v[100:101], v29 offset:2048\n"
"ds_read_b64_tr_b16 v[104:105], v30 offset:2048\n"
"ds_read_b64_tr_b16 v[108:109], v31 offset:2048\n"
"s_add_u32 s64, s64, s79\n"
"s_add_u32 s64, s64, 0xfffffe23\n"
"s_sub_u32 s65, s65, s64\n"
"s_max_i32 s65, s65, 0\n"
"s_mov_b32 s86, 0x1000\n"
".LkLPC014:\n"
"s_memrealtime s[66:67]\n"
"s_waitcnt lgkmcnt(0)\n"
"s_lshl_b32 s66, s66, 8\n"
"s_sub_u32 s66, s66, s64\n"
"s_cmp_ge_i32 s66, s65\n"
"s_cbranch_scc1 .LkLPC114\n"
"s_sub_u32 s86, s86, 1\n"
"s_cbranch_scc0 .LkLPC014\n"
".LkLPC114:\n"
"s_add_u32 s65, s66, s64\n"
"s_add_u32 s65, s65, 13824\n"
"s_waitcnt lgkmcnt(0)\n"
"v_cmp_gt_i32 vcc, 0xb0000018, v14\n"
"s_cbranch_vccz .LkLPD10\n"
"s_mov_b32 s86, 0x10000\n"
".LkLP10:\n"
"ds_read_b32 v14, v13\n"
"s_waitcnt lgkmcnt(0)\n"
"v_cmp_gt_i32 vcc, 0xb0000018, v14\n"
"s_cbranch_vccz .LkLPD10\n"
"s_sleep 1\n"
"s_sub_u32 s86, s86, 1\n"
"s_cbranch_scc0 .LkLP10\n"
".LkLPD10:\n"
"s_mov_b32 s78, 114688\n"
"s_add_u32 m0, s77, 49152\n"
"s_nop 0\n"
"buffer_load_dwordx4 v0, s[44:47], s78 offen nt lds\n"
"buffer_load_dwordx4 v1, s[44:47], s78 offen offset:1024 nt lds\n"
"s_add_u32 m0, s77, 106496\n"
"s_nop 0\n"
"buffer_load_dwordx4 v2, s[48:51], s78 offen nt lds\n"
"buffer_load_dwordx4 v3, s[48:51], s78 offen offset:1024 nt lds\n"
"s_sub_u32 m0, s76, 124\n"
"s_nop 0\n"
"buffer_load_dword off, s[72:75], 0 offset:124 lds\n"
"s_add_u32 m0, s85, 2048\n"
"s_add_u32 s29, s26, 28160\n"
"s_and_b32 s29, s29, 0x7e00\n"
"s_mul_i32 s29, s29, s83\n"
"buffer_load_dwordx4 v20, s[60:63], s29 offen sc1 lds\n"
"buffer_load_dwordx4 v21, s[60:63], s29 offen offset:1024 sc1 lds\n"
"ds_read_b32 v14, v13\n"
"s_waitcnt vmcnt(14)\n"
"ds_read_b64_tr_b16 v[98:99], v28 offset:4096\n"
"ds_read_b64_tr_b16 v[102:103], v29 offset:4096\n"
"ds_read_b64_tr_b16 v[106:107], v30 offset:4096\n"
"ds_read_b64_tr_b16 v[110:111], v31 offset:4096\n"
"s_add_u32 s64, s64, s79\n"
"s_add_u32 s64, s64, 0xfffffe23\n"
"s_sub_u32 s65, s65, s64\n"
"s_max_i32 s65, s65, 0\n"
"s_mov_b32 s86, 0x1000\n"
".LkLPC015:\n"
"s_memrealtime s[66:67]\n"
"s_waitcnt lgkmcnt(0)\n"
"s_lshl_b32 s66, s66, 8\n"
"s_sub_u32 s66, s66, s64\n"
"s_cmp_ge_i32 s66, s65\n"
"s_cbranch_scc1 .LkLPC115\n"
"s_sub_u32 s86, s86, 1\n"
"s_cbranch_scc0 .LkLPC015\n"
".LkLPC115:\n"
"s_add_u32 s65, s66, s64\n"
"s_add_u32 s65, s65, 13824\n"
"s_waitcnt lgkmcnt(0)\n"
"v_cmp_gt_i32 vcc, 0xb0000019, v14\n"
"s_cbranch_vccz .LkLPD11\n"
"s_mov_b32 s86, 0x10000\n"
".LkLP11:\n"
"ds_read_b32 v14, v13\n"
"s_waitcnt lgkmcnt(0)\n"
"v_cmp_gt_i32 vcc, 0xb0000019, v14\n"
"s_cbranch_vccz .LkLPD11\n"
"s_sleep 1\n"
"s_sub_u32 s86, s86, 1\n"
"s_cbranch_scc0 .LkLP11\n"
".LkLPD11:\n"
"s_mov_b32 s78, 122880\n"
"s_add_u32 m0, s77, 57344\n"
"s_nop 0\n"
"buffer_load_dwordx4 v0, s[44:47], s78 offen nt lds\n"
"buffer_load_dwordx4 v1, s[44:47], s78 offen offset:1024 nt lds\n"
"s_add_u32 m0, s77, 114688\n"
"s_nop 0\n"
"buffer_load_dwordx4 v2, s[48:51], s78 offen nt lds\n"
"buffer_load_dwordx4 v3, s[48:51], s78 offen offset:1024 nt lds\n"
"s_sub_u32 m0, s76, 128\n"
"s_nop 0\n"
"buffer_load_dword off, s[72:75], 0 offset:128 lds\n"
"s_add_u32 m0, s85, 4096\n"
"s_add_u32 s29, s26, 28672\n"
"s_and_b32 s29, s29, 0x7e00\n"
"s_mul_i32 s29, s29, s83\n"
"buffer_load_dwordx4 v20, s[60:63], s29 offen sc1 lds\n"
"buffer_load_dwordx4 v21, s[60:63], s29 offen offset:1024 sc1 lds\n"
"ds_read_b32 v14, v13\n"
"s_waitcnt vmcnt(14)\n"
"ds_read_b64_tr_b16 v[112:113], v28 offset:0\n"
"ds_read_b64_tr_b16 v[116:117], v29 offset:0\n"
"ds_read_b64_tr_b16 v[120:121], v30 offset:0\n"
"ds_read_b64_tr_b16 v[124:125], v31 offset:0\n"
"s_add_u32 s64, s64, s79\n"
"s_add_u32 s64, s64, 0xfffffe23\n"
"s_sub_u32 s65, s65, s64\n"
"s_max_i32 s65, s65, 0\n"
"s_mov_b32 s86, 0x1000\n"
".LkLPC016:\n"
"s_memrealtime s[66:67]\n"
"s_waitcnt lgkmcnt(0)\n"
"s_lshl_b32 s66, s66, 8\n"
"s_sub_u32 s66, s66, s64\n"
"s_cmp_ge_i32 s66, s65\n"
"s_cbranch_scc1 .LkLPC116\n"
"s_sub_u32 s86, s86, 1\n"
"s_cbranch_scc0 .LkLPC016\n"
".LkLPC116:\n"
"s_add_u32 s65, s66, s64\n"
"s_add_u32 s65, s65, 13824\n"
"s_waitcnt lgkmcnt(0)\n"
"v_cmp_gt_i32 vcc, 0xb000001a, v14\n"
"s_cbranch_vccz .LkLPD12\n"
"s_mov_b32 s86, 0x10000\n"
".LkLP12:\n"
"ds_read_b32 v14, v13\n"
"s_waitcnt lgkmcnt(0)\n"
"v_cmp_gt_i32 vcc, 0xb000001a, v14\n"
"s_cbranch_vccz .LkLPD12\n"
"s_sleep 1\n"
"s_sub_u32 s86, s86, 1\n"
"s_cbranch_scc0 .LkLP12\n"
".LkLPD12:\n"
"s_mov_b32 s78, 131072\n"
"s_add_u32 m0, s77, 0\n"
"s_nop 0\n"
"buffer_load_dwordx4 v0, s[44:47], s78 offen nt lds\n"
"buffer_load_dwordx4 v1, s[44:47], s78 offen offset:1024 nt lds\n"
"s_add_u32 m0, s77, 122880\n"
"s_nop 0\n"
"buffer_load_dwordx4 v2, s[48:51], s78 offen nt lds\n"
"buffer_load_dwordx4 v3, s[48:51], s78 offen offset:1024 nt lds\n"
"s_sub_u32 m0, s76, 132\n"
"s_nop 0\n"
"buffer_load_dword off, s[72:75], 0 offset:132 lds\n"
"s_add_u32 m0, s85, 0\n"
"s_add_u32 s29, s26, 29184\n"
"s_and_b32 s29, s29, 0x7e00\n"
"s_mul_i32 s29, s29, s83\n"
"buffer_load_dwordx4 v20, s[60:63], s29 offen sc1 lds\n"
"buffer_load_dwordx4 v21, s[60:63], s29 offen offset:1024 sc1 lds\n"
"ds_read_b32 v14, v13\n"
"s_waitcnt vmcnt(14)\n"
"ds_read_b64_tr_b16 v[114:115], v28 offset:2048\n"
"ds_read_b64_tr_b16 v[118:119], v29 offset:2048\n"
"ds_read_b64_tr_b16 v[122:123], v30 offset:2048\n"
"ds_read_b64_tr_b16 v[126:127], v31 offset:2048\n"
"s_add_u32 s64, s64, s79\n"
"s_add_u32 s64, s64, 0xfffffe23\n"
"s_sub_u32 s65, s65, s64\n"
"s_max_i32 s65, s65, 0\n"
"s_mov_b32 s86, 0x1000\n"
".LkLPC017:\n"
"s_memrealtime s[66:67]\n"
"s_waitcnt lgkmcnt(0)\n"
"s_lshl_b32 s66, s66, 8\n"
"s_sub_u32 s66, s66, s64\n"
"s_cmp_ge_i32 s66, s65\n"
"s_cbranch_scc1 .LkLPC117\n"
"s_sub_u32 s86, s86, 1\n"
"s_cbranch_scc0 .LkLPC017\n"
".LkLPC117:\n"
"s_add_u32 s65, s66, s64\n"
"s_add_u32 s65, s65, 13824\n"
"s_waitcnt lgkmcnt(0)\n"
"v_cmp_gt_i32 vcc, 0xb000001b, v14\n"
"s_cbranch_vccz .LkLPD13\n"
"s_mov_b32 s86, 0x10000\n"
".LkLP13:\n"
"ds_read_b32 v14, v13\n"
"s_waitcnt lgkmcnt(0)\n"
"v_cmp_gt_i32 vcc, 0xb000001b, v14\n"
"s_cbranch_vccz .LkLPD13\n"
"s_sleep 1\n"
"s_sub_u32 s86, s86, 1\n"
"s_cbranch_scc0 .LkLP13\n"
".LkLPD13:\n"
"s_mov_b32 s78, 139264\n"
"s_add_u32 m0, s77, 8192\n"
"s_nop 0\n"
"buffer_load_dwordx4 v0, s[44:47], s78 offen nt lds\n"
"buffer_load_dwordx4 v1, s[44:47], s78 offen offset:1024 nt lds\n"
"s_add_u32 m0, s77, 65536\n"
"s_nop 0\n"
"buffer_load_dwordx4 v2, s[48:51], s78 offen nt lds\n"
"buffer_load_dwordx4 v3, s[48:51], s78 offen offset:1024 nt lds\n"
"s_sub_u32 m0, s76, 136\n"
"s_nop 0\n"
"buffer_load_dword off, s[72:75], 0 offset:136 lds\n"
"s_add_u32 m0, s85, 2048\n"
"s_add_u32 s29, s26, 29696\n"
"s_and_b32 s29, s29, 0x7e00\n"
"s_mul_i32 s29, s29, s83\n"
"buffer_load_dwordx4 v20, s[60:63], s29 offen sc1 lds\n"
"buffer_load_dwordx4 v21, s[60:63], s29 offen offset:1024 sc1 lds\n"
"ds_read_b32 v14, v13\n"
"s_waitcnt vmcnt(14)\n"
"ds_read_b64_tr_b16 v[128:129], v28 offset:4096\n"
"ds_read_b64_tr_b16 v[132:133], v29 offset:4096\n"
"ds_read_b64_tr_b16 v[136:137], v30 offset:4096\n"
"ds_read_b64_tr_b16 v[140:141], v31 offset:4096\n"
"s_add_u32 s64, s64, s79\n"
"s_add_u32 s64, s64, 0xffffff23\n"
"s_sub_u32 s65, s65, s64\n"
"s_max_i32 s65, s65, 0\n"
"s_mov_b32 s86, 0x1000\n"
".LkLPC018:\n"
"s_memrealtime s[66:67]\n"
"s_waitcnt lgkmcnt(0)\n"
"s_lshl_b32 s66, s66, 8\n"
"s_sub_u32 s66, s66, s64\n"
"s_cmp_ge_i32 s66, s65\n"
"s_cbranch_scc1 .LkLPC118\n"
"s_sub_u32 s86, s86, 1\n"
"s_cbranch_scc0 .LkLPC018\n"
".LkLPC118:\n"
"s_add_u32 s65, s66, s64\n"
"s_add_u32 s65, s65, 13824\n"
"s_waitcnt lgkmcnt(0)\n"
"v_cmp_gt_i32 vcc, 0xb000001c, v14\n"
"s_cbranch_vccz .LkLPD14\n"
"s_mov_b32 s86, 0x10000\n"
".LkLP14:\n"
"ds_read_b32 v14, v13\n"
"s_waitcnt lgkmcnt(0)\n"
"v_cmp_gt_i32 vcc, 0xb000001c, v14\n"
"s_cbranch_vccz .LkLPD14\n"
"s_sleep 1\n"
"s_sub_u32 s86, s86, 1\n"
"s_cbranch_scc0 .LkLP14\n"
".LkLPD14:\n"
"s_mov_b32 s78, 147456\n"
"s_add_u32 m0, s77, 16384\n"
"s_nop 0\n"
"buffer_load_dwordx4 v0, s[44:47], s78 offen nt lds\n"
"buffer_load_dwordx4 v1, s[44:47], s78 offen offset:1024 nt lds\n"
"s_add_u32 m0, s77, 73728\n"
"s_nop 0\n"
"buffer_load_dwordx4 v2, s[48:51], s78 offen nt lds\n"
"buffer_load_dwordx4 v3, s[48:51], s78 offen offset:1024 nt lds\n"
"s_sub_u32 m0, s76, 140\n"
"s_nop 0\n"
"buffer_load_dword off, s[72:75], 0 offset:140 lds\n"
"s_add_u32 m0, s85, 4096\n"
"s_add_u32 s29, s26, 30208\n"
"s_and_b32 s29, s29, 0x7e00\n"
"s_mul_i32 s29, s29, s83\n"
"buffer_load_dwordx4 v20, s[60:63], s29 offen sc1 lds\n"
"buffer_load_dwordx4 v21, s[60:63], s29 offen offset:1024 sc1 lds\n"
"ds_read_b32 v14, v13\n"
"s_waitcnt vmcnt(14)\n"
"ds_read_b64_tr_b16 v[130:131], v28 offset:0\n"
"ds_read_b64_tr_b16 v[134:135], v29 offset:0\n"
"ds_read_b64_tr_b16 v[138:139], v30 offset:0\n"
"ds_read_b64_tr_b16 v[142:143], v31 offset:0\n"
"s_add_u32 s64, s64, s79\n"
"s_add_u32 s64, s64, 0xffffff23\n"
"s_sub_u32 s65, s65, s64\n"
"s_max_i32 s65, s65, 0\n"
"s_mov_b32 s86, 0x1000\n"
".LkLPC019:\n"
"s_memrealtime s[66:67]\n"
"s_waitcnt lgkmcnt(0)\n"
"s_lshl_b32 s66, s66, 8\n"
"s_sub_u32 s66, s66, s64\n"
"s_cmp_ge_i32 s66, s65\n"
"s_cbranch_scc1 .LkLPC119\n"
"s_sub_u32 s86, s86, 1\n"
"s_cbranch_scc0 .LkLPC019\n"
".LkLPC119:\n"
"s_add_u32 s65, s66, s64\n"
"s_add_u32 s65, s65, 13824\n"
"s_waitcnt lgkmcnt(0)\n"
"v_cmp_gt_i32 vcc, 0xb000001d, v14\n"
"s_cbranch_vccz .LkLPD15\n"
"s_mov_b32 s86, 0x10000\n"
".LkLP15:\n"
"ds_read_b32 v14, v13\n"
"s_waitcnt lgkmcnt(0)\n"
"v_cmp_gt_i32 vcc, 0xb000001d, v14\n"
"s_cbranch_vccz .LkLPD15\n"
"s_sleep 1\n"
"s_sub_u32 s86, s86, 1\n"
"s_cbranch_scc0 .LkLP15\n"
".LkLPD15:\n"
"s_mov_b32 s78, 155648\n"
"s_add_u32 m0, s77, 24576\n"
"s_nop 0\n"
"buffer_load_dwordx4 v0, s[44:47], s78 offen nt lds\n"
"buffer_load_dwordx4 v1, s[44:47], s78 offen offset:1024 nt lds\n"
"s_add_u32 m0, s77, 81920\n"
"s_nop 0\n"
"buffer_load_dwordx4 v2, s[48:51], s78 offen nt lds\n"
"buffer_load_dwordx4 v3, s[48:51], s78 offen offset:1024 nt lds\n"
"s_sub_u32 m0, s76, 144\n"
"s_nop 0\n"
"buffer_load_dword off, s[72:75], 0 offset:144 lds\n"
"s_add_u32 m0, s85, 0\n"
"s_add_u32 s29, s26, 30720\n"
"s_and_b32 s29, s29, 0x7e00\n"
"s_mul_i32 s29, s29, s83\n"
"buffer_load_dwordx4 v20, s[60:63], s29 offen sc1 lds\n"
"buffer_load_dwordx4 v21, s[60:63], s29 offen offset:1024 sc1 lds\n"
"ds_read_b32 v14, v13\n"
"s_waitcnt vmcnt(14)\n"
"ds_read_b64_tr_b16 v[144:145], v28 offset:2048\n"
"ds_read_b64_tr_b16 v[148:149], v29 offset:2048\n"
"ds_read_b64_tr_b16 v[152:153], v30 offset:2048\n"
"ds_read_b64_tr_b16 v[156:157], v31 offset:2048\n"
"s_add_u32 s64, s64, s79\n"
"s_add_u32 s64, s64, 0xffffff23\n"
"s_sub_u32 s65, s65, s64\n"
"s_max_i32 s65, s65, 0\n"
"s_mov_b32 s86, 0x1000\n"
".LkLPC020:\n"
"s_memrealtime s[66:67]\n"
"s_waitcnt lgkmcnt(0)\n"
"s_lshl_b32 s66, s66, 8\n"
"s_sub_u32 s66, s66, s64\n"
"s_cmp_ge_i32 s66, s65\n"
"s_cbranch_scc1 .LkLPC120\n"
"s_sub_u32 s86, s86, 1\n"
"s_cbranch_scc0 .LkLPC020\n"
".LkLPC120:\n"
"s_add_u32 s65, s66, s64\n"
"s_add_u32 s65, s65, 13824\n"
"s_waitcnt lgkmcnt(0)\n"
"v_cmp_gt_i32 vcc, 0xb000001e, v14\n"
"s_cbranch_vccz .LkLPD16\n"
"s_mov_b32 s86, 0x10000\n"
".LkLP16:\n"
"ds_read_b32 v14, v13\n"
"s_waitcnt lgkmcnt(0)\n"
"v_cmp_gt_i32 vcc, 0xb000001e, v14\n"
"s_cbranch_vccz .LkLPD16\n"
"s_sleep 1\n"
"s_sub_u32 s86, s86, 1\n"
"s_cbranch_scc0 .LkLP16\n"
".LkLPD16:\n"
"s_mov_b32 s78, 163840\n"
"s_add_u32 m0, s77, 32768\n"
"s_nop 0\n"
"buffer_load_dwordx4 v0, s[44:47], s78 offen nt lds\n"
"buffer_load_dwordx4 v1, s[44:47], s78 offen offset:1024 nt lds\n"
"s_add_u32 m0, s77, 90112\n"
"s_nop 0\n"
"buffer_load_dwordx4 v2, s[48:51], s78 offen nt lds\n"
"buffer_load_dwordx4 v3, s[48:51], s78 offen offset:1024 nt lds\n"
"s_sub_u32 m0, s76, 148\n"
"s_nop 0\n"
"buffer_load_dword off, s[72:75], 0 offset:148 lds\n"
"s_add_u32 m0, s85, 2048\n"
"s_add_u32 s29, s26, 31232\n"
"s_and_b32 s29, s29, 0x7e00\n"
"s_mul_i32 s29, s29, s83\n"
"buffer_load_dwordx4 v20, s[60:63], s29 offen sc1 lds\n"
"buffer_load_dwordx4 v21, s[60:63], s29 offen offset:1024 sc1 lds\n"
"ds_read_b32 v14, v13\n"
"s_waitcnt vmcnt(14)\n"
"ds_read_b64_tr_b16 v[146:147], v28 offset:4096\n"
"ds_read_b64_tr_b16 v[150:151], v29 offset:4096\n"
"ds_read_b64_tr_b16 v[154:155], v30 offset:4096\n"
"ds_read_b64_tr_b16 v[158:159], v31 offset:4096\n"
"s_add_u32 s64, s64, s79\n"
"s_add_u32 s64, s64, 0xffffff23\n"
"s_sub_u32 s65, s65, s64\n"
"s_max_i32 s65, s65, 0\n"
"s_mov_b32 s86, 0x1000\n"
".LkLPC021:\n"
"s_memrealtime s[66:67]\n"
"s_waitcnt lgkmcnt(0)\n"
"s_lshl_b32 s66, s66, 8\n"
"s_sub_u32 s66, s66, s64\n"
"s_cmp_ge_i32 s66, s65\n"
"s_cbranch_scc1 .LkLPC121\n"
"s_sub_u32 s86, s86, 1\n"
"s_cbranch_scc0 .LkLPC021\n"
".LkLPC121:\n"
"s_add_u32 s65, s66, s64\n"
"s_add_u32 s65, s65, 13824\n"
"s_waitcnt lgkmcnt(0)\n"
"v_cmp_gt_i32 vcc, 0xb000001f, v14\n"
"s_cbranch_vccz .LkLPD17\n"
"s_mov_b32 s86, 0x10000\n"
".LkLP17:\n"
"ds_read_b32 v14, v13\n"
"s_waitcnt lgkmcnt(0)\n"
"v_cmp_gt_i32 vcc, 0xb000001f, v14\n"
"s_cbranch_vccz .LkLPD17\n"
"s_sleep 1\n"
"s_sub_u32 s86, s86, 1\n"
"s_cbranch_scc0 .LkLP17\n"
".LkLPD17:\n"
"s_mov_b32 s78, 172032\n"
"s_add_u32 m0, s77, 40960\n"
"s_nop 0\n"
"buffer_load_dwordx4 v0, s[44:47], s78 offen nt lds\n"
"buffer_load_dwordx4 v1, s[44:47], s78 offen offset:1024 nt lds\n"
"s_add_u32 m0, s77, 98304\n"
"s_nop 0\n"
"buffer_load_dwordx4 v2, s[48:51], s78 offen nt lds\n"
"buffer_load_dwordx4 v3, s[48:51], s78 offen offset:1024 nt lds\n"
"s_sub_u32 m0, s76, 152\n"
"s_nop 0\n"
"buffer_load_dword off, s[72:75], 0 offset:152 lds\n"
"s_add_u32 m0, s85, 4096\n"
"s_add_u32 s29, s26, 31744\n"
"s_and_b32 s29, s29, 0x7e00\n"
"s_mul_i32 s29, s29, s83\n"
"buffer_load_dwordx4 v20, s[60:63], s29 offen sc1 lds\n"
"buffer_load_dwordx4 v21, s[60:63], s29 offen offset:1024 sc1 lds\n"
"ds_read_b32 v14, v13\n"
"s_waitcnt vmcnt(14)\n"
"ds_read_b64_tr_b16 v[160:161], v28 offset:0\n"
"ds_read_b64_tr_b16 v[164:165], v29 offset:0\n"
"ds_read_b64_tr_b16 v[168:169], v30 offset:0\n"
"ds_read_b64_tr_b16 v[172:173], v31 offset:0\n"
"s_add_u32 s64, s64, s79\n"
"s_add_u32 s64, s64, 0x23\n"
"s_sub_u32 s65, s65, s64\n"
"s_max_i32 s65, s65, 0\n"
"s_mov_b32 s86, 0x1000\n"
".LkLPC022:\n"
"s_memrealtime s[66:67]\n"
"s_waitcnt lgkmcnt(0)\n"
"s_lshl_b32 s66, s66, 8\n"
"s_sub_u32 s66, s66, s64\n"
"s_cmp_ge_i32 s66, s65\n"
"s_cbranch_scc1 .LkLPC122\n"
"s_sub_u32 s86, s86, 1\n"
"s_cbranch_scc0 .LkLPC022\n"
".LkLPC122:\n"
"s_add_u32 s65, s66, s64\n"
"s_add_u32 s65, s65, 13824\n"
"s_waitcnt lgkmcnt(0)\n"
"v_cmp_gt_i32 vcc, 0xb0000020, v14\n"
"s_cbranch_vccz .LkLPD18\n"
"s_mov_b32 s86, 0x10000\n"
".LkLP18:\n"
"ds_read_b32 v14, v13\n"
"s_waitcnt lgkmcnt(0)\n"
"v_cmp_gt_i32 vcc, 0xb0000020, v14\n"
"s_cbranch_vccz .LkLPD18\n"
"s_sleep 1\n"
"s_sub_u32 s86, s86, 1\n"
"s_cbranch_scc0 .LkLP18\n"
".LkLPD18:\n"
"s_mov_b32 s78, 180224\n"
"s_add_u32 m0, s77, 49152\n"
"s_nop 0\n"
"buffer_load_dwordx4 v0, s[44:47], s78 offen nt lds\n"
"buffer_load_dwordx4 v1, s[44:47], s78 offen offset:1024 nt lds\n"
"s_add_u32 m0, s77, 106496\n"
"s_nop 0\n"
"buffer_load_dwordx4 v2, s[48:51], s78 offen nt lds\n"
"buffer_load_dwordx4 v3, s[48:51], s78 offen offset:1024 nt lds\n"
"s_sub_u32 m0, s76, 156\n"
"s_nop 0\n"
"buffer_load_dword off, s[72:75], 0 offset:156 lds\n"
"s_add_u32 m0, s85, 0\n"
"s_add_u32 s29, s26, 32256\n"
"s_and_b32 s29, s29, 0x7e00\n"
"s_mul_i32 s29, s29, s83\n"
"buffer_load_dwordx4 v20, s[60:63], s29 offen sc1 lds\n"
"buffer_load_dwordx4 v21, s[60:63], s29 offen offset:1024 sc1 lds\n"
"ds_read_b32 v14, v13\n"
"s_waitcnt vmcnt(14)\n"
"ds_read_b64_tr_b16 v[162:163], v28 offset:2048\n"
"ds_read_b64_tr_b16 v[166:167], v29 offset:2048\n"
"ds_read_b64_tr_b16 v[170:171], v30 offset:2048\n"
"ds_read_b64_tr_b16 v[174:175], v31 offset:2048\n"
"s_add_u32 s64, s64, s79\n"
"s_add_u32 s64, s64, 0x23\n"
"s_sub_u32 s65, s65, s64\n"
"s_max_i32 s65, s65, 0\n"
"s_mov_b32 s86, 0x1000\n"
".LkLPC023:\n"
"s_memrealtime s[66:67]\n"
"s_waitcnt lgkmcnt(0)\n"
"s_lshl_b32 s66, s66, 8\n"
"s_sub_u32 s66, s66, s64\n"
"s_cmp_ge_i32 s66, s65\n"
"s_cbranch_scc1 .LkLPC123\n"
"s_sub_u32 s86, s86, 1\n"
"s_cbranch_scc0 .LkLPC023\n"
".LkLPC123:\n"
"s_add_u32 s65, s66, s64\n"
"s_add_u32 s65, s65, 13824\n"
"s_waitcnt lgkmcnt(0)\n"
"v_cmp_gt_i32 vcc, 0xb0000021, v14\n"
"s_cbranch_vccz .LkLPD19\n"
"s_mov_b32 s86, 0x10000\n"
".LkLP19:\n"
"ds_read_b32 v14, v13\n"
"s_waitcnt lgkmcnt(0)\n"
"v_cmp_gt_i32 vcc, 0xb0000021, v14\n"
"s_cbranch_vccz .LkLPD19\n"
"s_sleep 1\n"
"s_sub_u32 s86, s86, 1\n"
"s_cbranch_scc0 .LkLP19\n"
".LkLPD19:\n"
"s_mov_b32 s78, 188416\n"
"s_add_u32 m0, s77, 57344\n"
"s_nop 0\n"
"buffer_load_dwordx4 v0, s[44:47], s78 offen nt lds\n"
"buffer_load_dwordx4 v1, s[44:47], s78 offen offset:1024 nt lds\n"
"s_add_u32 m0, s77, 114688\n"
"s_nop 0\n"
"buffer_load_dwordx4 v2, s[48:51], s78 offen nt lds\n"
"buffer_load_dwordx4 v3, s[48:51], s78 offen offset:1024 nt lds\n"
"s_sub_u32 m0, s76, 160\n"
"s_nop 0\n"
"buffer_load_dword off, s[72:75], 0 offset:160 lds\n"
"s_add_u32 m0, s85, 2048\n"
"s_add_u32 s29, s26, 20480\n"
"s_and_b32 s29, s29, 0x7e00\n"
"s_mul_i32 s29, s29, s83\n"
"buffer_load_dwordx4 v20, s[60:63], s29 offen sc1 lds\n"
"buffer_load_dwordx4 v21, s[60:63], s29 offen offset:1024 sc1 lds\n"
"ds_read_b32 v14, v13\n"
"s_waitcnt vmcnt(14)\n"
"ds_read_b64_tr_b16 v[176:177], v28 offset:4096\n"
"ds_read_b64_tr_b16 v[180:181], v29 offset:4096\n"
"ds_read_b64_tr_b16 v[184:185], v30 offset:4096\n"
"ds_read_b64_tr_b16 v[188:189], v31 offset:4096\n"
"s_add_u32 s64, s64, s79\n"
"s_add_u32 s64, s64, 0x23\n"
"s_sub_u32 s65, s65, s64\n"
"s_max_i32 s65, s65, 0\n"
"s_mov_b32 s86, 0x1000\n"
".LkLPC024:\n"
"s_memrealtime s[66:67]\n"
"s_waitcnt lgkmcnt(0)\n"
"s_lshl_b32 s66, s66, 8\n"
"s_sub_u32 s66, s66, s64\n"
"s_cmp_ge_i32 s66, s65\n"
"s_cbranch_scc1 .LkLPC124\n"
"s_sub_u32 s86, s86, 1\n"
"s_cbranch_scc0 .LkLPC024\n"
".LkLPC124:\n"
"s_add_u32 s65, s66, s64\n"
"s_add_u32 s65, s65, 13824\n"
"s_waitcnt lgkmcnt(0)\n"
"v_cmp_gt_i32 vcc, 0xb0000022, v14\n"
"s_cbranch_vccz .LkLPD20\n"
"s_mov_b32 s86, 0x10000\n"
".LkLP20:\n"
"ds_read_b32 v14, v13\n"
"s_waitcnt lgkmcnt(0)\n"
"v_cmp_gt_i32 vcc, 0xb0000022, v14\n"
"s_cbranch_vccz .LkLPD20\n"
"s_sleep 1\n"
"s_sub_u32 s86, s86, 1\n"
"s_cbranch_scc0 .LkLP20\n"
".LkLPD20:\n"
"s_mov_b32 s78, 196608\n"
"s_add_u32 m0, s77, 0\n"
"s_nop 0\n"
"buffer_load_dwordx4 v0, s[44:47], s78 offen nt lds\n"
"buffer_load_dwordx4 v1, s[44:47], s78 offen offset:1024 nt lds\n"
"s_add_u32 m0, s77, 122880\n"
"s_nop 0\n"
"buffer_load_dwordx4 v2, s[48:51], s78 offen nt lds\n"
"buffer_load_dwordx4 v3, s[48:51], s78 offen offset:1024 nt lds\n"
"s_sub_u32 m0, s76, 164\n"
"s_nop 0\n"
"buffer_load_dword off, s[72:75], 0 offset:164 lds\n"
"s_add_u32 m0, s85, 4096\n"
"s_add_u32 s29, s26, 20992\n"
"s_and_b32 s29, s29, 0x7e00\n"
"s_mul_i32 s29, s29, s83\n"
"buffer_load_dwordx4 v20, s[60:63], s29 offen sc1 lds\n"
"buffer_load_dwordx4 v21, s[60:63], s29 offen offset:1024 sc1 lds\n"
"ds_read_b32 v14, v13\n"
"s_waitcnt vmcnt(14)\n"
"ds_read_b64_tr_b16 v[178:179], v28 offset:0\n"
"ds_read_b64_tr_b16 v[182:183], v29 offset:0\n"
"ds_read_b64_tr_b16 v[186:187], v30 offset:0\n"
"ds_read_b64_tr_b16 v[190:191], v31 offset:0\n"
"s_add_u32 s64, s64, s79\n"
"s_add_u32 s64, s64, 0x23\n"
"s_sub_u32 s65, s65, s64\n"
"s_max_i32 s65, s65, 0\n"
"s_mov_b32 s86, 0x1000\n"
".LkLPC025:\n"
"s_memrealtime s[66:67]\n"
"s_waitcnt lgkmcnt(0)\n"
"s_lshl_b32 s66, s66, 8\n"
"s_sub_u32 s66, s66, s64\n"
"s_cmp_ge_i32 s66, s65\n"
"s_cbranch_scc1 .LkLPC125\n"
"s_sub_u32 s86, s86, 1\n"
"s_cbranch_scc0 .LkLPC025\n"
".LkLPC125:\n"
"s_add_u32 s65, s66, s64\n"
"s_add_u32 s65, s65, 13824\n"
"s_waitcnt lgkmcnt(0)\n"
"v_cmp_gt_i32 vcc, 0xb0000023, v14\n"
"s_cbranch_vccz .LkLPD21\n"
"s_mov_b32 s86, 0x10000\n"
".LkLP21:\n"
"ds_read_b32 v14, v13\n"
"s_waitcnt lgkmcnt(0)\n"
"v_cmp_gt_i32 vcc, 0xb0000023, v14\n"
"s_cbranch_vccz .LkLPD21\n"
"s_sleep 1\n"
"s_sub_u32 s86, s86, 1\n"
"s_cbranch_scc0 .LkLP21\n"
".LkLPD21:\n"
"s_mov_b32 s78, 204800\n"
"s_add_u32 m0, s77, 8192\n"
"s_nop 0\n"
"buffer_load_dwordx4 v0, s[44:47], s78 offen nt lds\n"
"buffer_load_dwordx4 v1, s[44:47], s78 offen offset:1024 nt lds\n"
"s_add_u32 m0, s77, 65536\n"
"s_nop 0\n"
"buffer_load_dwordx4 v2, s[48:51], s78 offen nt lds\n"
"buffer_load_dwordx4 v3, s[48:51], s78 offen offset:1024 nt lds\n"
"s_sub_u32 m0, s76, 168\n"
"s_nop 0\n"
"buffer_load_dword off, s[72:75], 0 offset:168 lds\n"
"s_add_u32 m0, s85, 0\n"
"s_add_u32 s29, s26, 21504\n"
"s_and_b32 s29, s29, 0x7e00\n"
"s_mul_i32 s29, s29, s83\n"
"buffer_load_dwordx4 v20, s[60:63], s29 offen sc1 lds\n"
"buffer_load_dwordx4 v21, s[60:63], s29 offen offset:1024 sc1 lds\n"
"ds_read_b32 v14, v13\n"
"s_waitcnt vmcnt(14)\n"
"ds_read_b64_tr_b16 v[192:193], v28 offset:2048\n"
"ds_read_b64_tr_b16 v[196:197], v29 offset:2048\n"
"ds_read_b64_tr_b16 v[200:201], v30 offset:2048\n"
"ds_read_b64_tr_b16 v[204:205], v31 offset:2048\n"
"s_add_u32 s64, s64, s79\n"
"s_add_u32 s64, s64, 0x123\n"
"s_sub_u32 s65, s65, s64\n"
"s_max_i32 s65, s65, 0\n"
"s_mov_b32 s86, 0x1000\n"
".LkLPC026:\n"
"s_memrealtime s[66:67]\n"
"s_waitcnt lgkmcnt(0)\n"
"s_lshl_b32 s66, s66, 8\n"
"s_sub_u32 s66, s66, s64\n"
"s_cmp_ge_i32 s66, s65\n"
"s_cbranch_scc1 .LkLPC126\n"
"s_sub_u32 s86, s86, 1\n"
"s_cbranch_scc0 .LkLPC026\n"
".LkLPC126:\n"
"s_add_u32 s65, s66, s64\n"
"s_add_u32 s65, s65, 13824\n"
"s_waitcnt lgkmcnt(0)\n"
"v_cmp_gt_i32 vcc, 0xb0000024, v14\n"
"s_cbranch_vccz .LkLPD22\n"
"s_mov_b32 s86, 0x10000\n"
".LkLP22:\n"
"ds_read_b32 v14, v13\n"
"s_waitcnt lgkmcnt(0)\n"
"v_cmp_gt_i32 vcc, 0xb0000024, v14\n"
"s_cbranch_vccz .LkLPD22\n"
"s_sleep 1\n"
"s_sub_u32 s86, s86, 1\n"
"s_cbranch_scc0 .LkLP22\n"
".LkLPD22:\n"
"s_mov_b32 s78, 212992\n"
"s_add_u32 m0, s77, 16384\n"
"s_nop 0\n"
"buffer_load_dwordx4 v0, s[44:47], s78 offen nt lds\n"
"buffer_load_dwordx4 v1, s[44:47], s78 offen offset:1024 nt lds\n"
"s_add_u32 m0, s77, 73728\n"
"s_nop 0\n"
"buffer_load_dwordx4 v2, s[48:51], s78 offen nt lds\n"
"buffer_load_dwordx4 v3, s[48:51], s78 offen offset:1024 nt lds\n"
"s_sub_u32 m0, s76, 172\n"
"s_nop 0\n"
"buffer_load_dword off, s[72:75], 0 offset:172 lds\n"
"s_add_u32 m0, s85, 2048\n"
"s_add_u32 s29, s26, 22016\n"
"s_and_b32 s29, s29, 0x7e00\n"
"s_mul_i32 s29, s29, s83\n"
"buffer_load_dwordx4 v20, s[60:63], s29 offen sc1 lds\n"
"buffer_load_dwordx4 v21, s[60:63], s29 offen offset:1024 sc1 lds\n"
"ds_read_b32 v14, v13\n"
"s_waitcnt vmcnt(14)\n"
"ds_read_b64_tr_b16 v[194:195], v28 offset:4096\n"
"ds_read_b64_tr_b16 v[198:199], v29 offset:4096\n"
"ds_read_b64_tr_b16 v[202:203], v30 offset:4096\n"
"ds_read_b64_tr_b16 v[206:207], v31 offset:4096\n"
"s_add_u32 s64, s64, s79\n"
"s_add_u32 s64, s64, 0x123\n"
"s_sub_u32 s65, s65, s64\n"
"s_max_i32 s65, s65, 0\n"
"s_mov_b32 s86, 0x1000\n"
".LkLPC027:\n"
"s_memrealtime s[66:67]\n"
"s_waitcnt lgkmcnt(0)\n"
"s_lshl_b32 s66, s66, 8\n"
"s_sub_u32 s66, s66, s64\n"
"s_cmp_ge_i32 s66, s65\n"
"s_cbranch_scc1 .LkLPC127\n"
"s_sub_u32 s86, s86, 1\n"
"s_cbranch_scc0 .LkLPC027\n"
".LkLPC127:\n"
"s_add_u32 s65, s66, s64\n"
"s_add_u32 s65, s65, 13824\n"
"s_waitcnt lgkmcnt(0)\n"
"v_cmp_gt_i32 vcc, 0xb0000025, v14\n"
"s_cbranch_vccz .LkLPD23\n"
"s_mov_b32 s86, 0x10000\n"
".LkLP23:\n"
"ds_read_b32 v14, v13\n"
"s_waitcnt lgkmcnt(0)\n"
"v_cmp_gt_i32 vcc, 0xb0000025, v14\n"
"s_cbranch_vccz .LkLPD23\n"
"s_sleep 1\n"
"s_sub_u32 s86, s86, 1\n"
"s_cbranch_scc0 .LkLP23\n"
".LkLPD23:\n"
"s_mov_b32 s78, 221184\n"
"s_add_u32 m0, s77, 24576\n"
"s_nop 0\n"
"buffer_load_dwordx4 v0, s[44:47], s78 offen nt lds\n"
"buffer_load_dwordx4 v1, s[44:47], s78 offen offset:1024 nt lds\n"
"s_add_u32 m0, s77, 81920\n"
"s_nop 0\n"
"buffer_load_dwordx4 v2, s[48:51], s78 offen nt lds\n"
"buffer_load_dwordx4 v3, s[48:51], s78 offen offset:1024 nt lds\n"
"s_sub_u32 m0, s76, 176\n"
"s_nop 0\n"
"buffer_load_dword off, s[72:75], 0 offset:176 lds\n"
"s_add_u32 m0, s85, 4096\n"
"s_add_u32 s29, s26, 22528\n"
"s_and_b32 s29, s29, 0x7e00\n"
"s_mul_i32 s29, s29, s83\n"
"buffer_load_dwordx4 v20, s[60:63], s29 offen sc1 lds\n"
"buffer_load_dwordx4 v21, s[60:63], s29 offen offset:1024 sc1 lds\n"
"ds_read_b32 v14, v13\n"
"s_waitcnt vmcnt(14)\n"
"ds_read_b64_tr_b16 v[208:209], v28 offset:0\n"
"ds_read_b64_tr_b16 v[212:213], v29 offset:0\n"
"ds_read_b64_tr_b16 v[216:217], v30 offset:0\n"
"ds_read_b64_tr_b16 v[220:221], v31 offset:0\n"
"s_add_u32 s64, s64, s79\n"
"s_add_u32 s64, s64, 0x123\n"
"s_sub_u32 s65, s65, s64\n"
"s_max_i32 s65, s65, 0\n"
"s_mov_b32 s86, 0x1000\n"
".LkLPC028:\n"
"s_memrealtime s[66:67]\n"
"s_waitcnt lgkmcnt(0)\n"
"s_lshl_b32 s66, s66, 8\n"
"s_sub_u32 s66, s66, s64\n"
"s_cmp_ge_i32 s66, s65\n"
"s_cbranch_scc1 .LkLPC128\n"
"s_sub_u32 s86, s86, 1\n"
"s_cbranch_scc0 .LkLPC028\n"
".LkLPC128:\n"
"s_add_u32 s65, s66, s64\n"
"s_add_u32 s65, s65, 13824\n"
"s_waitcnt lgkmcnt(0)\n"
"v_cmp_gt_i32 vcc, 0xb0000026, v14\n"
"s_cbranch_vccz .LkLPD24\n"
"s_mov_b32 s86, 0x10000\n"
".LkLP24:\n"
"ds_read_b32 v14, v13\n"
"s_waitcnt lgkmcnt(0)\n"
"v_cmp_gt_i32 vcc, 0xb0000026, v14\n"
"s_cbranch_vccz .LkLPD24\n"
"s_sleep 1\n"
"s_sub_u32 s86, s86, 1\n"
"s_cbranch_scc0 .LkLP24\n"
".LkLPD24:\n"
"s_mov_b32 s78, 229376\n"
"s_add_u32 m0, s77, 32768\n"
"s_nop 0\n"
"buffer_load_dwordx4 v0, s[44:47], s78 offen nt lds\n"
"buffer_load_dwordx4 v1, s[44:47], s78 offen offset:1024 nt lds\n"
"s_add_u32 m0, s77, 90112\n"
"s_nop 0\n"
"buffer_load_dwordx4 v2, s[48:51], s78 offen nt lds\n"
"buffer_load_dwordx4 v3, s[48:51], s78 offen offset:1024 nt lds\n"
"s_sub_u32 m0, s76, 180\n"
"s_nop 0\n"
"buffer_load_dword off, s[72:75], 0 offset:180 lds\n"
"s_add_u32 m0, s85, 0\n"
"s_add_u32 s29, s26, 23040\n"
"s_and_b32 s29, s29, 0x7e00\n"
"s_mul_i32 s29, s29, s83\n"
"buffer_load_dwordx4 v20, s[60:63], s29 offen sc1 lds\n"
"buffer_load_dwordx4 v21, s[60:63], s29 offen offset:1024 sc1 lds\n"
"ds_read_b32 v14, v13\n"
"s_waitcnt vmcnt(14)\n"
"ds_read_b64_tr_b16 v[210:211], v28 offset:2048\n"
"ds_read_b64_tr_b16 v[214:215], v29 offset:2048\n"
"ds_read_b64_tr_b16 v[218:219], v30 offset:2048\n"
"ds_read_b64_tr_b16 v[222:223], v31 offset:2048\n"
"s_add_u32 s64, s64, s79\n"
"s_add_u32 s64, s64, 0x123\n"
"s_sub_u32 s65, s65, s64\n"
"s_max_i32 s65, s65, 0\n"
"s_mov_b32 s86, 0x1000\n"
".LkLPC029:\n"
"s_memrealtime s[66:67]\n"
"s_waitcnt lgkmcnt(0)\n"
"s_lshl_b32 s66, s66, 8\n"
"s_sub_u32 s66, s66, s64\n"
"s_cmp_ge_i32 s66, s65\n"
"s_cbranch_scc1 .LkLPC129\n"
"s_sub_u32 s86, s86, 1\n"
"s_cbranch_scc0 .LkLPC029\n"
".LkLPC129:\n"
"s_add_u32 s65, s66, s64\n"
"s_add_u32 s65, s65, 13824\n"
"s_waitcnt lgkmcnt(0)\n"
"v_cmp_gt_i32 vcc, 0xb0000027, v14\n"
"s_cbranch_vccz .LkLPD25\n"
"s_mov_b32 s86, 0x10000\n"
".LkLP25:\n"
"ds_read_b32 v14, v13\n"
"s_waitcnt lgkmcnt(0)\n"
"v_cmp_gt_i32 vcc, 0xb0000027, v14\n"
"s_cbranch_vccz .LkLPD25\n"
"s_sleep 1\n"
"s_sub_u32 s86, s86, 1\n"
"s_cbranch_scc0 .LkLP25\n"
".LkLPD25:\n"
"s_mov_b32 s78, 237568\n"
"s_add_u32 m0, s77, 40960\n"
"s_nop 0\n"
"buffer_load_dwordx4 v0, s[44:47], s78 offen nt lds\n"
"buffer_load_dwordx4 v1, s[44:47], s78 offen offset:1024 nt lds\n"
"s_add_u32 m0, s77, 98304\n"
"s_nop 0\n"
"buffer_load_dwordx4 v2, s[48:51], s78 offen nt lds\n"
"buffer_load_dwordx4 v3, s[48:51], s78 offen offset:1024 nt lds\n"
"s_sub_u32 m0, s76, 184\n"
"s_nop 0\n"
"buffer_load_dword off, s[72:75], 0 offset:184 lds\n"
"s_add_u32 m0, s85, 2048\n"
"s_add_u32 s29, s26, 23552\n"
"s_and_b32 s29, s29, 0x7e00\n"
"s_mul_i32 s29, s29, s83\n"
"buffer_load_dwordx4 v20, s[60:63], s29 offen sc1 lds\n"
"buffer_load_dwordx4 v21, s[60:63], s29 offen offset:1024 sc1 lds\n"
"ds_read_b32 v14, v13\n"
"s_waitcnt vmcnt(14)\n"
"ds_read_b64_tr_b16 v[224:225], v28 offset:4096\n"
"ds_read_b64_tr_b16 v[228:229], v29 offset:4096\n"
"ds_read_b64_tr_b16 v[232:233], v30 offset:4096\n"
"ds_read_b64_tr_b16 v[236:237], v31 offset:4096\n"
"s_add_u32 s64, s64, s79\n"
"s_add_u32 s64, s64, 0x223\n"
"s_sub_u32 s65, s65, s64\n"
"s_max_i32 s65, s65, 0\n"
"s_mov_b32 s86, 0x1000\n"
".LkLPC030:\n"
"s_memrealtime s[66:67]\n"
"s_waitcnt lgkmcnt(0)\n"
"s_lshl_b32 s66, s66, 8\n"
"s_sub_u32 s66, s66, s64\n"
"s_cmp_ge_i32 s66, s65\n"
"s_cbranch_scc1 .LkLPC130\n"
"s_sub_u32 s86, s86, 1\n"
"s_cbranch_scc0 .LkLPC030\n"
".LkLPC130:\n"
"s_add_u32 s65, s66, s64\n"
"s_add_u32 s65, s65, 13824\n"
"s_waitcnt lgkmcnt(0)\n"
"v_cmp_gt_i32 vcc, 0xb0000028, v14\n"
"s_cbranch_vccz .LkLPD26\n"
"s_mov_b32 s86, 0x10000\n"
".LkLP26:\n"
"ds_read_b32 v14, v13\n"
"s_waitcnt lgkmcnt(0)\n"
"v_cmp_gt_i32 vcc, 0xb0000028, v14\n"
"s_cbranch_vccz .LkLPD26\n"
"s_sleep 1\n"
"s_sub_u32 s86, s86, 1\n"
"s_cbranch_scc0 .LkLP26\n"
".LkLPD26:\n"
"s_mov_b32 s78, 245760\n"
"s_add_u32 m0, s77, 49152\n"
"s_nop 0\n"
"buffer_load_dwordx4 v0, s[44:47], s78 offen nt lds\n"
"buffer_load_dwordx4 v1, s[44:47], s78 offen offset:1024 nt lds\n"
"s_add_u32 m0, s77, 106496\n"
"s_nop 0\n"
"buffer_load_dwordx4 v2, s[48:51], s78 offen nt lds\n"
"buffer_load_dwordx4 v3, s[48:51], s78 offen offset:1024 nt lds\n"
"s_sub_u32 m0, s76, 188\n"
"s_nop 0\n"
"buffer_load_dword off, s[72:75], 0 offset:188 lds\n"
"s_add_u32 m0, s85, 4096\n"
"s_add_u32 s29, s26, 24064\n"
"s_and_b32 s29, s29, 0x7e00\n"
"s_mul_i32 s29, s29, s83\n"
"buffer_load_dwordx4 v20, s[60:63], s29 offen sc1 lds\n"
"buffer_load_dwordx4 v21, s[60:63], s29 offen offset:1024 sc1 lds\n"
"ds_read_b32 v14, v13\n"
"s_waitcnt vmcnt(14)\n"
"ds_read_b64_tr_b16 v[226:227], v28 offset:0\n"
"ds_read_b64_tr_b16 v[230:231], v29 offset:0\n"
"ds_read_b64_tr_b16 v[234:235], v30 offset:0\n"
"ds_read_b64_tr_b16 v[238:239], v31 offset:0\n"
"s_add_u32 s64, s64, s79\n"
"s_add_u32 s64, s64, 0x223\n"
"s_sub_u32 s65, s65, s64\n"
"s_max_i32 s65, s65, 0\n"
"s_mov_b32 s86, 0x1000\n"
".LkLPC031:\n"
"s_memrealtime s[66:67]\n"
"s_waitcnt lgkmcnt(0)\n"
"s_lshl_b32 s66, s66, 8\n"
"s_sub_u32 s66, s66, s64\n"
"s_cmp_ge_i32 s66, s65\n"
"s_cbranch_scc1 .LkLPC131\n"
"s_sub_u32 s86, s86, 1\n"
"s_cbranch_scc0 .LkLPC031\n"
".LkLPC131:\n"
"s_add_u32 s65, s66, s64\n"
"s_add_u32 s65, s65, 13824\n"
"s_waitcnt lgkmcnt(0)\n"
"v_cmp_gt_i32 vcc, 0xb0000029, v14\n"
"s_cbranch_vccz .LkLPD27\n"
"s_mov_b32 s86, 0x10000\n"
".LkLP27:\n"
"ds_read_b32 v14, v13\n"
"s_waitcnt lgkmcnt(0)\n"
"v_cmp_gt_i32 vcc, 0xb0000029, v14\n"
"s_cbranch_vccz .LkLPD27\n"
"s_sleep 1\n"
"s_sub_u32 s86, s86, 1\n"
"s_cbranch_scc0 .LkLP27\n"
".LkLPD27:\n"
"s_mov_b32 s78, 253952\n"
"s_add_u32 m0, s77, 57344\n"
"s_nop 0\n"
"buffer_load_dwordx4 v0, s[44:47], s78 offen nt lds\n"
"buffer_load_dwordx4 v1, s[44:47], s78 offen offset:1024 nt lds\n"
"s_add_u32 m0, s77, 114688\n"
"s_nop 0\n"
"buffer_load_dwordx4 v2, s[48:51], s78 offen nt lds\n"
"buffer_load_dwordx4 v3, s[48:51], s78 offen offset:1024 nt lds\n"
"s_sub_u32 m0, s76, 192\n"
"s_nop 0\n"
"buffer_load_dword off, s[72:75], 0 offset:192 lds\n"
"ds_read_b32 v14, v13\n"
"s_waitcnt vmcnt(12)\n"
"ds_read_b64_tr_b16 v[240:241], v28 offset:2048\n"
"ds_read_b64_tr_b16 v[244:245], v29 offset:2048\n"
"ds_read_b64_tr_b16 v[248:249], v30 offset:2048\n"
"ds_read_b64_tr_b16 v[252:253], v31 offset:2048\n"
"s_add_u32 s64, s64, s79\n"
"s_add_u32 s64, s64, 0x223\n"
"s_sub_u32 s65, s65, s64\n"
"s_max_i32 s65, s65, 0\n"
"s_mov_b32 s86, 0x1000\n"
".LkLPC032:\n"
"s_memrealtime s[66:67]\n"
"s_waitcnt lgkmcnt(0)\n"
"s_lshl_b32 s66, s66, 8\n"
"s_sub_u32 s66, s66, s64\n"
"s_cmp_ge_i32 s66, s65\n"
"s_cbranch_scc1 .LkLPC132\n"
"s_sub_u32 s86, s86, 1\n"
"s_cbranch_scc0 .LkLPC032\n"
".LkLPC132:\n"
"s_add_u32 s65, s66, s64\n"
"s_add_u32 s65, s65, 13824\n"
"s_waitcnt lgkmcnt(0)\n"
"v_cmp_gt_i32 vcc, 0xb000002a, v14\n"
"s_cbranch_vccz .LkLPD28\n"
"s_mov_b32 s86, 0x10000\n"
".LkLP28:\n"
"ds_read_b32 v14, v13\n"
"s_waitcnt lgkmcnt(0)\n"
"v_cmp_gt_i32 vcc, 0xb000002a, v14\n"
"s_cbranch_vccz .LkLPD28\n"
"s_sleep 1\n"
"s_sub_u32 s86, s86, 1\n"
"s_cbranch_scc0 .LkLP28\n"
".LkLPD28:\n"
"s_mov_b32 s78, 262144\n"
"s_add_u32 m0, s77, 122880\n"
"s_nop 0\n"
"buffer_load_dwordx4 v2, s[48:51], s78 offen nt lds\n"
"buffer_load_dwordx4 v3, s[48:51], s78 offen offset:1024 nt lds\n"
"s_sub_u32 m0, s76, 196\n"
"s_nop 0\n"
"buffer_load_dword off, s[72:75], 0 offset:196 lds\n"
"ds_read_b32 v14, v13\n"
"s_waitcnt vmcnt(8)\n"
"ds_read_b64_tr_b16 v[242:243], v28 offset:4096\n"
"ds_read_b64_tr_b16 v[246:247], v29 offset:4096\n"
"ds_read_b64_tr_b16 v[250:251], v30 offset:4096\n"
"ds_read_b64_tr_b16 v[254:255], v31 offset:4096\n"
"s_waitcnt lgkmcnt(0)\n"
"v_cmp_gt_i32 vcc, 0xb000002b, v14\n"
"s_cbranch_vccz .LkLPD29\n"
"s_mov_b32 s86, 0x10000\n"
".LkLP29:\n"
"ds_read_b32 v14, v13\n"
"s_waitcnt lgkmcnt(0)\n"
"v_cmp_gt_i32 vcc, 0xb000002b, v14\n"
"s_cbranch_vccz .LkLPD29\n"
"s_sleep 1\n"
"s_sub_u32 s86, s86, 1\n"
"s_cbranch_scc0 .LkLP29\n"
".LkLPD29:\n"
"s_add_u32 m0, s84, 0\n"
"s_xor_b32 s29, s27, 8192\n"
"s_add_u32 s29, s29, s26\n"
"s_and_b32 s29, s29, 0x7e00\n"
"s_mul_i32 s29, s29, s83\n"
"buffer_load_dwordx4 v20, s[60:63], s29 offen sc1 lds\n"
"buffer_load_dwordx4 v21, s[60:63], s29 offen offset:1024 sc1 lds\n"
"buffer_load_dwordx4 v22, s[60:63], s29 offen offset:2048 sc1 lds\n"
"buffer_load_dwordx4 v23, s[60:63], s29 offen offset:3072 sc1 lds\n"
"ds_read_b32 v14, v13\n"
"s_add_u32 m0, s85, 0\n"
"s_xor_b32 s29, s27, 15360\n"
"s_add_u32 s29, s29, s26\n"
"s_and_b32 s29, s29, 0x7e00\n"
"s_mul_i32 s29, s29, s83\n"
"buffer_load_dwordx4 v20, s[60:63], s29 offen sc1 lds\n"
"buffer_load_dwordx4 v21, s[60:63], s29 offen offset:1024 sc1 lds\n"
"buffer_load_dwordx4 v22, s[60:63], s29 offen offset:2048 sc1 lds\n"
"buffer_load_dwordx4 v23, s[60:63], s29 offen offset:3072 sc1 lds\n"
"s_waitcnt lgkmcnt(0)\n"
"v_cmp_gt_i32 vcc, 0xb000002c, v14\n"
"s_cbranch_vccz .LkLPD30\n"
"s_mov_b32 s86, 0x10000\n"
".LkLP30:\n"
"ds_read_b32 v14, v13\n"
"s_waitcnt lgkmcnt(0)\n"
"v_cmp_gt_i32 vcc, 0xb000002c, v14\n"
"s_cbranch_vccz .LkLPD30\n"
"s_sleep 1\n"
"s_sub_u32 s86, s86, 1\n"
"s_cbranch_scc0 .LkLP30\n"
".LkLPD30:\n"
"s_add_u32 m0, s84, 8192\n"
"s_xor_b32 s29, s27, 9216\n"
"s_add_u32 s29, s29, s26\n"
"s_and_b32 s29, s29, 0x7e00\n"
"s_mul_i32 s29, s29, s83\n"
"buffer_load_dwordx4 v20, s[60:63], s29 offen sc1 lds\n"
"buffer_load_dwordx4 v21, s[60:63], s29 offen offset:1024 sc1 lds\n"
"buffer_load_dwordx4 v22, s[60:63], s29 offen offset:2048 sc1 lds\n"
"buffer_load_dwordx4 v23, s[60:63], s29 offen offset:3072 sc1 lds\n"
"ds_read_b32 v14, v13\n"
"s_waitcnt lgkmcnt(0)\n"
"v_cmp_gt_i32 vcc, 0xb000002d, v14\n"
"s_cbranch_vccz .LkLPD31\n"
"s_mov_b32 s86, 0x10000\n"
".LkLP31:\n"
"ds_read_b32 v14, v13\n"
"s_waitcnt lgkmcnt(0)\n"
"v_cmp_gt_i32 vcc, 0xb000002d, v14\n"
"s_cbranch_vccz .LkLPD31\n"
"s_sleep 1\n"
"s_sub_u32 s86, s86, 1\n"
"s_cbranch_scc0 .LkLP31\n"
".LkLPD31:\n"
"s_add_u32 m0, s84, 16384\n"
"s_xor_b32 s29, s27, 10240\n"
"s_add_u32 s29, s29, s26\n"
"s_and_b32 s29, s29, 0x7e00\n"
"s_mul_i32 s29, s29, s83\n"
"buffer_load_dwordx4 v20, s[60:63], s29 offen sc1 lds\n"
"buffer_load_dwordx4 v21, s[60:63], s29 offen offset:1024 sc1 lds\n"
"buffer_load_dwordx4 v22, s[60:63], s29 offen offset:2048 sc1 lds\n"
"buffer_load_dwordx4 v23, s[60:63], s29 offen offset:3072 sc1 lds\n"
"ds_read_b32 v14, v13\n"
"s_waitcnt lgkmcnt(0)\n"
"v_cmp_gt_i32 vcc, 0xb000002e, v14\n"
"s_cbranch_vccz .LkLPD32\n"
"s_mov_b32 s86, 0x10000\n"
".LkLP32:\n"
"ds_read_b32 v14, v13\n"
"s_waitcnt lgkmcnt(0)\n"
"v_cmp_gt_i32 vcc, 0xb000002e, v14\n"
"s_cbranch_vccz .LkLPD32\n"
"s_sleep 1\n"
"s_sub_u32 s86, s86, 1\n"
"s_cbranch_scc0 .LkLP32\n"
".LkLPD32:\n"
"s_add_u32 m0, s84, 24576\n"
"s_xor_b32 s29, s27, 11264\n"
"s_add_u32 s29, s29, s26\n"
"s_and_b32 s29, s29, 0x7e00\n"
"s_mul_i32 s29, s29, s83\n"
"buffer_load_dwordx4 v20, s[60:63], s29 offen sc1 lds\n"
"buffer_load_dwordx4 v21, s[60:63], s29 offen offset:1024 sc1 lds\n"
"buffer_load_dwordx4 v22, s[60:63], s29 offen offset:2048 sc1 lds\n"
"buffer_load_dwordx4 v23, s[60:63], s29 offen offset:3072 sc1 lds\n"
"ds_read_b32 v14, v13\n"
"s_waitcnt lgkmcnt(0)\n"
"v_cmp_gt_i32 vcc, 0xb000002f, v14\n"
"s_cbranch_vccz .LkLPD33\n"
"s_mov_b32 s86, 0x10000\n"
".LkLP33:\n"
"ds_read_b32 v14, v13\n"
"s_waitcnt lgkmcnt(0)\n"
"v_cmp_gt_i32 vcc, 0xb000002f, v14\n"
"s_cbranch_vccz .LkLPD33\n"
"s_sleep 1\n"
"s_sub_u32 s86, s86, 1\n"
"s_cbranch_scc0 .LkLP33\n"
".LkLPD33:\n"
"s_add_u32 m0, s84, 32768\n"
"s_xor_b32 s29, s27, 12288\n"
"s_add_u32 s29, s29, s26\n"
"s_and_b32 s29, s29, 0x7e00\n"
"s_mul_i32 s29, s29, s83\n"
"buffer_load_dwordx4 v20, s[60:63], s29 offen sc1 lds\n"
"buffer_load_dwordx4 v21, s[60:63], s29 offen offset:1024 sc1 lds\n"
"buffer_load_dwordx4 v22, s[60:63], s29 offen offset:2048 sc1 lds\n"
"buffer_load_dwordx4 v23, s[60:63], s29 offen offset:3072 sc1 lds\n"
"ds_read_b32 v14, v13\n"
"s_waitcnt lgkmcnt(0)\n"
"v_cmp_gt_i32 vcc, 0xb0000030, v14\n"
"s_cbranch_vccz .LkLPD34\n"
"s_mov_b32 s86, 0x10000\n"
".LkLP34:\n"
"ds_read_b32 v14, v13\n"
"s_waitcnt lgkmcnt(0)\n"
"v_cmp_gt_i32 vcc, 0xb0000030, v14\n"
"s_cbranch_vccz .LkLPD34\n"
"s_sleep 1\n"
"s_sub_u32 s86, s86, 1\n"
"s_cbranch_scc0 .LkLP34\n"
".LkLPD34:\n"
"s_add_u32 m0, s84, 40960\n"
"s_xor_b32 s29, s27, 13312\n"
"s_add_u32 s29, s29, s26\n"
"s_and_b32 s29, s29, 0x7e00\n"
"s_mul_i32 s29, s29, s83\n"
"buffer_load_dwordx4 v20, s[60:63], s29 offen sc1 lds\n"
"buffer_load_dwordx4 v21, s[60:63], s29 offen offset:1024 sc1 lds\n"
"buffer_load_dwordx4 v22, s[60:63], s29 offen offset:2048 sc1 lds\n"
"buffer_load_dwordx4 v23, s[60:63], s29 offen offset:3072 sc1 lds\n"
"ds_read_b32 v14, v13\n"
"s_waitcnt lgkmcnt(0)\n"
"v_cmp_gt_i32 vcc, 0xb0000031, v14\n"
"s_cbranch_vccz .LkLPD35\n"
"s_mov_b32 s86, 0x10000\n"
".LkLP35:\n"
"ds_read_b32 v14, v13\n"
"s_waitcnt lgkmcnt(0)\n"
"v_cmp_gt_i32 vcc, 0xb0000031, v14\n"
"s_cbranch_vccz .LkLPD35\n"
"s_sleep 1\n"
"s_sub_u32 s86, s86, 1\n"
"s_cbranch_scc0 .LkLP35\n"
".LkLPD35:\n"
"s_add_u32 m0, s84, 49152\n"
"s_xor_b32 s29, s27, 14336\n"
"s_add_u32 s29, s29, s26\n"
"s_and_b32 s29, s29, 0x7e00\n"
"s_mul_i32 s29, s29, s83\n"
"buffer_load_dwordx4 v20, s[60:63], s29 offen sc1 lds\n"
"buffer_load_dwordx4 v21, s[60:63], s29 offen offset:1024 sc1 lds\n"
"buffer_load_dwordx4 v22, s[60:63], s29 offen offset:2048 sc1 lds\n"
"buffer_load_dwordx4 v23, s[60:63], s29 offen offset:3072 sc1 lds\n"
"ds_read_b32 v14, v13\n"
"s_sub_u32 m0, s76, 200\n"
"s_nop 0\n"
"buffer_load_dword off, s[72:75], 0 offset:200 lds\n"
"s_mov_b32 m0, s76\n"
"v_mov_b32 v79, 0xb0000037\n"
"s_setprio 0\n"
"s_mov_b32 s86, 0x40000\n"
".LkLHW:\n"
"ds_read_b32 v14, v78\n"
"s_waitcnt lgkmcnt(0)\n"
"v_cmp_gt_i32 vcc, 0xb0000034, v14\n"
"s_cbranch_vccz .LkLHWX\n"
"s_sub_u32 s86, s86, 1\n"
"s_cbranch_scc0 .LkLHW\n"
".LkLHWX:\n"
"s_lshl2_add_u32 s78, s34, s40\n"
"s_lshl2_add_u32 s78, s78, s38\n"
"s_lshl_b32 s78, s78, 10\n"
"s_mov_b32 s86, 64\n"
".LkLSP:\n"
"s_buffer_load_dwordx2 s[44:45], s[68:71], s78 offset:0 glc\n"
"s_buffer_load_dwordx2 s[46:47], s[68:71], s78 offset:128 glc\n"
"s_buffer_load_dwordx2 s[48:49], s[68:71], s78 offset:256 glc\n"
"s_buffer_load_dwordx2 s[50:51], s[68:71], s78 offset:384 glc\n"
"s_buffer_load_dwordx2 s[56:57], s[68:71], s78 offset:512 glc\n"
"s_buffer_load_dwordx2 s[58:59], s[68:71], s78 offset:640 glc\n"
"s_buffer_load_dwordx2 s[60:61], s[68:71], s78 offset:768 glc\n"
"s_buffer_load_dwordx2 s[62:63], s[68:71], s78 offset:896 glc\n"
"s_waitcnt lgkmcnt(0)\n"
"s_xor_b64 s[44:45], s[44:45], s[20:21]\n"
"s_xor_b64 s[46:47], s[46:47], s[20:21]\n"
"s_or_b64 s[44:45], s[44:45], s[46:47]\n"
"s_xor_b64 s[48:49], s[48:49], s[20:21]\n"
"s_or_b64 s[44:45], s[44:45], s[48:49]\n"
"s_xor_b64 s[50:51], s[50:51], s[20:21]\n"
"s_or_b64 s[44:45], s[44:45], s[50:51]\n"
"s_xor_b64 s[56:57], s[56:57], s[20:21]\n"
"s_or_b64 s[44:45], s[44:45], s[56:57]\n"
"s_xor_b64 s[58:59], s[58:59], s[20:21]\n"
"s_or_b64 s[44:45], s[44:45], s[58:59]\n"
"s_xor_b64 s[60:61], s[60:61], s[20:21]\n"
"s_or_b64 s[44:45], s[44:45], s[60:61]\n"
"s_xor_b64 s[62:63], s[62:63], s[20:21]\n"
"s_or_b64 s[44:45], s[44:45], s[62:63]\n"
"s_cbranch_scc0 .LkLFD\n"
"s_sleep 1\n"
"s_sub_u32 s86, s86, 1\n"
"s_cbranch_scc0 .LkLSP\n"
"s_mov_b32 s86, 0x8000\n"
".LkLFP:\n"
"buffer_load_dwordx2 v[4:5], v7, s[68:71], 0 offen sc1\n"
"s_waitcnt vmcnt(0)\n"
"v_cmp_ne_u64 vcc, s[20:21], v[4:5]\n"
"s_cbranch_vccz .LkLFD\n"
"s_sleep 2\n"
"s_sub_u32 s86, s86, 1\n"
"s_cbranch_scc0 .LkLFP\n"
".LkLFD:\n"
"s_waitcnt vmcnt(0)\n"
"ds_write_addtid_b32 v79 offset:0\n"
"s_waitcnt lgkmcnt(0)\n"
"v_mov_b32 v79, 0xb000003c\n"
"s_add_u32 s29, s26, 24576\n"
"s_and_b32 s29, s29, 0x7e00\n"
"s_mul_i32 s29, s29, 3\n"
"buffer_load_dwordx4 v[48:51], v76, s[52:55], s29 offen offset:0 sc1\n"
"buffer_load_dwordx4 v[52:55], v76, s[52:55], s29 offen offset:1024 sc1\n"
"buffer_load_dwordx4 v[56:59], v76, s[52:55], s29 offen offset:2048 sc1\n"
"s_add_u32 s29, s26, 25600\n"
"s_and_b32 s29, s29, 0x7e00\n"
"s_mul_i32 s29, s29, 3\n"
"buffer_load_dwordx4 v[60:63], v76, s[52:55], s29 offen offset:0 sc1\n"
"buffer_load_dwordx4 v[64:67], v76, s[52:55], s29 offen offset:1024 sc1\n"
"buffer_load_dwordx4 v[68:71], v76, s[52:55], s29 offen offset:2048 sc1\n"
"s_add_u32 s29, s26, 26624\n"
"s_and_b32 s29, s29, 0x7e00\n"
"s_mul_i32 s29, s29, 3\n"
"buffer_load_dwordx4 v[72:75], v76, s[52:55], s29 offen offset:0 sc1\n"
"s_waitcnt vmcnt(6)\n"
"v_mfma_f32_16x16x32_bf16 v[0:3], v[48:51], v[32:35], 0\n"
"v_mfma_f32_16x16x32_bf16 v[4:7], v[48:51], v[36:39], 0\n"
"v_mfma_f32_16x16x32_bf16 v[8:11], v[48:51], v[40:43], 0\n"
"v_mfma_f32_16x16x32_bf16 v[12:15], v[48:51], v[44:47], 0\n"
"buffer_load_dwordx4 v[48:51], v76, s[52:55], s29 offen offset:1024 sc1\n"
"s_waitcnt vmcnt(6)\n"
"v_mfma_f32_16x16x32_bf16 v[28:31], v[52:55], v[44:47], 0\n"
"v_mfma_f32_16x16x32_bf16 v[24:27], v[52:55], v[40:43], 0\n"
"v_mfma_f32_16x16x32_bf16 v[20:23], v[52:55], v[36:39], 0\n"
"v_mfma_f32_16x16x32_bf16 v[16:19], v[52:55], v[32:35], 0\n"
"buffer_load_dwordx4 v[52:55], v76, s[52:55], s29 offen offset:2048 sc1\n"
"s_waitcnt vmcnt(6)\n"
"v_mfma_f32_16x16x32_bf16 v[32:35], v[56:59], v[32:35], 0\n"
"v_mfma_f32_16x16x32_bf16 v[36:39], v[56:59], v[36:39], 0\n"
"v_mfma_f32_16x16x32_bf16 v[40:43], v[56:59], v[40:43], 0\n"
"v_mfma_f32_16x16x32_bf16 v[44:47], v[56:59], v[44:47], 0\n"
"s_add_u32 s29, s26, 27648\n"
"s_and_b32 s29, s29, 0x7e00\n"
"s_mul_i32 s29, s29, 3\n"
"buffer_load_dwordx4 v[56:59], v76, s[52:55], s29 offen offset:0 sc1\n"
"s_waitcnt vmcnt(6)\n"
"v_mfma_f32_16x16x32_bf16 v[12:15], v[60:63], v[92:95], v[12:15]\n"
"v_mfma_f32_16x16x32_bf16 v[8:11], v[60:63], v[88:91], v[8:11]\n"
"v_mfma_f32_16x16x32_bf16 v[4:7], v[60:63], v[84:87], v[4:7]\n"
"v_mfma_f32_16x16x32_bf16 v[0:3], v[60:63], v[80:83], v[0:3]\n"
"buffer_load_dwordx4 v[60:63], v76, s[52:55], s29 offen offset:1024 sc1\n"
"s_waitcnt vmcnt(6)\n"
"v_mfma_f32_16x16x32_bf16 v[16:19], v[64:67], v[80:83], v[16:19]\n"
"v_mfma_f32_16x16x32_bf16 v[20:23], v[64:67], v[84:87], v[20:23]\n"
"v_mfma_f32_16x16x32_bf16 v[24:27], v[64:67], v[88:91], v[24:27]\n"
"v_mfma_f32_16x16x32_bf16 v[28:31], v[64:67], v[92:95], v[28:31]\n"
"buffer_load_dwordx4 v[64:67], v76, s[52:55], s29 offen offset:2048 sc1\n"
"s_waitcnt vmcnt(6)\n"
"v_mfma_f32_16x16x32_bf16 v[44:47], v[68:71], v[92:95], v[44:47]\n"
"v_mfma_f32_16x16x32_bf16 v[40:43], v[68:71], v[88:91], v[40:43]\n"
"v_mfma_f32_16x16x32_bf16 v[36:39], v[68:71], v[84:87], v[36:39]\n"
"v_mfma_f32_16x16x32_bf16 v[32:35], v[68:71], v[80:83], v[32:35]\n"
"s_add_u32 s29, s26, 28672\n"
"s_and_b32 s29, s29, 0x7e00\n"
"s_mul_i32 s29, s29, 3\n"
"buffer_load_dwordx4 v[68:71], v76, s[52:55], s29 offen offset:0 sc1\n"
"s_waitcnt vmcnt(6)\n"
"v_mfma_f32_16x16x32_bf16 v[0:3], v[72:75], v[96:99], v[0:3]\n"
"v_mfma_f32_16x16x32_bf16 v[4:7], v[72:75], v[100:103], v[4:7]\n"
"v_mfma_f32_16x16x32_bf16 v[8:11], v[72:75], v[104:107], v[8:11]\n"
"v_mfma_f32_16x16x32_bf16 v[12:15], v[72:75], v[108:111], v[12:15]\n"
"buffer_load_dwordx4 v[72:75], v76, s[52:55], s29 offen offset:1024 sc1\n"
"s_waitcnt vmcnt(6)\n"
"v_mfma_f32_16x16x32_bf16 v[28:31], v[48:51], v[108:111], v[28:31]\n"
"v_mfma_f32_16x16x32_bf16 v[24:27], v[48:51], v[104:107], v[24:27]\n"
"v_mfma_f32_16x16x32_bf16 v[20:23], v[48:51], v[100:103], v[20:23]\n"
"v_mfma_f32_16x16x32_bf16 v[16:19], v[48:51], v[96:99], v[16:19]\n"
"buffer_load_dwordx4 v[48:51], v76, s[52:55], s29 offen offset:2048 sc1\n"
"s_waitcnt vmcnt(6)\n"
"v_mfma_f32_16x16x32_bf16 v[32:35], v[52:55], v[96:99], v[32:35]\n"
"v_mfma_f32_16x16x32_bf16 v[36:39], v[52:55], v[100:103], v[36:39]\n"
"v_mfma_f32_16x16x32_bf16 v[40:43], v[52:55], v[104:107], v[40:43]\n"
"v_mfma_f32_16x16x32_bf16 v[44:47], v[52:55], v[108:111], v[44:47]\n"
"s_add_u32 s29, s26, 29696\n"
"s_and_b32 s29, s29, 0x7e00\n"
"s_mul_i32 s29, s29, 3\n"
"buffer_load_dwordx4 v[52:55], v76, s[52:55], s29 offen offset:0 sc1\n"
"s_waitcnt vmcnt(6)\n"
"v_mfma_f32_16x16x32_bf16 v[12:15], v[56:59], v[124:127], v[12:15]\n"
"v_mfma_f32_16x16x32_bf16 v[8:11], v[56:59], v[120:123], v[8:11]\n"
"v_mfma_f32_16x16x32_bf16 v[4:7], v[56:59], v[116:119], v[4:7]\n"
"v_mfma_f32_16x16x32_bf16 v[0:3], v[56:59], v[112:115], v[0:3]\n"
"buffer_load_dwordx4 v[56:59], v76, s[52:55], s29 offen offset:1024 sc1\n"
"s_waitcnt vmcnt(6)\n"
"v_mfma_f32_16x16x32_bf16 v[16:19], v[60:63], v[112:115], v[16:19]\n"
"v_mfma_f32_16x16x32_bf16 v[20:23], v[60:63], v[116:119], v[20:23]\n"
"v_mfma_f32_16x16x32_bf16 v[24:27], v[60:63], v[120:123], v[24:27]\n"
"v_mfma_f32_16x16x32_bf16 v[28:31], v[60:63], v[124:127], v[28:31]\n"
"buffer_load_dwordx4 v[60:63], v76, s[52:55], s29 offen offset:2048 sc1\n"
"s_waitcnt vmcnt(6)\n"
"v_mfma_f32_16x16x32_bf16 v[44:47], v[64:67], v[124:127], v[44:47]\n"
"v_mfma_f32_16x16x32_bf16 v[40:43], v[64:67], v[120:123], v[40:43]\n"
"v_mfma_f32_16x16x32_bf16 v[36:39], v[64:67], v[116:119], v[36:39]\n"
"v_mfma_f32_16x16x32_bf16 v[32:35], v[64:67], v[112:115], v[32:35]\n"
"s_add_u32 s29, s26, 30720\n"
"s_and_b32 s29, s29, 0x7e00\n"
"s_mul_i32 s29, s29, 3\n"
"buffer_load_dwordx4 v[64:67], v76, s[52:55], s29 offen offset:0 sc1\n"
"s_waitcnt vmcnt(6)\n"
"v_mfma_f32_16x16x32_bf16 v[0:3], v[68:71], v[128:131], v[0:3]\n"
"v_mfma_f32_16x16x32_bf16 v[4:7], v[68:71], v[132:135], v[4:7]\n"
"v_mfma_f32_16x16x32_bf16 v[8:11], v[68:71], v[136:139], v[8:11]\n"
"v_mfma_f32_16x16x32_bf16 v[12:15], v[68:71], v[140:143], v[12:15]\n"
"buffer_load_dwordx4 v[68:71], v76, s[52:55], s29 offen offset:1024 sc1\n"
"s_waitcnt vmcnt(6)\n"
"v_mfma_f32_16x16x32_bf16 v[28:31], v[72:75], v[140:143], v[28:31]\n"
"v_mfma_f32_16x16x32_bf16 v[24:27], v[72:75], v[136:139], v[24:27]\n"
"v_mfma_f32_16x16x32_bf16 v[20:23], v[72:75], v[132:135], v[20:23]\n"
"v_mfma_f32_16x16x32_bf16 v[16:19], v[72:75], v[128:131], v[16:19]\n"
"buffer_load_dwordx4 v[72:75], v76, s[52:55], s29 offen offset:2048 sc1\n"
"s_waitcnt vmcnt(6)\n"
"v_mfma_f32_16x16x32_bf16 v[32:35], v[48:51], v[128:131], v[32:35]\n"
"v_mfma_f32_16x16x32_bf16 v[36:39], v[48:51], v[132:135], v[36:39]\n"
"v_mfma_f32_16x16x32_bf16 v[40:43], v[48:51], v[136:139], v[40:43]\n"
"v_mfma_f32_16x16x32_bf16 v[44:47], v[48:51], v[140:143], v[44:47]\n"
"s_add_u32 s29, s26, 31744\n"
"s_and_b32 s29, s29, 0x7e00\n"
"s_mul_i32 s29, s29, 3\n"
"buffer_load_dwordx4 v[48:51], v76, s[52:55], s29 offen offset:0 sc1\n"
"s_waitcnt vmcnt(6)\n"
"v_mfma_f32_16x16x32_bf16 v[12:15], v[52:55], v[156:159], v[12:15]\n"
"v_mfma_f32_16x16x32_bf16 v[8:11], v[52:55], v[152:155], v[8:11]\n"
"v_mfma_f32_16x16x32_bf16 v[4:7], v[52:55], v[148:151], v[4:7]\n"
"v_mfma_f32_16x16x32_bf16 v[0:3], v[52:55], v[144:147], v[0:3]\n"
"buffer_load_dwordx4 v[52:55], v76, s[52:55], s29 offen offset:1024 sc1\n"
"s_waitcnt vmcnt(6)\n"
"v_mfma_f32_16x16x32_bf16 v[16:19], v[56:59], v[144:147], v[16:19]\n"
"v_mfma_f32_16x16x32_bf16 v[20:23], v[56:59], v[148:151], v[20:23]\n"
"v_mfma_f32_16x16x32_bf16 v[24:27], v[56:59], v[152:155], v[24:27]\n"
"v_mfma_f32_16x16x32_bf16 v[28:31], v[56:59], v[156:159], v[28:31]\n"
"buffer_load_dwordx4 v[56:59], v76, s[52:55], s29 offen offset:2048 sc1\n"
"s_waitcnt vmcnt(6)\n"
"v_mfma_f32_16x16x32_bf16 v[44:47], v[60:63], v[156:159], v[44:47]\n"
"v_mfma_f32_16x16x32_bf16 v[40:43], v[60:63], v[152:155], v[40:43]\n"
"v_mfma_f32_16x16x32_bf16 v[36:39], v[60:63], v[148:151], v[36:39]\n"
"v_mfma_f32_16x16x32_bf16 v[32:35], v[60:63], v[144:147], v[32:35]\n"
"s_add_u32 s29, s26, 20480\n"
"s_and_b32 s29, s29, 0x7e00\n"
"s_mul_i32 s29, s29, 3\n"
"buffer_load_dwordx4 v[60:63], v76, s[52:55], s29 offen offset:0 sc1\n"
"s_waitcnt vmcnt(6)\n"
"v_mfma_f32_16x16x32_bf16 v[0:3], v[64:67], v[160:163], v[0:3]\n"
"v_mfma_f32_16x16x32_bf16 v[4:7], v[64:67], v[164:167], v[4:7]\n"
"v_mfma_f32_16x16x32_bf16 v[8:11], v[64:67], v[168:171], v[8:11]\n"
"v_mfma_f32_16x16x32_bf16 v[12:15], v[64:67], v[172:175], v[12:15]\n"
"buffer_load_dwordx4 v[64:67], v76, s[52:55], s29 offen offset:1024 sc1\n"
"s_waitcnt vmcnt(6)\n"
"v_mfma_f32_16x16x32_bf16 v[28:31], v[68:71], v[172:175], v[28:31]\n"
"v_mfma_f32_16x16x32_bf16 v[24:27], v[68:71], v[168:171], v[24:27]\n"
"v_mfma_f32_16x16x32_bf16 v[20:23], v[68:71], v[164:167], v[20:23]\n"
"v_mfma_f32_16x16x32_bf16 v[16:19], v[68:71], v[160:163], v[16:19]\n"
"buffer_load_dwordx4 v[68:71], v76, s[52:55], s29 offen offset:2048 sc1\n"
"s_waitcnt vmcnt(6)\n"
"v_mfma_f32_16x16x32_bf16 v[32:35], v[72:75], v[160:163], v[32:35]\n"
"v_mfma_f32_16x16x32_bf16 v[36:39], v[72:75], v[164:167], v[36:39]\n"
"v_mfma_f32_16x16x32_bf16 v[40:43], v[72:75], v[168:171], v[40:43]\n"
"v_mfma_f32_16x16x32_bf16 v[44:47], v[72:75], v[172:175], v[44:47]\n"
"s_add_u32 s29, s26, 21504\n"
"s_and_b32 s29, s29, 0x7e00\n"
"s_mul_i32 s29, s29, 3\n"
"buffer_load_dwordx4 v[72:75], v76, s[52:55], s29 offen offset:0 sc1\n"
"v_xor_b32 v80, 0, v77\n"
"v_xor_b32 v81, 8, v77\n"
"v_xor_b32 v82, 16, v77\n"
"v_xor_b32 v83, 24, v77\n"
"s_sub_u32 s85, s85, s84\n"
"v_add_u32 v84, s85, v80\n"
"v_add_u32 v85, s85, v81\n"
"v_add_u32 v86, s85, v82\n"
"v_add_u32 v87, s85, v83\n"
"s_mov_b32 s86, 0x40000\n"
".LkLHX:\n"
"ds_read_b32 v88, v78\n"
"s_waitcnt lgkmcnt(0)\n"
"v_cmp_gt_i32 vcc, 0xb0000039, v88\n"
"s_cbranch_vccz .LkLHXX\n"
"s_sub_u32 s86, s86, 1\n"
"s_cbranch_scc0 .LkLHX\n"
".LkLHXX:\n"
"ds_read_b64_tr_b16 v[96:97], v80 offset:0\n"
"ds_read_b64_tr_b16 v[98:99], v80 offset:2048\n"
"ds_read_b64_tr_b16 v[100:101], v81 offset:0\n"
"ds_read_b64_tr_b16 v[102:103], v81 offset:2048\n"
"ds_read_b64_tr_b16 v[104:105], v82 offset:0\n"
"ds_read_b64_tr_b16 v[106:107], v82 offset:2048\n"
"ds_read_b64_tr_b16 v[108:109], v83 offset:0\n"
"ds_read_b64_tr_b16 v[110:111], v83 offset:2048\n"
"ds_read_b64_tr_b16 v[112:113], v80 offset:8192\n"
"ds_read_b64_tr_b16 v[114:115], v80 offset:10240\n"
"ds_read_b64_tr_b16 v[116:117], v81 offset:8192\n"
"ds_read_b64_tr_b16 v[118:119], v81 offset:10240\n"
"ds_read_b64_tr_b16 v[120:121], v82 offset:8192\n"
"ds_read_b64_tr_b16 v[122:123], v82 offset:10240\n"
"ds_read_b64_tr_b16 v[124:125], v83 offset:8192\n"
"ds_read_b64_tr_b16 v[126:127], v83 offset:10240\n"
"s_waitcnt vmcnt(6)\n"
"v_mfma_f32_16x16x32_bf16 v[12:15], v[48:51], v[188:191], v[12:15]\n"
"v_mfma_f32_16x16x32_bf16 v[8:11], v[48:51], v[184:187], v[8:11]\n"
"v_mfma_f32_16x16x32_bf16 v[4:7], v[48:51], v[180:183], v[4:7]\n"
"v_mfma_f32_16x16x32_bf16 v[0:3], v[48:51], v[176:179], v[0:3]\n"
"buffer_load_dwordx4 v[48:51], v76, s[52:55], s29 offen offset:1024 sc1\n"
"s_waitcnt vmcnt(6)\n"
"v_mfma_f32_16x16x32_bf16 v[16:19], v[52:55], v[176:179], v[16:19]\n"
"v_mfma_f32_16x16x32_bf16 v[20:23], v[52:55], v[180:183], v[20:23]\n"
"v_mfma_f32_16x16x32_bf16 v[24:27], v[52:55], v[184:187], v[24:27]\n"
"v_mfma_f32_16x16x32_bf16 v[28:31], v[52:55], v[188:191], v[28:31]\n"
"buffer_load_dwordx4 v[52:55], v76, s[52:55], s29 offen offset:2048 sc1\n"
"s_waitcnt vmcnt(6)\n"
"v_mfma_f32_16x16x32_bf16 v[44:47], v[56:59], v[188:191], v[44:47]\n"
"v_mfma_f32_16x16x32_bf16 v[40:43], v[56:59], v[184:187], v[40:43]\n"
"v_mfma_f32_16x16x32_bf16 v[36:39], v[56:59], v[180:183], v[36:39]\n"
"v_mfma_f32_16x16x32_bf16 v[32:35], v[56:59], v[176:179], v[32:35]\n"
"s_add_u32 s29, s26, 22528\n"
"s_and_b32 s29, s29, 0x7e00\n"
"s_mul_i32 s29, s29, 3\n"
"buffer_load_dwordx4 v[56:59], v76, s[52:55], s29 offen offset:0 sc1\n"
"ds_read_b64_tr_b16 v[128:129], v80 offset:16384\n"
"ds_read_b64_tr_b16 v[130:131], v80 offset:18432\n"
"ds_read_b64_tr_b16 v[132:133], v81 offset:16384\n"
"ds_read_b64_tr_b16 v[134:135], v81 offset:18432\n"
"ds_read_b64_tr_b16 v[136:137], v82 offset:16384\n"
"ds_read_b64_tr_b16 v[138:139], v82 offset:18432\n"
"ds_read_b64_tr_b16 v[140:141], v83 offset:16384\n"
"ds_read_b64_tr_b16 v[142:143], v83 offset:18432\n"
"ds_read_b64_tr_b16 v[144:145], v80 offset:24576\n"
"ds_read_b64_tr_b16 v[146:147], v80 offset:26624\n"
"ds_read_b64_tr_b16 v[148:149], v81 offset:24576\n"
"ds_read_b64_tr_b16 v[150:151], v81 offset:26624\n"
"ds_read_b64_tr_b16 v[152:153], v82 offset:24576\n"
"ds_read_b64_tr_b16 v[154:155], v82 offset:26624\n"
"ds_read_b64_tr_b16 v[156:157], v83 offset:24576\n"
"ds_read_b64_tr_b16 v[158:159], v83 offset:26624\n"
"ds_write_addtid_b32 v79 offset:0\n"
"s_waitcnt vmcnt(6)\n"
"v_mfma_f32_16x16x32_bf16 v[0:3], v[60:63], v[192:195], v[0:3]\n"
"v_mfma_f32_16x16x32_bf16 v[4:7], v[60:63], v[196:199], v[4:7]\n"
"v_mfma_f32_16x16x32_bf16 v[8:11], v[60:63], v[200:203], v[8:11]\n"
"v_mfma_f32_16x16x32_bf16 v[12:15], v[60:63], v[204:207], v[12:15]\n"
"buffer_load_dwordx4 v[60:63], v76, s[52:55], s29 offen offset:1024 sc1\n"
"s_waitcnt vmcnt(6)\n"
"v_mfma_f32_16x16x32_bf16 v[28:31], v[64:67], v[204:207], v[28:31]\n"
"v_mfma_f32_16x16x32_bf16 v[24:27], v[64:67], v[200:203], v[24:27]\n"
"v_mfma_f32_16x16x32_bf16 v[20:23], v[64:67], v[196:199], v[20:23]\n"
"v_mfma_f32_16x16x32_bf16 v[16:19], v[64:67], v[192:195], v[16:19]\n"
"buffer_load_dwordx4 v[64:67], v76, s[52:55], s29 offen offset:2048 sc1\n"
"s_waitcnt vmcnt(6)\n"
"v_mfma_f32_16x16x32_bf16 v[32:35], v[68:71], v[192:195], v[32:35]\n"
"v_mfma_f32_16x16x32_bf16 v[36:39], v[68:71], v[196:199], v[36:39]\n"
"v_mfma_f32_16x16x32_bf16 v[40:43], v[68:71], v[200:203], v[40:43]\n"
"v_mfma_f32_16x16x32_bf16 v[44:47], v[68:71], v[204:207], v[44:47]\n"
"s_add_u32 s29, s26, 23552\n"
"s_and_b32 s29, s29, 0x7e00\n"
"s_mul_i32 s29, s29, 3\n"
"buffer_load_dwordx4 v[68:71], v76, s[52:55], s29 offen offset:0 sc1\n"
"ds_read_b64_tr_b16 v[160:161], v80 offset:32768\n"
"ds_read_b64_tr_b16 v[162:163], v80 offset:34816\n"
"ds_read_b64_tr_b16 v[164:165], v81 offset:32768\n"
"ds_read_b64_tr_b16 v[166:167], v81 offset:34816\n"
"ds_read_b64_tr_b16 v[168:169], v82 offset:32768\n"
"ds_read_b64_tr_b16 v[170:171], v82 offset:34816\n"
"ds_read_b64_tr_b16 v[172:173], v83 offset:32768\n"
"ds_read_b64_tr_b16 v[174:175], v83 offset:34816\n"
"ds_read_b64_tr_b16 v[176:177], v80 offset:40960\n"
"ds_read_b64_tr_b16 v[178:179], v80 offset:43008\n"
"ds_read_b64_tr_b16 v[180:181], v81 offset:40960\n"
"ds_read_b64_tr_b16 v[182:183], v81 offset:43008\n"
"ds_read_b64_tr_b16 v[184:185], v82 offset:40960\n"
"ds_read_b64_tr_b16 v[186:187], v82 offset:43008\n"
"ds_read_b64_tr_b16 v[188:189], v83 offset:40960\n"
"ds_read_b64_tr_b16 v[190:191], v83 offset:43008\n"
"s_waitcnt vmcnt(6)\n"
"v_mfma_f32_16x16x32_bf16 v[12:15], v[72:75], v[220:223], v[12:15]\n"
"v_mfma_f32_16x16x32_bf16 v[8:11], v[72:75], v[216:219], v[8:11]\n"
"v_mfma_f32_16x16x32_bf16 v[4:7], v[72:75], v[212:215], v[4:7]\n"
"v_mfma_f32_16x16x32_bf16 v[0:3], v[72:75], v[208:211], v[0:3]\n"
"buffer_load_dwordx4 v[72:75], v76, s[52:55], s29 offen offset:1024 sc1\n"
"s_waitcnt vmcnt(6)\n"
"v_mfma_f32_16x16x32_bf16 v[16:19], v[48:51], v[208:211], v[16:19]\n"
"v_mfma_f32_16x16x32_bf16 v[20:23], v[48:51], v[212:215], v[20:23]\n"
"v_mfma_f32_16x16x32_bf16 v[24:27], v[48:51], v[216:219], v[24:27]\n"
"v_mfma_f32_16x16x32_bf16 v[28:31], v[48:51], v[220:223], v[28:31]\n"
"buffer_load_dwordx4 v[48:51], v76, s[52:55], s29 offen offset:2048 sc1\n"
"s_waitcnt vmcnt(6)\n"
"v_mfma_f32_16x16x32_bf16 v[44:47], v[52:55], v[220:223], v[44:47]\n"
"v_mfma_f32_16x16x32_bf16 v[40:43], v[52:55], v[216:219], v[40:43]\n"
"v_mfma_f32_16x16x32_bf16 v[36:39], v[52:55], v[212:215], v[36:39]\n"
"v_mfma_f32_16x16x32_bf16 v[32:35], v[52:55], v[208:211], v[32:35]\n"
"s_xor_b32 s29, s27, 4096\n"
"s_add_u32 s29, s29, s26\n"
"s_and_b32 s29, s29, 0x7e00\n"
"s_mul_i32 s29, s29, 3\n"
"buffer_load_dwordx4 v[52:55], v76, s[52:55], s29 offen offset:0 sc1\n"
"ds_read_b64_tr_b16 v[192:193], v80 offset:49152\n"
"ds_read_b64_tr_b16 v[194:195], v80 offset:51200\n"
"ds_read_b64_tr_b16 v[196:197], v81 offset:49152\n"
"ds_read_b64_tr_b16 v[198:199], v81 offset:51200\n"
"ds_read_b64_tr_b16 v[200:201], v82 offset:49152\n"
"ds_read_b64_tr_b16 v[202:203], v82 offset:51200\n"
"ds_read_b64_tr_b16 v[204:205], v83 offset:49152\n"
"ds_read_b64_tr_b16 v[206:207], v83 offset:51200\n"
"ds_read_b64_tr_b16 v[208:209], v84 offset:0\n"
"ds_read_b64_tr_b16 v[210:211], v84 offset:2048\n"
"ds_read_b64_tr_b16 v[212:213], v85 offset:0\n"
"ds_read_b64_tr_b16 v[214:215], v85 offset:2048\n"
"ds_read_b64_tr_b16 v[216:217], v86 offset:0\n"
"ds_read_b64_tr_b16 v[218:219], v86 offset:2048\n"
"ds_read_b64_tr_b16 v[220:221], v87 offset:0\n"
"ds_read_b64_tr_b16 v[222:223], v87 offset:2048\n"
"s_waitcnt vmcnt(6)\n"
"v_mfma_f32_16x16x32_bf16 v[0:3], v[56:59], v[224:227], v[0:3]\n"
"v_mfma_f32_16x16x32_bf16 v[4:7], v[56:59], v[228:231], v[4:7]\n"
"v_mfma_f32_16x16x32_bf16 v[8:11], v[56:59], v[232:235], v[8:11]\n"
"v_mfma_f32_16x16x32_bf16 v[12:15], v[56:59], v[236:239], v[12:15]\n"
"buffer_load_dwordx4 v[56:59], v76, s[52:55], s29 offen offset:1024 sc1\n"
"s_waitcnt vmcnt(6)\n"
"v_mfma_f32_16x16x32_bf16 v[28:31], v[60:63], v[236:239], v[28:31]\n"
"v_mfma_f32_16x16x32_bf16 v[24:27], v[60:63], v[232:235], v[24:27]\n"
"v_mfma_f32_16x16x32_bf16 v[20:23], v[60:63], v[228:231], v[20:23]\n"
"v_mfma_f32_16x16x32_bf16 v[16:19], v[60:63], v[224:227], v[16:19]\n"
"buffer_load_dwordx4 v[60:63], v76, s[52:55], s29 offen offset:2048 sc1\n"
"s_waitcnt vmcnt(6)\n"
"v_mfma_f32_16x16x32_bf16 v[32:35], v[64:67], v[224:227], v[32:35]\n"
"v_mfma_f32_16x16x32_bf16 v[36:39], v[64:67], v[228:231], v[36:39]\n"
"v_mfma_f32_16x16x32_bf16 v[40:43], v[64:67], v[232:235], v[40:43]\n"
"v_mfma_f32_16x16x32_bf16 v[44:47], v[64:67], v[236:239], v[44:47]\n"
"s_xor_b32 s29, s27, 5120\n"
"s_add_u32 s29, s29, s26\n"
"s_and_b32 s29, s29, 0x7e00\n"
"s_mul_i32 s29, s29, 3\n"
"buffer_load_dwordx4 v[64:67], v76, s[52:55], s29 offen offset:0 sc1\n"
"s_waitcnt vmcnt(6)\n"
"v_mfma_f32_16x16x32_bf16 v[12:15], v[68:71], v[252:255], v[12:15]\n"
"v_mfma_f32_16x16x32_bf16 v[8:11], v[68:71], v[248:251], v[8:11]\n"
"v_mfma_f32_16x16x32_bf16 v[4:7], v[68:71], v[244:247], v[4:7]\n"
"v_mfma_f32_16x16x32_bf16 v[0:3], v[68:71], v[240:243], v[0:3]\n"
"buffer_load_dwordx4 v[68:71], v76, s[52:55], s29 offen offset:1024 sc1\n"
"s_waitcnt vmcnt(6)\n"
"v_mfma_f32_16x16x32_bf16 v[16:19], v[72:75], v[240:243], v[16:19]\n"
"v_mfma_f32_16x16x32_bf16 v[20:23], v[72:75], v[244:247], v[20:23]\n"
"v_mfma_f32_16x16x32_bf16 v[24:27], v[72:75], v[248:251], v[24:27]\n"
"v_mfma_f32_16x16x32_bf16 v[28:31], v[72:75], v[252:255], v[28:31]\n"
"buffer_load_dwordx4 v[72:75], v76, s[52:55], s29 offen offset:2048 sc1\n"
"s_waitcnt vmcnt(6)\n"
"v_mfma_f32_16x16x32_bf16 v[44:47], v[48:51], v[252:255], v[44:47]\n"
"v_mfma_f32_16x16x32_bf16 v[40:43], v[48:51], v[248:251], v[40:43]\n"
"v_mfma_f32_16x16x32_bf16 v[36:39], v[48:51], v[244:247], v[36:39]\n"
"v_mfma_f32_16x16x32_bf16 v[32:35], v[48:51], v[240:243], v[32:35]\n"
"s_xor_b32 s29, s27, 6144\n"
"s_add_u32 s29, s29, s26\n"
"s_and_b32 s29, s29, 0x7e00\n"
"s_mul_i32 s29, s29, 3\n"
"buffer_load_dwordx4 v[48:51], v76, s[52:55], s29 offen offset:0 sc1\n"
"s_waitcnt vmcnt(6)\n"
"s_waitcnt lgkmcnt(15)\n"
"v_mfma_f32_16x16x32_bf16 v[0:3], v[52:55], v[96:99], v[0:3]\n"
"v_mfma_f32_16x16x32_bf16 v[4:7], v[52:55], v[100:103], v[4:7]\n"
"v_mfma_f32_16x16x32_bf16 v[8:11], v[52:55], v[104:107], v[8:11]\n"
"v_mfma_f32_16x16x32_bf16 v[12:15], v[52:55], v[108:111], v[12:15]\n"
"buffer_load_dwordx4 v[52:55], v76, s[52:55], s29 offen offset:1024 sc1\n"
"s_waitcnt vmcnt(6)\n"
"v_mfma_f32_16x16x32_bf16 v[28:31], v[56:59], v[108:111], v[28:31]\n"
"v_mfma_f32_16x16x32_bf16 v[24:27], v[56:59], v[104:107], v[24:27]\n"
"v_mfma_f32_16x16x32_bf16 v[20:23], v[56:59], v[100:103], v[20:23]\n"
"v_mfma_f32_16x16x32_bf16 v[16:19], v[56:59], v[96:99], v[16:19]\n"
"buffer_load_dwordx4 v[56:59], v76, s[52:55], s29 offen offset:2048 sc1\n"
"s_waitcnt vmcnt(6)\n"
"v_mfma_f32_16x16x32_bf16 v[32:35], v[60:63], v[96:99], v[32:35]\n"
"v_mfma_f32_16x16x32_bf16 v[36:39], v[60:63], v[100:103], v[36:39]\n"
"v_mfma_f32_16x16x32_bf16 v[40:43], v[60:63], v[104:107], v[40:43]\n"
"v_mfma_f32_16x16x32_bf16 v[44:47], v[60:63], v[108:111], v[44:47]\n"
"s_xor_b32 s29, s27, 7168\n"
"s_add_u32 s29, s29, s26\n"
"s_and_b32 s29, s29, 0x7e00\n"
"s_mul_i32 s29, s29, 3\n"
"buffer_load_dwordx4 v[60:63], v76, s[52:55], s29 offen offset:0 sc1\n"
"s_waitcnt vmcnt(6)\n"
"s_waitcnt lgkmcnt(15)\n"
"v_mfma_f32_16x16x32_bf16 v[12:15], v[64:67], v[124:127], v[12:15]\n"
"v_mfma_f32_16x16x32_bf16 v[8:11], v[64:67], v[120:123], v[8:11]\n"
"v_mfma_f32_16x16x32_bf16 v[4:7], v[64:67], v[116:119], v[4:7]\n"
"v_mfma_f32_16x16x32_bf16 v[0:3], v[64:67], v[112:115], v[0:3]\n"
"buffer_load_dwordx4 v[64:67], v76, s[52:55], s29 offen offset:1024 sc1\n"
"s_waitcnt vmcnt(6)\n"
"v_mfma_f32_16x16x32_bf16 v[16:19], v[68:71], v[112:115], v[16:19]\n"
"v_mfma_f32_16x16x32_bf16 v[20:23], v[68:71], v[116:119], v[20:23]\n"
"v_mfma_f32_16x16x32_bf16 v[24:27], v[68:71], v[120:123], v[24:27]\n"
"v_mfma_f32_16x16x32_bf16 v[28:31], v[68:71], v[124:127], v[28:31]\n"
"buffer_load_dwordx4 v[68:71], v76, s[52:55], s29 offen offset:2048 sc1\n"
"s_waitcnt vmcnt(6)\n"
"v_mfma_f32_16x16x32_bf16 v[44:47], v[72:75], v[124:127], v[44:47]\n"
"v_mfma_f32_16x16x32_bf16 v[40:43], v[72:75], v[120:123], v[40:43]\n"
"v_mfma_f32_16x16x32_bf16 v[36:39], v[72:75], v[116:119], v[36:39]\n"
"v_mfma_f32_16x16x32_bf16 v[32:35], v[72:75], v[112:115], v[32:35]\n"
"s_xor_b32 s29, s27, 0\n"
"s_add_u32 s29, s29, s26\n"
"s_and_b32 s29, s29, 0x7e00\n"
"s_mul_i32 s29, s29, 3\n"
"buffer_load_dwordx4 v[72:75], v76, s[52:55], s29 offen offset:0 sc1\n"
"s_cmp_lg_u32 s43, 0\n"
"s_cbranch_scc1 .LkLB0\n"
"s_cmp_lg_u32 s39, 7\n"
"s_cbranch_scc1 .LkLB0\n"
"v_mbcnt_lo_u32_b32 v79, -1, 0\n"
"v_mbcnt_hi_u32_b32 v79, -1, v79\n"
"v_lshlrev_b32 v112, 3, v79\n"
"v_add_u32 v104, 0x20000, v112\n"
"v_add_u32 v105, 0x20200, v112\n"
"v_add_u32 v106, 0x20400, v112\n"
"v_add_u32 v107, 0x20600, v112\n"
"buffer_load_dwordx2 v[96:97], v104, s[68:71], 0 offen sc1\n"
"buffer_load_dwordx2 v[98:99], v105, s[68:71], 0 offen sc1\n"
"buffer_load_dwordx2 v[100:101], v106, s[68:71], 0 offen sc1\n"
"buffer_load_dwordx2 v[102:103], v107, s[68:71], 0 offen sc1\n"
".LkLB0:\n"
"s_waitcnt vmcnt(6)\n"
"s_waitcnt lgkmcnt(15)\n"
"v_mfma_f32_16x16x32_bf16 v[0:3], v[48:51], v[128:131], v[0:3]\n"
"v_mfma_f32_16x16x32_bf16 v[4:7], v[48:51], v[132:135], v[4:7]\n"
"v_mfma_f32_16x16x32_bf16 v[8:11], v[48:51], v[136:139], v[8:11]\n"
"v_mfma_f32_16x16x32_bf16 v[12:15], v[48:51], v[140:143], v[12:15]\n"
"buffer_load_dwordx4 v[48:51], v76, s[52:55], s29 offen offset:1024 sc1\n"
"s_waitcnt vmcnt(6)\n"
"v_mfma_f32_16x16x32_bf16 v[28:31], v[52:55], v[140:143], v[28:31]\n"
"v_mfma_f32_16x16x32_bf16 v[24:27], v[52:55], v[136:139], v[24:27]\n"
"v_mfma_f32_16x16x32_bf16 v[20:23], v[52:55], v[132:135], v[20:23]\n"
"v_mfma_f32_16x16x32_bf16 v[16:19], v[52:55], v[128:131], v[16:19]\n"
"buffer_load_dwordx4 v[52:55], v76, s[52:55], s29 offen offset:2048 sc1\n"
"s_waitcnt vmcnt(6)\n"
"v_mfma_f32_16x16x32_bf16 v[32:35], v[56:59], v[128:131], v[32:35]\n"
"v_mfma_f32_16x16x32_bf16 v[36:39], v[56:59], v[132:135], v[36:39]\n"
"v_mfma_f32_16x16x32_bf16 v[40:43], v[56:59], v[136:139], v[40:43]\n"
"v_mfma_f32_16x16x32_bf16 v[44:47], v[56:59], v[140:143], v[44:47]\n"
"s_xor_b32 s29, s27, 1024\n"
"s_add_u32 s29, s29, s26\n"
"s_and_b32 s29, s29, 0x7e00\n"
"s_mul_i32 s29, s29, 3\n"
"buffer_load_dwordx4 v[56:59], v76, s[52:55], s29 offen offset:0 sc1\n"
"s_lshr_b32 s81, s38, 1\n"
"s_mul_i32 s81, s81, 49152\n"
"s_lshl_b32 s82, s38, 12\n"
"s_add_u32 m0, s81, s82\n"
"s_waitcnt vmcnt(6)\n"
"s_waitcnt lgkmcnt(15)\n"
"v_mfma_f32_16x16x32_bf16 v[12:15], v[60:63], v[156:159], v[12:15]\n"
"v_mfma_f32_16x16x32_bf16 v[8:11], v[60:63], v[152:155], v[8:11]\n"
"v_mfma_f32_16x16x32_bf16 v[4:7], v[60:63], v[148:151], v[4:7]\n"
"v_mfma_f32_16x16x32_bf16 v[0:3], v[60:63], v[144:147], v[0:3]\n"
"s_xor_b32 s29, s27, 2048\n"
"s_add_u32 s29, s29, s26\n"
"s_and_b32 s29, s29, 0x7e00\n"
"s_mul_i32 s29, s29, 3\n"
"buffer_load_dwordx4 v[60:63], v76, s[52:55], s29 offen offset:0 sc1\n"
"s_waitcnt vmcnt(6)\n"
"v_mfma_f32_16x16x32_bf16 v[16:19], v[64:67], v[144:147], v[16:19]\n"
"v_mfma_f32_16x16x32_bf16 v[20:23], v[64:67], v[148:151], v[20:23]\n"
"v_mfma_f32_16x16x32_bf16 v[24:27], v[64:67], v[152:155], v[24:27]\n"
"v_mfma_f32_16x16x32_bf16 v[28:31], v[64:67], v[156:159], v[28:31]\n"
"s_xor_b32 s29, s27, 3072\n"
"s_add_u32 s29, s29, s26\n"
"s_and_b32 s29, s29, 0x7e00\n"
"s_mul_i32 s29, s29, 3\n"
"buffer_load_dwordx4 v[64:67], v76, s[52:55], s29 offen offset:0 sc1\n"
"s_waitcnt vmcnt(6)\n"
"v_mfma_f32_16x16x32_bf16 v[44:47], v[68:71], v[156:159], v[44:47]\n"
"v_mfma_f32_16x16x32_bf16 v[40:43], v[68:71], v[152:155], v[40:43]\n"
"v_mfma_f32_16x16x32_bf16 v[36:39], v[68:71], v[148:151], v[36:39]\n"
"v_mfma_f32_16x16x32_bf16 v[32:35], v[68:71], v[144:147], v[32:35]\n"
"s_xor_b32 s29, s27, 1024\n"
"s_add_u32 s29, s29, s26\n"
"s_and_b32 s29, s29, 0x7e00\n"
"s_mul_i32 s29, s29, 3\n"
"buffer_load_dwordx4 v[68:71], v76, s[52:55], s29 offen offset:1024 sc1\n"
"s_waitcnt vmcnt(6)\n"
"s_waitcnt lgkmcnt(15)\n"
"v_mfma_f32_16x16x32_bf16 v[0:3], v[72:75], v[160:163], v[0:3]\n"
"v_mfma_f32_16x16x32_bf16 v[4:7], v[72:75], v[164:167], v[4:7]\n"
"v_mfma_f32_16x16x32_bf16 v[8:11], v[72:75], v[168:171], v[8:11]\n"
"v_mfma_f32_16x16x32_bf16 v[12:15], v[72:75], v[172:175], v[12:15]\n"
"s_xor_b32 s29, s27, 2048\n"
"s_add_u32 s29, s29, s26\n"
"s_and_b32 s29, s29, 0x7e00\n"
"s_mul_i32 s29, s29, 3\n"
"buffer_load_dwordx4 v[72:75], v76, s[52:55], s29 offen offset:1024 sc1\n"
"s_waitcnt vmcnt(6)\n"
"v_mfma_f32_16x16x32_bf16 v[28:31], v[48:51], v[172:175], v[28:31]\n"
"v_mfma_f32_16x16x32_bf16 v[24:27], v[48:51], v[168:171], v[24:27]\n"
"v_mfma_f32_16x16x32_bf16 v[20:23], v[48:51], v[164:167], v[20:23]\n"
"v_mfma_f32_16x16x32_bf16 v[16:19], v[48:51], v[160:163], v[16:19]\n"
"s_xor_b32 s29, s27, 3072\n"
"s_add_u32 s29, s29, s26\n"
"s_and_b32 s29, s29, 0x7e00\n"
"s_mul_i32 s29, s29, 3\n"
"buffer_load_dwordx4 v[48:51], v76, s[52:55], s29 offen offset:1024 sc1\n"
"s_waitcnt vmcnt(6)\n"
"v_mfma_f32_16x16x32_bf16 v[32:35], v[52:55], v[160:163], v[32:35]\n"
"v_mfma_f32_16x16x32_bf16 v[36:39], v[52:55], v[164:167], v[36:39]\n"
"v_mfma_f32_16x16x32_bf16 v[40:43], v[52:55], v[168:171], v[40:43]\n"
"v_mfma_f32_16x16x32_bf16 v[44:47], v[52:55], v[172:175], v[44:47]\n"
"s_xor_b32 s29, s27, 1024\n"
"s_add_u32 s29, s29, s26\n"
"s_and_b32 s29, s29, 0x7e00\n"
"s_mul_i32 s29, s29, 3\n"
"buffer_load_dwordx4 v[52:55], v76, s[52:55], s29 offen offset:2048 sc1\n"
"s_waitcnt vmcnt(6)\n"
"s_waitcnt lgkmcnt(15)\n"
"v_mfma_f32_16x16x32_bf16 v[0:3], v[56:59], v[176:179], v[0:3]\n"
"v_mfma_f32_16x16x32_bf16 v[4:7], v[56:59], v[180:183], v[4:7]\n"
"v_mfma_f32_16x16x32_bf16 v[8:11], v[56:59], v[184:187], v[8:11]\n"
"v_mfma_f32_16x16x32_bf16 v[12:15], v[56:59], v[188:191], v[12:15]\n"
"s_xor_b32 s29, s27, 2048\n"
"s_add_u32 s29, s29, s26\n"
"s_and_b32 s29, s29, 0x7e00\n"
"s_mul_i32 s29, s29, 3\n"
"buffer_load_dwordx4 v[56:59], v76, s[52:55], s29 offen offset:2048 sc1\n"
"s_waitcnt vmcnt(6)\n"
"s_waitcnt lgkmcnt(8)\n"
"v_mfma_f32_16x16x32_bf16 v[0:3], v[60:63], v[192:195], v[0:3]\n"
"v_mfma_f32_16x16x32_bf16 v[4:7], v[60:63], v[196:199], v[4:7]\n"
"v_mfma_f32_16x16x32_bf16 v[8:11], v[60:63], v[200:203], v[8:11]\n"
"v_mfma_f32_16x16x32_bf16 v[12:15], v[60:63], v[204:207], v[12:15]\n"
"s_xor_b32 s29, s27, 3072\n"
"s_add_u32 s29, s29, s26\n"
"s_and_b32 s29, s29, 0x7e00\n"
"s_mul_i32 s29, s29, 3\n"
"buffer_load_dwordx4 v[60:63], v76, s[52:55], s29 offen offset:2048 sc1\n"
"s_waitcnt vmcnt(6)\n"
"s_waitcnt lgkmcnt(0)\n"
"v_mfma_f32_16x16x32_bf16 v[0:3], v[64:67], v[208:211], v[0:3]\n"
"v_mfma_f32_16x16x32_bf16 v[4:7], v[64:67], v[212:215], v[4:7]\n"
"v_mfma_f32_16x16x32_bf16 v[8:11], v[64:67], v[216:219], v[8:11]\n"
"v_mfma_f32_16x16x32_bf16 v[12:15], v[64:67], v[220:223], v[12:15]\n"
"s_waitcnt vmcnt(5)\n"
"v_mfma_f32_16x16x32_bf16 v[16:19], v[68:71], v[176:179], v[16:19]\n"
"s_nop 2\n"
"ds_write_addtid_b32 v0 offset:34816\n"
"v_mfma_f32_16x16x32_bf16 v[20:23], v[68:71], v[180:183], v[20:23]\n"
"ds_write_addtid_b32 v1 offset:35072\n"
"ds_write_addtid_b32 v2 offset:35328\n"
"v_mfma_f32_16x16x32_bf16 v[24:27], v[68:71], v[184:187], v[24:27]\n"
"ds_write_addtid_b32 v3 offset:35584\n"
"ds_write_addtid_b32 v4 offset:35840\n"
"v_mfma_f32_16x16x32_bf16 v[28:31], v[68:71], v[188:191], v[28:31]\n"
"ds_write_addtid_b32 v5 offset:36096\n"
"ds_write_addtid_b32 v6 offset:36352\n"
"s_waitcnt vmcnt(4)\n"
"v_mfma_f32_16x16x32_bf16 v[16:19], v[72:75], v[192:195], v[16:19]\n"
"ds_write_addtid_b32 v7 offset:36608\n"
"ds_write_addtid_b32 v8 offset:36864\n"
"v_mfma_f32_16x16x32_bf16 v[20:23], v[72:75], v[196:199], v[20:23]\n"
"ds_write_addtid_b32 v9 offset:37120\n"
"ds_write_addtid_b32 v10 offset:37376\n"
"v_mfma_f32_16x16x32_bf16 v[24:27], v[72:75], v[200:203], v[24:27]\n"
"ds_write_addtid_b32 v11 offset:37632\n"
"ds_write_addtid_b32 v12 offset:37888\n"
"v_mfma_f32_16x16x32_bf16 v[28:31], v[72:75], v[204:207], v[28:31]\n"
"ds_write_addtid_b32 v13 offset:38144\n"
"ds_write_addtid_b32 v14 offset:38400\n"
"s_waitcnt vmcnt(3)\n"
"v_mfma_f32_16x16x32_bf16 v[16:19], v[48:51], v[208:211], v[16:19]\n"
"ds_write_addtid_b32 v15 offset:38656\n"
"v_mfma_f32_16x16x32_bf16 v[20:23], v[48:51], v[212:215], v[20:23]\n"
"v_mfma_f32_16x16x32_bf16 v[24:27], v[48:51], v[216:219], v[24:27]\n"
"v_mfma_f32_16x16x32_bf16 v[28:31], v[48:51], v[220:223], v[28:31]\n"
"s_waitcnt vmcnt(2)\n"
"v_mfma_f32_16x16x32_bf16 v[32:35], v[52:55], v[176:179], v[32:35]\n"
"s_nop 1\n"
"ds_write_addtid_b32 v16 offset:43008\n"
"v_mfma_f32_16x16x32_bf16 v[36:39], v[52:55], v[180:183], v[36:39]\n"
"ds_write_addtid_b32 v17 offset:43264\n"
"ds_write_addtid_b32 v18 offset:43520\n"
"v_mfma_f32_16x16x32_bf16 v[40:43], v[52:55], v[184:187], v[40:43]\n"
"ds_write_addtid_b32 v19 offset:43776\n"
"ds_write_addtid_b32 v20 offset:44032\n"
"v_mfma_f32_16x16x32_bf16 v[44:47], v[52:55], v[188:191], v[44:47]\n"
"ds_write_addtid_b32 v21 offset:44288\n"
"ds_write_addtid_b32 v22 offset:44544\n"
"s_waitcnt vmcnt(1)\n"
"v_mfma_f32_16x16x32_bf16 v[32:35], v[56:59], v[192:195], v[32:35]\n"
"ds_write_addtid_b32 v23 offset:44800\n"
"ds_write_addtid_b32 v24 offset:45056\n"
"v_mfma_f32_16x16x32_bf16 v[36:39], v[56:59], v[196:199], v[36:39]\n"
"ds_write_addtid_b32 v25 offset:45312\n"
"ds_write_addtid_b32 v26 offset:45568\n"
"v_mfma_f32_16x16x32_bf16 v[40:43], v[56:59], v[200:203], v[40:43]\n"
"ds_write_addtid_b32 v27 offset:45824\n"
"ds_write_addtid_b32 v28 offset:46080\n"
"v_mfma_f32_16x16x32_bf16 v[44:47], v[56:59], v[204:207], v[44:47]\n"
"ds_write_addtid_b32 v29 offset:46336\n"
"ds_write_addtid_b32 v30 offset:46592\n"
"s_waitcnt vmcnt(0)\n"
"v_mfma_f32_16x16x32_bf16 v[32:35], v[60:63], v[208:211], v[32:35]\n"
"ds_write_addtid_b32 v31 offset:46848\n"
"v_mfma_f32_16x16x32_bf16 v[36:39], v[60:63], v[212:215], v[36:39]\n"
"v_mfma_f32_16x16x32_bf16 v[40:43], v[60:63], v[216:219], v[40:43]\n"
"v_mfma_f32_16x16x32_bf16 v[44:47], v[60:63], v[220:223], v[44:47]\n"
"s_nop 3\n"
"ds_write_addtid_b32 v32 offset:51200\n"
"ds_write_addtid_b32 v33 offset:51456\n"
"ds_write_addtid_b32 v34 offset:51712\n"
"ds_write_addtid_b32 v35 offset:51968\n"
"ds_write_addtid_b32 v36 offset:52224\n"
"ds_write_addtid_b32 v37 offset:52480\n"
"ds_write_addtid_b32 v38 offset:52736\n"
"ds_write_addtid_b32 v39 offset:52992\n"
"ds_write_addtid_b32 v40 offset:53248\n"
"ds_write_addtid_b32 v41 offset:53504\n"
"ds_write_addtid_b32 v42 offset:53760\n"
"ds_write_addtid_b32 v43 offset:54016\n"
"ds_write_addtid_b32 v44 offset:54272\n"
"ds_write_addtid_b32 v45 offset:54528\n"
"ds_write_addtid_b32 v46 offset:54784\n"
"ds_write_addtid_b32 v47 offset:55040\n"
"s_waitcnt lgkmcnt(0)\n"
"s_barrier\n"
"s_branch .LkLBUMP\n"
".LkLCOMP:\n"
"s_memrealtime s[84:85]\n"
"s_lshl_b32 m0, s38, 8\n"
"v_mov_b32 v250, 0xb0000000\n"
"ds_write_addtid_b32 v250 offset:0\n"
"ds_write_addtid_b32 v250 offset:1024\n"
"s_getpc_b64 s[76:77]\n"
".LkLPC:\n"
"s_sub_u32 s76, s76, .LkLPC-.LkLHQ0\n"
"s_subb_u32 s77, s77, 0\n"
"s_lshl_b32 s78, s38, 13\n"
"v_lshl_add_u32 v0, v255, 7, s78\n"
"global_load_dword v253, v0, s[76:77]\n"
"v_add_u32 v0, 0x8000, v0\n"
"v_cmp_gt_u32 vcc, .LkLSLOW-.LkLHQ0, v0\n"
"s_and_b64 exec, vcc, vcc\n"
"s_cbranch_execz .LkLPC2\n"
"global_load_dword v253, v0, s[76:77]\n"
".LkLPC2:\n"
"s_mov_b64 exec, -1\n"
"s_waitcnt lgkmcnt(0)\n"
"s_add_u32 s68, s32, 0x1000000\n"
"s_addc_u32 s69, s33, 0\n"
"s_mov_b32 s70, 0x200000\n"
"s_mov_b32 s71, 0x20000\n"
"s_barrier\n"
"s_cmp_lg_u32 s38, 0\n"
"s_cbranch_scc1 .LkLCP0\n"
"v_and_b32 v4, 7, v255\n"
"v_lshrrev_b32 v5, 3, v255\n"
"v_lshlrev_b32 v6, 10, v5\n"
"v_lshl_add_u32 v131, v4, 4, v6\n"
"v_lshlrev_b32 v132, 4, v255\n"
"s_lshl_b32 s44, s43, 14\n"
"s_add_u32 s44, s44, 384\n"
"s_lshl_b32 s48, s43, 11\n"
"s_add_u32 s48, s48, 0xd00000\n"
"s_lshl3_add_u32 s80, s43, 131072\n"
"s_movk_i32 s81, 0x2000\n"
"v_mov_b32 v128, 0x40000\n"
"v_mov_b32 v130, s80\n"
"s_add_u32 s44, s28, s44\n"
"s_addc_u32 s45, s29, 0\n"
"s_mov_b32 s46, 0x3c80\n"
"s_mov_b32 s47, 0x20000\n"
"buffer_load_dwordx4 v[96:99], v131, s[44:47], 0 offen\n"
"buffer_load_dwordx4 v[100:103], v131, s[44:47], s81 offen\n"
"buffer_load_dwordx2 v[128:129], v128, s[68:71], 0 offen sc1\n"
"s_add_u32 s48, s32, s48\n"
"s_addc_u32 s49, s33, 0\n"
"s_mov_b32 s50, 0x800\n"
"s_mov_b32 s51, 0x20000\n"
".LkLCP0:\n"
"s_setprio 3\n"
"s_lshl_b32 s60, s38, 13\n"
"s_add_u32 s60, s60, s41\n"
"s_lshl_b32 s60, s60, 7\n"
"s_add_u32 s60, s28, s60\n"
"s_addc_u32 s61, s29, 0\n"
"s_mov_b32 s62, 0xffc80\n"
"s_mov_b32 s63, 0x20000\n"
"s_lshl_b32 s29, s38, 17\n"
"s_add_u32 s29, s29, 0xd00000\n"
"s_add_u32 s29, s32, s29\n"
"s_addc_u32 s28, s33, 0\n"
"s_cmp_eq_u32 s41, 3\n"
"s_cselect_b32 s60, s29, s60\n"
"s_cselect_b32 s61, s28, s61\n"
"s_cselect_b32 s62, 0x20000, s62\n"
"s_mul_i32 s26, s41, 0\n"
"s_and_b32 s29, s43, 1\n"
"s_mul_i32 s29, s29, 8192\n"
"s_add_u32 s26, s26, s29\n"
"s_bfe_u32 s29, s43, 0x10001\n"
"s_mul_i32 s29, s29, 8192\n"
"s_add_u32 s26, s26, s29\n"
"s_bfe_u32 s29, s43, 0x10002\n"
"s_mul_i32 s29, s29, 16384\n"
"s_add_u32 s26, s26, s29\n"
"s_and_b32 s27, s40, 0\n"
"s_lshl_b32 s27, s27, 13\n"
"s_and_b32 s28, s40, 0\n"
"s_lshl_b32 s28, s28, 10\n"
"s_lshl2_add_u32 s52, s34, s40\n"
"s_lshl2_add_u32 s52, s52, s38\n"
"s_mul_i32 s52, s52, 98304\n"
"s_add_u32 s52, s32, s52\n"
"s_addc_u32 s53, s33, 0\n"
"s_mov_b32 s54, 0x18000\n"
"s_mov_b32 s55, 0x20000\n"
"s_lshl2_add_u32 s81, s34, s40\n"
"s_mul_i32 s81, s81, 49152\n"
"s_lshl_b32 s82, s41, 7\n"
"s_add_u32 s72, s81, s82\n"
"s_add_u32 s72, s30, s72\n"
"s_addc_u32 s73, s31, 0\n"
"s_mov_b32 s74, 0xbc80\n"
"s_mov_b32 s75, 0x20000\n"
"v_lshrrev_b32 v4, 4, v255\n"
"v_bfe_u32 v5, v255, 1, 3\n"
"v_xor_b32 v6, v4, v5\n"
"v_and_b32 v7, 15, v255\n"
"v_lshlrev_b32 v8, 7, v7\n"
"v_lshl_add_u32 v9, v6, 4, v8\n"
"v_mov_b32 v10, s38\n"
"v_mul_u32_u24 v11, 0x1800, v10\n"
"v_add_u32 v12, v9, v11\n"
"v_add_u32 v204, 0x14800, v12\n"
"v_add_u32 v13, 4, v4\n"
"v_xor_b32 v14, v13, v5\n"
"v_lshl_add_u32 v15, v14, 4, v8\n"
"v_add_u32 v16, v15, v11\n"
"v_add_u32 v205, 0x14800, v16\n"
"v_and_b32 v17, 1, v255\n"
"v_and_b32 v18, 3, v255\n"
"v_bfe_u32 v19, v255, 2, 2\n"
"v_and_b32 v20, 1, v4\n"
"v_lshl_add_u32 v21, v19, 2, v20\n"
"v_xor_b32 v22, v18, v21\n"
"v_lshlrev_b32 v23, 11, v4\n"
"v_lshl_add_u32 v24, v19, 8, v23\n"
"v_add_u32 v25, 0x800, v24\n"
"v_lshl_add_u32 v26, v22, 4, v25\n"
"v_lshl_add_u32 v196, v17, 3, v26\n"
"v_xor_b32 v27, 1, v17\n"
"v_lshl_add_u32 v197, v27, 3, v26\n"
"v_add_u32 v28, 4, v18\n"
"v_xor_b32 v29, v28, v21\n"
"v_lshl_add_u32 v30, v29, 4, v25\n"
"v_lshl_add_u32 v198, v17, 3, v30\n"
"v_lshl_add_u32 v199, v27, 3, v30\n"
"v_add_u32 v31, 8, v18\n"
"v_xor_b32 v32, v31, v21\n"
"v_lshl_add_u32 v33, v32, 4, v25\n"
"v_lshl_add_u32 v200, v17, 3, v33\n"
"v_lshl_add_u32 v201, v27, 3, v33\n"
"v_add_u32 v34, 12, v18\n"
"v_xor_b32 v35, v34, v21\n"
"v_lshl_add_u32 v36, v35, 4, v25\n"
"v_lshl_add_u32 v202, v17, 3, v36\n"
"v_lshl_add_u32 v203, v27, 3, v36\n"
"v_lshlrev_b32 v37, 10, v19\n"
"v_lshl_add_u32 v38, v18, 7, v37\n"
"v_lshl_add_u32 v39, v6, 4, v38\n"
"v_add_u32 v244, 0x800, v39\n"
"v_lshl_add_u32 v40, v14, 4, v38\n"
"v_add_u32 v245, 0x800, v40\n"
"v_bfe_u32 v41, v255, 1, 1\n"
"v_bfe_u32 v42, v255, 3, 1\n"
"v_lshlrev_b32 v43, 2, v20\n"
"v_lshl_add_u32 v44, v42, 1, v43\n"
"v_xor_b32 v45, v41, v44\n"
"v_lshlrev_b32 v46, 10, v4\n"
"v_lshl_add_u32 v47, v19, 7, v46\n"
"v_lshl_add_u32 v48, v17, 3, v47\n"
"v_add_u32 v49, 0x10800, v48\n"
"v_lshl_add_u32 v246, v45, 4, v49\n"
"v_add_u32 v50, 2, v41\n"
"v_xor_b32 v51, v50, v44\n"
"v_lshl_add_u32 v247, v51, 4, v49\n"
"v_add_u32 v52, 4, v41\n"
"v_xor_b32 v53, v52, v44\n"
"v_lshl_add_u32 v248, v53, 4, v49\n"
"v_add_u32 v54, 6, v41\n"
"v_xor_b32 v55, v54, v44\n"
"v_lshl_add_u32 v249, v55, 4, v49\n"
"v_mul_u32_u24 v56, 0x104, v18\n"
"v_add_u32 v252, 0x400, v56\n"
"v_mov_b32 v143, 0x9e3779b1\n"
"v_add_u32 v144, 77, v255\n"
"s_mov_b32 s78, 0x80ff80ff\n"
"v_mov_b32 v160, 0x3e003e00\n"
"v_mul_lo_u32 v145, v144, v143\n"
"v_mul_lo_u32 v146, v145, v143\n"
"v_mul_lo_u32 v147, v146, v143\n"
"v_mul_lo_u32 v148, v147, v143\n"
"v_mul_lo_u32 v149, v148, v143\n"
"v_mul_lo_u32 v150, v149, v143\n"
"v_mul_lo_u32 v151, v150, v143\n"
"v_mul_lo_u32 v152, v151, v143\n"
"v_mul_lo_u32 v153, v152, v143\n"
"v_mul_lo_u32 v154, v153, v143\n"
"v_mul_lo_u32 v155, v154, v143\n"
"v_mul_lo_u32 v156, v155, v143\n"
"v_mul_lo_u32 v157, v156, v143\n"
"v_mul_lo_u32 v158, v157, v143\n"
"v_mul_lo_u32 v159, v158, v143\n"
"v_mul_lo_u32 v144, v159, v143\n"
"v_and_or_b32 v144, v144, s78, v160\n"
"v_and_or_b32 v145, v145, s78, v160\n"
"v_and_or_b32 v146, v146, s78, v160\n"
"v_and_or_b32 v147, v147, s78, v160\n"
"v_and_or_b32 v148, v148, s78, v160\n"
"v_and_or_b32 v149, v149, s78, v160\n"
"v_and_or_b32 v150, v150, s78, v160\n"
"v_and_or_b32 v151, v151, s78, v160\n"
"v_and_or_b32 v152, v152, s78, v160\n"
"v_and_or_b32 v153, v153, s78, v160\n"
"v_and_or_b32 v154, v154, s78, v160\n"
"v_and_or_b32 v155, v155, s78, v160\n"
"v_and_or_b32 v156, v156, s78, v160\n"
"v_and_or_b32 v157, v157, s78, v160\n"
"v_and_or_b32 v158, v158, s78, v160\n"
"v_and_or_b32 v159, v159, s78, v160\n"
"s_mov_b32 vcc_lo, 0x11111111\n"
"s_mov_b32 vcc_hi, 0x11111111\n"
"v_mov_b32 v171, 0x3f803f80\n"
"v_cndmask_b32 v168, 0, v171, vcc\n"
"v_cndmask_b32 v169, 0, v171, vcc\n"
"v_cndmask_b32 v170, 0, v171, vcc\n"
"v_cndmask_b32 v171, 0, v171, vcc\n"
"s_mov_b32 s78, 0x3e0293ee\n"
"s_mov_b32 s79, 0x3e0293ee\n"
"s_lshl2_add_u32 s80, s36, s38\n"
"s_lshl_b32 s80, s80, 5\n"
"s_add_u32 s80, s80, s37\n"
"s_mul_i32 s64, s80, 12288\n"
"s_lshl_b32 s80, s80, 7\n"
"s_add_u32 s64, s32, s64\n"
"s_addc_u32 s65, s33, 0\n"
"s_mov_b32 s66, 0x3000\n"
"s_mov_b32 s67, 0x20000\n"
"s_mov_b32 s81, 4096\n"
"s_mov_b32 s82, 8192\n"
"s_cmp_eq_u32 s38, 0\n"
"s_cselect_b32 s76, 1, 0\n"
"s_mov_b32 s77, 0\n"
"s_cbranch_scc0 .LkLCP1\n"
"v_mov_b32 v131, 163824\n"
"s_waitcnt vmcnt(0)\n"
"buffer_store_dwordx4 v[96:99], v132, s[48:51], 0 offen offset:0 sc1\n"
"buffer_store_dwordx4 v[100:103], v132, s[48:51], 0 offen offset:1024 sc1\n"
"v_add_co_u32 v128, vcc, 0x9e3779b9, v128\n"
"s_nop 1\n"
"v_addc_co_u32 v129, vcc, 0, v129, vcc\n"
"ds_write_b64 v131, v[128:129]\n"
"v_mov_b32 v138, s84\n"
"ds_write_b32 v131, v138 offset:8\n"
"v_mov_b32 v136, v128\n"
"v_mov_b32 v137, v129\n"
"v_mov_b32 v139, s84\n"
"v_mov_b32 v140, 0x40080\n"
"s_cmp_lg_u32 s43, 0\n"
"s_cbranch_scc1 .LkLCP1\n"
"s_mov_b64 exec, 1\n"
"buffer_store_dwordx4 v[136:139], v140, s[68:71], 0 offen sc1\n"
"s_mov_b64 exec, -1\n"
".LkLCP1:\n"
"v_add_u32 v250, 1, v250\n"
"ds_read_b32 v251, v252\n"
"s_waitcnt lgkmcnt(0)\n"
"v_cmp_gt_i32 vcc, v250, v251\n"
"s_cbranch_vccnz .LkLS0\n"
".LkLR0:\n"
"ds_read_b64_tr_b16 v[236:237], v196 offset:57344\n"
"ds_read_b64_tr_b16 v[238:239], v196 offset:58368\n"
"ds_read_b128 v[172:175], v204 offset:24576\n"
"ds_read_b128 v[176:179], v204 offset:26624\n"
"ds_read_b128 v[180:183], v204 offset:28672\n"
"ds_read_b64_tr_b16 v[240:241], v197 offset:57344\n"
"ds_read_b64_tr_b16 v[242:243], v197 offset:58368\n"
"ds_read_b64_tr_b16 v[220:221], v198 offset:57344\n"
"ds_read_b64_tr_b16 v[222:223], v198 offset:58368\n"
"ds_read_b64_tr_b16 v[224:225], v199 offset:57344\n"
"ds_read_b64_tr_b16 v[226:227], v199 offset:58368\n"
"s_waitcnt lgkmcnt(4)\n"
"v_mfma_f32_16x16x32_bf16 v[0:3], v[236:239], v[172:175], 0\n"
"ds_read_b64_tr_b16 v[228:229], v200 offset:57344\n"
"ds_read_b64_tr_b16 v[230:231], v200 offset:58368\n"
"v_mfma_f32_16x16x32_bf16 v[32:35], v[236:239], v[176:179], 0\n"
"v_mfma_f32_16x16x32_bf16 v[64:67], v[236:239], v[180:183], 0\n"
"v_mfma_f32_16x16x32_bf16 v[68:71], v[240:243], v[180:183], 0\n"
"ds_read_b64_tr_b16 v[232:233], v201 offset:57344\n"
"ds_read_b64_tr_b16 v[234:235], v201 offset:58368\n"
"v_mfma_f32_16x16x32_bf16 v[36:39], v[240:243], v[176:179], 0\n"
"ds_read_b32 v251, v252\n"
"v_mfma_f32_16x16x32_bf16 v[4:7], v[240:243], v[172:175], 0\n"
"s_waitcnt lgkmcnt(5)\n"
"v_mfma_f32_16x16x32_bf16 v[8:11], v[220:223], v[172:175], 0\n"
"ds_read_b64_tr_b16 v[236:237], v202 offset:57344\n"
"ds_read_b64_tr_b16 v[238:239], v202 offset:58368\n"
"v_mfma_f32_16x16x32_bf16 v[40:43], v[220:223], v[176:179], 0\n"
"v_add_u32 v250, 1, v250\n"
"v_mfma_f32_16x16x32_bf16 v[72:75], v[220:223], v[180:183], 0\n"
"s_sleep 4\n"
"v_mfma_f32_16x16x32_bf16 v[76:79], v[224:227], v[180:183], 0\n"
"ds_read_b64_tr_b16 v[240:241], v203 offset:57344\n"
"ds_read_b64_tr_b16 v[242:243], v203 offset:58368\n"
"v_mfma_f32_16x16x32_bf16 v[44:47], v[224:227], v[176:179], 0\n"
"v_mfma_f32_16x16x32_bf16 v[12:15], v[224:227], v[172:175], 0\n"
"s_waitcnt lgkmcnt(4)\n"
"v_cmp_gt_i32 vcc, v250, v251\n"
"v_mfma_f32_16x16x32_bf16 v[16:19], v[228:231], v[172:175], 0\n"
"s_cbranch_vccnz .LkLS1\n"
".LkLR1:\n"
"ds_read_b64_tr_b16 v[220:221], v196 offset:24576\n"
"v_mfma_f32_16x16x32_bf16 v[48:51], v[228:231], v[176:179], 0\n"
"ds_read_b64_tr_b16 v[222:223], v196 offset:25600\n"
"ds_read_b128 v[184:187], v205 offset:24576\n"
"v_mfma_f32_16x16x32_bf16 v[80:83], v[228:231], v[180:183], 0\n"
"ds_read_b128 v[188:191], v205 offset:26624\n"
"ds_read_b128 v[192:195], v205 offset:28672\n"
"v_mfma_f32_16x16x32_bf16 v[84:87], v[232:235], v[180:183], 0\n"
"ds_read_b64_tr_b16 v[224:225], v197 offset:24576\n"
"ds_read_b64_tr_b16 v[226:227], v197 offset:25600\n"
"v_mfma_f32_16x16x32_bf16 v[52:55], v[232:235], v[176:179], 0\n"
"v_mfma_f32_16x16x32_bf16 v[20:23], v[232:235], v[172:175], 0\n"
"s_waitcnt lgkmcnt(7)\n"
"v_mfma_f32_16x16x32_bf16 v[24:27], v[236:239], v[172:175], 0\n"
"ds_read_b64_tr_b16 v[228:229], v198 offset:24576\n"
"ds_read_b64_tr_b16 v[230:231], v198 offset:25600\n"
"v_mfma_f32_16x16x32_bf16 v[56:59], v[236:239], v[176:179], 0\n"
"v_mfma_f32_16x16x32_bf16 v[88:91], v[236:239], v[180:183], 0\n"
"v_mfma_f32_16x16x32_bf16 v[92:95], v[240:243], v[180:183], 0\n"
"ds_read_b64_tr_b16 v[232:233], v199 offset:24576\n"
"ds_read_b64_tr_b16 v[234:235], v199 offset:25600\n"
"v_mfma_f32_16x16x32_bf16 v[60:63], v[240:243], v[176:179], 0\n"
"v_mfma_f32_16x16x32_bf16 v[28:31], v[240:243], v[172:175], 0\n"
"s_waitcnt lgkmcnt(4)\n"
"v_mfma_f32_16x16x32_bf16 v[0:3], v[220:223], v[184:187], v[0:3]\n"
"ds_read_b64_tr_b16 v[236:237], v200 offset:24576\n"
"ds_read_b64_tr_b16 v[238:239], v200 offset:25600\n"
"v_mfma_f32_16x16x32_bf16 v[32:35], v[220:223], v[188:191], v[32:35]\n"
"v_mfma_f32_16x16x32_bf16 v[64:67], v[220:223], v[192:195], v[64:67]\n"
"v_mfma_f32_16x16x32_bf16 v[68:71], v[224:227], v[192:195], v[68:71]\n"
"ds_read_b64_tr_b16 v[240:241], v201 offset:24576\n"
"ds_read_b64_tr_b16 v[242:243], v201 offset:25600\n"
"v_mfma_f32_16x16x32_bf16 v[36:39], v[224:227], v[188:191], v[36:39]\n"
"ds_read_b32 v251, v252\n"
"v_mfma_f32_16x16x32_bf16 v[4:7], v[224:227], v[184:187], v[4:7]\n"
"s_waitcnt lgkmcnt(5)\n"
"v_mfma_f32_16x16x32_bf16 v[8:11], v[228:231], v[184:187], v[8:11]\n"
"ds_read_b64_tr_b16 v[220:221], v202 offset:24576\n"
"ds_read_b64_tr_b16 v[222:223], v202 offset:25600\n"
"v_mfma_f32_16x16x32_bf16 v[40:43], v[228:231], v[188:191], v[40:43]\n"
"v_add_u32 v250, 1, v250\n"
"v_mfma_f32_16x16x32_bf16 v[72:75], v[228:231], v[192:195], v[72:75]\n"
"s_sleep 3\n"
"v_mfma_f32_16x16x32_bf16 v[76:79], v[232:235], v[192:195], v[76:79]\n"
"ds_read_b64_tr_b16 v[224:225], v203 offset:24576\n"
"ds_read_b64_tr_b16 v[226:227], v203 offset:25600\n"
"v_mfma_f32_16x16x32_bf16 v[44:47], v[232:235], v[188:191], v[44:47]\n"
"v_mfma_f32_16x16x32_bf16 v[12:15], v[232:235], v[184:187], v[12:15]\n"
"s_waitcnt lgkmcnt(4)\n"
"v_cmp_gt_i32 vcc, v250, v251\n"
"v_mfma_f32_16x16x32_bf16 v[16:19], v[236:239], v[184:187], v[16:19]\n"
"s_cbranch_vccnz .LkLS2\n"
".LkLR2:\n"
"ds_read_b64_tr_b16 v[228:229], v196 offset:32768\n"
"v_mfma_f32_16x16x32_bf16 v[48:51], v[236:239], v[188:191], v[48:51]\n"
"ds_read_b64_tr_b16 v[230:231], v196 offset:33792\n"
"ds_read_b128 v[172:175], v204 offset:49152\n"
"v_mfma_f32_16x16x32_bf16 v[80:83], v[236:239], v[192:195], v[80:83]\n"
"ds_read_b128 v[176:179], v204 offset:51200\n"
"ds_read_b128 v[180:183], v204 offset:53248\n"
"v_mfma_f32_16x16x32_bf16 v[84:87], v[240:243], v[192:195], v[84:87]\n"
"ds_read_b64_tr_b16 v[232:233], v197 offset:32768\n"
"ds_read_b64_tr_b16 v[234:235], v197 offset:33792\n"
"v_mfma_f32_16x16x32_bf16 v[52:55], v[240:243], v[188:191], v[52:55]\n"
"v_mfma_f32_16x16x32_bf16 v[20:23], v[240:243], v[184:187], v[20:23]\n"
"s_waitcnt lgkmcnt(7)\n"
"s_barrier\n"
"v_mfma_f32_16x16x32_bf16 v[24:27], v[220:223], v[184:187], v[24:27]\n"
"ds_read_b64_tr_b16 v[236:237], v198 offset:32768\n"
"ds_read_b64_tr_b16 v[238:239], v198 offset:33792\n"
"v_mfma_f32_16x16x32_bf16 v[56:59], v[220:223], v[188:191], v[56:59]\n"
"v_mfma_f32_16x16x32_bf16 v[88:91], v[220:223], v[192:195], v[88:91]\n"
"v_mfma_f32_16x16x32_bf16 v[92:95], v[224:227], v[192:195], v[92:95]\n"
"ds_read_b64_tr_b16 v[240:241], v199 offset:32768\n"
"ds_read_b64_tr_b16 v[242:243], v199 offset:33792\n"
"v_mfma_f32_16x16x32_bf16 v[60:63], v[224:227], v[188:191], v[60:63]\n"
"v_mfma_f32_16x16x32_bf16 v[28:31], v[224:227], v[184:187], v[28:31]\n"
"s_waitcnt lgkmcnt(4)\n"
"v_mfma_f32_16x16x32_bf16 v[0:3], v[228:231], v[172:175], v[0:3]\n"
"ds_read_b64_tr_b16 v[220:221], v200 offset:32768\n"
"ds_read_b64_tr_b16 v[222:223], v200 offset:33792\n"
"v_mfma_f32_16x16x32_bf16 v[32:35], v[228:231], v[176:179], v[32:35]\n"
"v_mfma_f32_16x16x32_bf16 v[64:67], v[228:231], v[180:183], v[64:67]\n"
"v_mfma_f32_16x16x32_bf16 v[68:71], v[232:235], v[180:183], v[68:71]\n"
"ds_read_b64_tr_b16 v[224:225], v201 offset:32768\n"
"ds_read_b64_tr_b16 v[226:227], v201 offset:33792\n"
"v_mfma_f32_16x16x32_bf16 v[36:39], v[232:235], v[176:179], v[36:39]\n"
"ds_read_b32 v251, v252\n"
"v_mfma_f32_16x16x32_bf16 v[4:7], v[232:235], v[172:175], v[4:7]\n"
"s_waitcnt lgkmcnt(5)\n"
"v_mfma_f32_16x16x32_bf16 v[8:11], v[236:239], v[172:175], v[8:11]\n"
"ds_read_b64_tr_b16 v[228:229], v202 offset:32768\n"
"ds_read_b64_tr_b16 v[230:231], v202 offset:33792\n"
"v_mfma_f32_16x16x32_bf16 v[40:43], v[236:239], v[176:179], v[40:43]\n"
"v_add_u32 v250, 1, v250\n"
"v_mfma_f32_16x16x32_bf16 v[72:75], v[236:239], v[180:183], v[72:75]\n"
"s_sleep 2\n"
"v_mfma_f32_16x16x32_bf16 v[76:79], v[240:243], v[180:183], v[76:79]\n"
"ds_read_b64_tr_b16 v[232:233], v203 offset:32768\n"
"ds_read_b64_tr_b16 v[234:235], v203 offset:33792\n"
"v_mfma_f32_16x16x32_bf16 v[44:47], v[240:243], v[176:179], v[44:47]\n"
"v_mfma_f32_16x16x32_bf16 v[12:15], v[240:243], v[172:175], v[12:15]\n"
"s_waitcnt lgkmcnt(4)\n"
"v_cmp_gt_i32 vcc, v250, v251\n"
"v_mfma_f32_16x16x32_bf16 v[16:19], v[220:223], v[172:175], v[16:19]\n"
"s_cbranch_vccnz .LkLS3\n"
".LkLR3:\n"
"ds_read_b64_tr_b16 v[236:237], v196 offset:40960\n"
"v_mfma_f32_16x16x32_bf16 v[48:51], v[220:223], v[176:179], v[48:51]\n"
"ds_read_b64_tr_b16 v[238:239], v196 offset:41984\n"
"ds_read_b128 v[184:187], v205 offset:49152\n"
"v_mfma_f32_16x16x32_bf16 v[80:83], v[220:223], v[180:183], v[80:83]\n"
"ds_read_b128 v[188:191], v205 offset:51200\n"
"ds_read_b128 v[192:195], v205 offset:53248\n"
"v_mfma_f32_16x16x32_bf16 v[84:87], v[224:227], v[180:183], v[84:87]\n"
"ds_read_b64_tr_b16 v[240:241], v197 offset:40960\n"
"ds_read_b64_tr_b16 v[242:243], v197 offset:41984\n"
"v_mfma_f32_16x16x32_bf16 v[52:55], v[224:227], v[176:179], v[52:55]\n"
"v_mfma_f32_16x16x32_bf16 v[20:23], v[224:227], v[172:175], v[20:23]\n"
"s_waitcnt lgkmcnt(7)\n"
"v_mfma_f32_16x16x32_bf16 v[24:27], v[228:231], v[172:175], v[24:27]\n"
"ds_read_b64_tr_b16 v[220:221], v198 offset:40960\n"
"ds_read_b64_tr_b16 v[222:223], v198 offset:41984\n"
"v_mfma_f32_16x16x32_bf16 v[56:59], v[228:231], v[176:179], v[56:59]\n"
"v_mfma_f32_16x16x32_bf16 v[88:91], v[228:231], v[180:183], v[88:91]\n"
"v_mfma_f32_16x16x32_bf16 v[92:95], v[232:235], v[180:183], v[92:95]\n"
"ds_read_b64_tr_b16 v[224:225], v199 offset:40960\n"
"ds_read_b64_tr_b16 v[226:227], v199 offset:41984\n"
"v_mfma_f32_16x16x32_bf16 v[60:63], v[232:235], v[176:179], v[60:63]\n"
"v_mfma_f32_16x16x32_bf16 v[28:31], v[232:235], v[172:175], v[28:31]\n"
"s_waitcnt lgkmcnt(4)\n"
"v_mfma_f32_16x16x32_bf16 v[0:3], v[236:239], v[184:187], v[0:3]\n"
"ds_read_b64_tr_b16 v[228:229], v200 offset:40960\n"
"ds_read_b64_tr_b16 v[230:231], v200 offset:41984\n"
"v_mfma_f32_16x16x32_bf16 v[32:35], v[236:239], v[188:191], v[32:35]\n"
"v_mfma_f32_16x16x32_bf16 v[64:67], v[236:239], v[192:195], v[64:67]\n"
"v_mfma_f32_16x16x32_bf16 v[68:71], v[240:243], v[192:195], v[68:71]\n"
"ds_read_b64_tr_b16 v[232:233], v201 offset:40960\n"
"ds_read_b64_tr_b16 v[234:235], v201 offset:41984\n"
"v_mfma_f32_16x16x32_bf16 v[36:39], v[240:243], v[188:191], v[36:39]\n"
"ds_read_b32 v251, v252\n"
"v_mfma_f32_16x16x32_bf16 v[4:7], v[240:243], v[184:187], v[4:7]\n"
"s_waitcnt lgkmcnt(5)\n"
"v_mfma_f32_16x16x32_bf16 v[8:11], v[220:223], v[184:187], v[8:11]\n"
"ds_read_b64_tr_b16 v[236:237], v202 offset:40960\n"
"ds_read_b64_tr_b16 v[238:239], v202 offset:41984\n"
"v_mfma_f32_16x16x32_bf16 v[40:43], v[220:223], v[188:191], v[40:43]\n"
"v_add_u32 v250, 1, v250\n"
"v_mfma_f32_16x16x32_bf16 v[72:75], v[220:223], v[192:195], v[72:75]\n"
"s_sleep 1\n"
"v_mfma_f32_16x16x32_bf16 v[76:79], v[224:227], v[192:195], v[76:79]\n"
"ds_read_b64_tr_b16 v[240:241], v203 offset:40960\n"
"ds_read_b64_tr_b16 v[242:243], v203 offset:41984\n"
"v_mfma_f32_16x16x32_bf16 v[44:47], v[224:227], v[188:191], v[44:47]\n"
"v_mfma_f32_16x16x32_bf16 v[12:15], v[224:227], v[184:187], v[12:15]\n"
"s_waitcnt lgkmcnt(4)\n"
"v_cmp_gt_i32 vcc, v250, v251\n"
"v_mfma_f32_16x16x32_bf16 v[16:19], v[228:231], v[184:187], v[16:19]\n"
"s_cbranch_vccnz .LkLS4\n"
".LkLR4:\n"
"ds_read_b64_tr_b16 v[220:221], v196 offset:49152\n"
"v_mfma_f32_16x16x32_bf16 v[48:51], v[228:231], v[188:191], v[48:51]\n"
"ds_read_b64_tr_b16 v[222:223], v196 offset:50176\n"
"ds_read_b128 v[172:175], v204 offset:0\n"
"v_mfma_f32_16x16x32_bf16 v[80:83], v[228:231], v[192:195], v[80:83]\n"
"ds_read_b128 v[176:179], v204 offset:2048\n"
"ds_read_b128 v[180:183], v204 offset:4096\n"
"v_mfma_f32_16x16x32_bf16 v[84:87], v[232:235], v[192:195], v[84:87]\n"
"ds_read_b64_tr_b16 v[224:225], v197 offset:49152\n"
"ds_read_b64_tr_b16 v[226:227], v197 offset:50176\n"
"v_mfma_f32_16x16x32_bf16 v[52:55], v[232:235], v[188:191], v[52:55]\n"
"v_mfma_f32_16x16x32_bf16 v[20:23], v[232:235], v[184:187], v[20:23]\n"
"s_waitcnt lgkmcnt(7)\n"
"s_barrier\n"
"v_mfma_f32_16x16x32_bf16 v[24:27], v[236:239], v[184:187], v[24:27]\n"
"ds_read_b64_tr_b16 v[228:229], v198 offset:49152\n"
"ds_read_b64_tr_b16 v[230:231], v198 offset:50176\n"
"v_mfma_f32_16x16x32_bf16 v[56:59], v[236:239], v[188:191], v[56:59]\n"
"v_mfma_f32_16x16x32_bf16 v[88:91], v[236:239], v[192:195], v[88:91]\n"
"v_mfma_f32_16x16x32_bf16 v[92:95], v[240:243], v[192:195], v[92:95]\n"
"ds_read_b64_tr_b16 v[232:233], v199 offset:49152\n"
"ds_read_b64_tr_b16 v[234:235], v199 offset:50176\n"
"v_mfma_f32_16x16x32_bf16 v[60:63], v[240:243], v[188:191], v[60:63]\n"
"v_mfma_f32_16x16x32_bf16 v[28:31], v[240:243], v[184:187], v[28:31]\n"
"s_waitcnt lgkmcnt(4)\n"
"v_mfma_f32_16x16x32_bf16 v[0:3], v[220:223], v[172:175], v[0:3]\n"
"ds_read_b64_tr_b16 v[236:237], v200 offset:49152\n"
"ds_read_b64_tr_b16 v[238:239], v200 offset:50176\n"
"v_mfma_f32_16x16x32_bf16 v[32:35], v[220:223], v[176:179], v[32:35]\n"
"v_mfma_f32_16x16x32_bf16 v[64:67], v[220:223], v[180:183], v[64:67]\n"
"v_mfma_f32_16x16x32_bf16 v[68:71], v[224:227], v[180:183], v[68:71]\n"
"ds_read_b64_tr_b16 v[240:241], v201 offset:49152\n"
"ds_read_b64_tr_b16 v[242:243], v201 offset:50176\n"
"v_mfma_f32_16x16x32_bf16 v[36:39], v[224:227], v[176:179], v[36:39]\n"
"ds_read_b32 v251, v252\n"
"v_mfma_f32_16x16x32_bf16 v[4:7], v[224:227], v[172:175], v[4:7]\n"
"s_waitcnt lgkmcnt(5)\n"
"v_mfma_f32_16x16x32_bf16 v[8:11], v[228:231], v[172:175], v[8:11]\n"
"ds_read_b64_tr_b16 v[220:221], v202 offset:49152\n"
"ds_read_b64_tr_b16 v[222:223], v202 offset:50176\n"
"v_mfma_f32_16x16x32_bf16 v[40:43], v[228:231], v[176:179], v[40:43]\n"
"v_add_u32 v250, 1, v250\n"
"v_mfma_f32_16x16x32_bf16 v[72:75], v[228:231], v[180:183], v[72:75]\n"
"v_mfma_f32_16x16x32_bf16 v[76:79], v[232:235], v[180:183], v[76:79]\n"
"ds_read_b64_tr_b16 v[224:225], v203 offset:49152\n"
"ds_read_b64_tr_b16 v[226:227], v203 offset:50176\n"
"v_mfma_f32_16x16x32_bf16 v[44:47], v[232:235], v[176:179], v[44:47]\n"
"v_mfma_f32_16x16x32_bf16 v[12:15], v[232:235], v[172:175], v[12:15]\n"
"s_waitcnt lgkmcnt(4)\n"
"v_cmp_gt_i32 vcc, v250, v251\n"
"v_mfma_f32_16x16x32_bf16 v[16:19], v[236:239], v[172:175], v[16:19]\n"
"s_cbranch_vccnz .LkLS5\n"
".LkLR5:\n"
"ds_read_b64_tr_b16 v[228:229], v196 offset:57344\n"
"v_mfma_f32_16x16x32_bf16 v[48:51], v[236:239], v[176:179], v[48:51]\n"
"ds_read_b64_tr_b16 v[230:231], v196 offset:58368\n"
"ds_read_b128 v[184:187], v205 offset:0\n"
"v_mfma_f32_16x16x32_bf16 v[80:83], v[236:239], v[180:183], v[80:83]\n"
"ds_read_b128 v[188:191], v205 offset:2048\n"
"ds_read_b128 v[192:195], v205 offset:4096\n"
"v_mfma_f32_16x16x32_bf16 v[84:87], v[240:243], v[180:183], v[84:87]\n"
"ds_read_b64_tr_b16 v[232:233], v197 offset:57344\n"
"ds_read_b64_tr_b16 v[234:235], v197 offset:58368\n"
"v_mfma_f32_16x16x32_bf16 v[52:55], v[240:243], v[176:179], v[52:55]\n"
"v_mfma_f32_16x16x32_bf16 v[20:23], v[240:243], v[172:175], v[20:23]\n"
"s_waitcnt lgkmcnt(7)\n"
"v_mfma_f32_16x16x32_bf16 v[24:27], v[220:223], v[172:175], v[24:27]\n"
"ds_read_b64_tr_b16 v[236:237], v198 offset:57344\n"
"ds_read_b64_tr_b16 v[238:239], v198 offset:58368\n"
"v_mfma_f32_16x16x32_bf16 v[56:59], v[220:223], v[176:179], v[56:59]\n"
"v_mfma_f32_16x16x32_bf16 v[88:91], v[220:223], v[180:183], v[88:91]\n"
"v_mfma_f32_16x16x32_bf16 v[92:95], v[224:227], v[180:183], v[92:95]\n"
"ds_read_b64_tr_b16 v[240:241], v199 offset:57344\n"
"ds_read_b64_tr_b16 v[242:243], v199 offset:58368\n"
"v_mfma_f32_16x16x32_bf16 v[60:63], v[224:227], v[176:179], v[60:63]\n"
"v_mfma_f32_16x16x32_bf16 v[28:31], v[224:227], v[172:175], v[28:31]\n"
"s_waitcnt lgkmcnt(4)\n"
"v_mfma_f32_16x16x32_bf16 v[0:3], v[228:231], v[184:187], v[0:3]\n"
"ds_read_b64_tr_b16 v[220:221], v200 offset:57344\n"
"ds_read_b64_tr_b16 v[222:223], v200 offset:58368\n"
"v_mfma_f32_16x16x32_bf16 v[32:35], v[228:231], v[188:191], v[32:35]\n"
"v_mfma_f32_16x16x32_bf16 v[64:67], v[228:231], v[192:195], v[64:67]\n"
"v_mfma_f32_16x16x32_bf16 v[68:71], v[232:235], v[192:195], v[68:71]\n"
"ds_read_b64_tr_b16 v[224:225], v201 offset:57344\n"
"ds_read_b64_tr_b16 v[226:227], v201 offset:58368\n"
"v_mfma_f32_16x16x32_bf16 v[36:39], v[232:235], v[188:191], v[36:39]\n"
"ds_read_b32 v251, v252\n"
"v_mfma_f32_16x16x32_bf16 v[4:7], v[232:235], v[184:187], v[4:7]\n"
"s_waitcnt lgkmcnt(5)\n"
"v_mfma_f32_16x16x32_bf16 v[8:11], v[236:239], v[184:187], v[8:11]\n"
"ds_read_b64_tr_b16 v[228:229], v202 offset:57344\n"
"ds_read_b64_tr_b16 v[230:231], v202 offset:58368\n"
"v_mfma_f32_16x16x32_bf16 v[40:43], v[236:239], v[188:191], v[40:43]\n"
"v_add_u32 v250, 1, v250\n"
"v_mfma_f32_16x16x32_bf16 v[72:75], v[236:239], v[192:195], v[72:75]\n"
"v_mfma_f32_16x16x32_bf16 v[76:79], v[240:243], v[192:195], v[76:79]\n"
"ds_read_b64_tr_b16 v[232:233], v203 offset:57344\n"
"ds_read_b64_tr_b16 v[234:235], v203 offset:58368\n"
"v_mfma_f32_16x16x32_bf16 v[44:47], v[240:243], v[188:191], v[44:47]\n"
"v_mfma_f32_16x16x32_bf16 v[12:15], v[240:243], v[184:187], v[12:15]\n"
"s_waitcnt lgkmcnt(4)\n"
"v_cmp_gt_i32 vcc, v250, v251\n"
"v_mfma_f32_16x16x32_bf16 v[16:19], v[220:223], v[184:187], v[16:19]\n"
"s_cbranch_vccnz .LkLS6\n"
".LkLR6:\n"
"ds_read_b64_tr_b16 v[236:237], v196 offset:24576\n"
"v_mfma_f32_16x16x32_bf16 v[48:51], v[220:223], v[188:191], v[48:51]\n"
"ds_read_b64_tr_b16 v[238:239], v196 offset:25600\n"
"ds_read_b128 v[172:175], v204 offset:24576\n"
"v_mfma_f32_16x16x32_bf16 v[80:83], v[220:223], v[192:195], v[80:83]\n"
"ds_read_b128 v[176:179], v204 offset:26624\n"
"ds_read_b128 v[180:183], v204 offset:28672\n"
"v_mfma_f32_16x16x32_bf16 v[84:87], v[224:227], v[192:195], v[84:87]\n"
"ds_read_b64_tr_b16 v[240:241], v197 offset:24576\n"
"ds_read_b64_tr_b16 v[242:243], v197 offset:25600\n"
"v_mfma_f32_16x16x32_bf16 v[52:55], v[224:227], v[188:191], v[52:55]\n"
"v_mfma_f32_16x16x32_bf16 v[20:23], v[224:227], v[184:187], v[20:23]\n"
"s_waitcnt lgkmcnt(7)\n"
"s_barrier\n"
"v_mfma_f32_16x16x32_bf16 v[24:27], v[228:231], v[184:187], v[24:27]\n"
"ds_read_b64_tr_b16 v[220:221], v198 offset:24576\n"
"ds_read_b64_tr_b16 v[222:223], v198 offset:25600\n"
"v_mfma_f32_16x16x32_bf16 v[56:59], v[228:231], v[188:191], v[56:59]\n"
"v_mfma_f32_16x16x32_bf16 v[88:91], v[228:231], v[192:195], v[88:91]\n"
"v_mfma_f32_16x16x32_bf16 v[92:95], v[232:235], v[192:195], v[92:95]\n"
"ds_read_b64_tr_b16 v[224:225], v199 offset:24576\n"
"ds_read_b64_tr_b16 v[226:227], v199 offset:25600\n"
"v_mfma_f32_16x16x32_bf16 v[60:63], v[232:235], v[188:191], v[60:63]\n"
"v_mfma_f32_16x16x32_bf16 v[28:31], v[232:235], v[184:187], v[28:31]\n"
"s_waitcnt lgkmcnt(4)\n"
"v_mfma_f32_16x16x32_bf16 v[0:3], v[236:239], v[172:175], v[0:3]\n"
"ds_read_b64_tr_b16 v[228:229], v200 offset:24576\n"
"ds_read_b64_tr_b16 v[230:231], v200 offset:25600\n"
"v_mfma_f32_16x16x32_bf16 v[32:35], v[236:239], v[176:179], v[32:35]\n"
"v_mfma_f32_16x16x32_bf16 v[64:67], v[236:239], v[180:183], v[64:67]\n"
"v_mfma_f32_16x16x32_bf16 v[68:71], v[240:243], v[180:183], v[68:71]\n"
"ds_read_b64_tr_b16 v[232:233], v201 offset:24576\n"
"ds_read_b64_tr_b16 v[234:235], v201 offset:25600\n"
"v_mfma_f32_16x16x32_bf16 v[36:39], v[240:243], v[176:179], v[36:39]\n"
"ds_read_b32 v251, v252\n"
"v_mfma_f32_16x16x32_bf16 v[4:7], v[240:243], v[172:175], v[4:7]\n"
"s_waitcnt lgkmcnt(5)\n"
"v_mfma_f32_16x16x32_bf16 v[8:11], v[220:223], v[172:175], v[8:11]\n"
"ds_read_b64_tr_b16 v[236:237], v202 offset:24576\n"
"ds_read_b64_tr_b16 v[238:239], v202 offset:25600\n"
"v_mfma_f32_16x16x32_bf16 v[40:43], v[220:223], v[176:179], v[40:43]\n"
"v_add_u32 v250, 1, v250\n"
"v_mfma_f32_16x16x32_bf16 v[72:75], v[220:223], v[180:183], v[72:75]\n"
"v_mfma_f32_16x16x32_bf16 v[76:79], v[224:227], v[180:183], v[76:79]\n"
"ds_read_b64_tr_b16 v[240:241], v203 offset:24576\n"
"ds_read_b64_tr_b16 v[242:243], v203 offset:25600\n"
"v_mfma_f32_16x16x32_bf16 v[44:47], v[224:227], v[176:179], v[44:47]\n"
"v_mfma_f32_16x16x32_bf16 v[12:15], v[224:227], v[172:175], v[12:15]\n"
"s_waitcnt lgkmcnt(4)\n"
"v_cmp_gt_i32 vcc, v250, v251\n"
"v_mfma_f32_16x16x32_bf16 v[16:19], v[228:231], v[172:175], v[16:19]\n"
"s_cbranch_vccnz .LkLS7\n"
".LkLR7:\n"
"ds_read_b64_tr_b16 v[220:221], v196 offset:32768\n"
"v_mfma_f32_16x16x32_bf16 v[48:51], v[228:231], v[176:179], v[48:51]\n"
"ds_read_b64_tr_b16 v[222:223], v196 offset:33792\n"
"ds_read_b128 v[184:187], v205 offset:24576\n"
"v_mfma_f32_16x16x32_bf16 v[80:83], v[228:231], v[180:183], v[80:83]\n"
"ds_read_b128 v[188:191], v205 offset:26624\n"
"ds_read_b128 v[192:195], v205 offset:28672\n"
"v_mfma_f32_16x16x32_bf16 v[84:87], v[232:235], v[180:183], v[84:87]\n"
"ds_read_b64_tr_b16 v[224:225], v197 offset:32768\n"
"ds_read_b64_tr_b16 v[226:227], v197 offset:33792\n"
"v_mfma_f32_16x16x32_bf16 v[52:55], v[232:235], v[176:179], v[52:55]\n"
"v_mfma_f32_16x16x32_bf16 v[20:23], v[232:235], v[172:175], v[20:23]\n"
"s_waitcnt lgkmcnt(7)\n"
"v_mfma_f32_16x16x32_bf16 v[24:27], v[236:239], v[172:175], v[24:27]\n"
"ds_read_b64_tr_b16 v[228:229], v198 offset:32768\n"
"ds_read_b64_tr_b16 v[230:231], v198 offset:33792\n"
"v_mfma_f32_16x16x32_bf16 v[56:59], v[236:239], v[176:179], v[56:59]\n"
"v_mfma_f32_16x16x32_bf16 v[88:91], v[236:239], v[180:183], v[88:91]\n"
"v_mfma_f32_16x16x32_bf16 v[92:95], v[240:243], v[180:183], v[92:95]\n"
"ds_read_b64_tr_b16 v[232:233], v199 offset:32768\n"
"ds_read_b64_tr_b16 v[234:235], v199 offset:33792\n"
"v_mfma_f32_16x16x32_bf16 v[60:63], v[240:243], v[176:179], v[60:63]\n"
"v_mfma_f32_16x16x32_bf16 v[28:31], v[240:243], v[172:175], v[28:31]\n"
"s_waitcnt lgkmcnt(4)\n"
"v_mfma_f32_16x16x32_bf16 v[0:3], v[220:223], v[184:187], v[0:3]\n"
"ds_read_b64_tr_b16 v[236:237], v200 offset:32768\n"
"ds_read_b64_tr_b16 v[238:239], v200 offset:33792\n"
"v_mfma_f32_16x16x32_bf16 v[32:35], v[220:223], v[188:191], v[32:35]\n"
"v_mfma_f32_16x16x32_bf16 v[64:67], v[220:223], v[192:195], v[64:67]\n"
"v_mfma_f32_16x16x32_bf16 v[68:71], v[224:227], v[192:195], v[68:71]\n"
"ds_read_b64_tr_b16 v[240:241], v201 offset:32768\n"
"ds_read_b64_tr_b16 v[242:243], v201 offset:33792\n"
"v_mfma_f32_16x16x32_bf16 v[36:39], v[224:227], v[188:191], v[36:39]\n"
"ds_read_b32 v251, v252\n"
"v_mfma_f32_16x16x32_bf16 v[4:7], v[224:227], v[184:187], v[4:7]\n"
"s_waitcnt lgkmcnt(5)\n"
"v_mfma_f32_16x16x32_bf16 v[8:11], v[228:231], v[184:187], v[8:11]\n"
"ds_read_b64_tr_b16 v[220:221], v202 offset:32768\n"
"ds_read_b64_tr_b16 v[222:223], v202 offset:33792\n"
"v_mfma_f32_16x16x32_bf16 v[40:43], v[228:231], v[188:191], v[40:43]\n"
"v_add_u32 v250, 1, v250\n"
"v_mfma_f32_16x16x32_bf16 v[72:75], v[228:231], v[192:195], v[72:75]\n"
"v_mfma_f32_16x16x32_bf16 v[76:79], v[232:235], v[192:195], v[76:79]\n"
"ds_read_b64_tr_b16 v[224:225], v203 offset:32768\n"
"ds_read_b64_tr_b16 v[226:227], v203 offset:33792\n"
"v_mfma_f32_16x16x32_bf16 v[44:47], v[232:235], v[188:191], v[44:47]\n"
"v_mfma_f32_16x16x32_bf16 v[12:15], v[232:235], v[184:187], v[12:15]\n"
"s_waitcnt lgkmcnt(4)\n"
"v_cmp_gt_i32 vcc, v250, v251\n"
"v_mfma_f32_16x16x32_bf16 v[16:19], v[236:239], v[184:187], v[16:19]\n"
"s_cbranch_vccnz .LkLS8\n"
".LkLR8:\n"
"ds_read_b64_tr_b16 v[228:229], v196 offset:40960\n"
"v_mfma_f32_16x16x32_bf16 v[48:51], v[236:239], v[188:191], v[48:51]\n"
"ds_read_b64_tr_b16 v[230:231], v196 offset:41984\n"
"ds_read_b128 v[172:175], v204 offset:49152\n"
"v_mfma_f32_16x16x32_bf16 v[80:83], v[236:239], v[192:195], v[80:83]\n"
"ds_read_b128 v[176:179], v204 offset:51200\n"
"ds_read_b128 v[180:183], v204 offset:53248\n"
"v_mfma_f32_16x16x32_bf16 v[84:87], v[240:243], v[192:195], v[84:87]\n"
"ds_read_b64_tr_b16 v[232:233], v197 offset:40960\n"
"ds_read_b64_tr_b16 v[234:235], v197 offset:41984\n"
"v_mfma_f32_16x16x32_bf16 v[52:55], v[240:243], v[188:191], v[52:55]\n"
"v_mfma_f32_16x16x32_bf16 v[20:23], v[240:243], v[184:187], v[20:23]\n"
"s_waitcnt lgkmcnt(7)\n"
"s_barrier\n"
"v_mfma_f32_16x16x32_bf16 v[24:27], v[220:223], v[184:187], v[24:27]\n"
"ds_read_b64_tr_b16 v[236:237], v198 offset:40960\n"
"ds_read_b64_tr_b16 v[238:239], v198 offset:41984\n"
"v_mfma_f32_16x16x32_bf16 v[56:59], v[220:223], v[188:191], v[56:59]\n"
"s_mov_b64 exec, s[76:77]\n"
"s_cbranch_execz .LkLCP3\n"
"buffer_load_dwordx3 v[136:138], v140, s[68:71], 0 offen sc1\n"
".LkLCP3:\n"
"s_mov_b64 exec, -1\n"
"v_mfma_f32_16x16x32_bf16 v[88:91], v[220:223], v[192:195], v[88:91]\n"
"v_mfma_f32_16x16x32_bf16 v[92:95], v[224:227], v[192:195], v[92:95]\n"
"ds_read_b64_tr_b16 v[240:241], v199 offset:40960\n"
"ds_read_b64_tr_b16 v[242:243], v199 offset:41984\n"
"v_mfma_f32_16x16x32_bf16 v[60:63], v[224:227], v[188:191], v[60:63]\n"
"v_mfma_f32_16x16x32_bf16 v[28:31], v[224:227], v[184:187], v[28:31]\n"
"s_waitcnt lgkmcnt(4)\n"
"v_mfma_f32_16x16x32_bf16 v[0:3], v[228:231], v[172:175], v[0:3]\n"
"ds_read_b64_tr_b16 v[220:221], v200 offset:40960\n"
"ds_read_b64_tr_b16 v[222:223], v200 offset:41984\n"
"v_mfma_f32_16x16x32_bf16 v[32:35], v[228:231], v[176:179], v[32:35]\n"
"v_mfma_f32_16x16x32_bf16 v[64:67], v[228:231], v[180:183], v[64:67]\n"
"v_mfma_f32_16x16x32_bf16 v[68:71], v[232:235], v[180:183], v[68:71]\n"
"ds_read_b64_tr_b16 v[224:225], v201 offset:40960\n"
"ds_read_b64_tr_b16 v[226:227], v201 offset:41984\n"
"v_mfma_f32_16x16x32_bf16 v[36:39], v[232:235], v[176:179], v[36:39]\n"
"ds_read_b32 v251, v252\n"
"v_mfma_f32_16x16x32_bf16 v[4:7], v[232:235], v[172:175], v[4:7]\n"
"s_waitcnt lgkmcnt(5)\n"
"v_mfma_f32_16x16x32_bf16 v[8:11], v[236:239], v[172:175], v[8:11]\n"
"ds_read_b64_tr_b16 v[228:229], v202 offset:40960\n"
"ds_read_b64_tr_b16 v[230:231], v202 offset:41984\n"
"v_mfma_f32_16x16x32_bf16 v[40:43], v[236:239], v[176:179], v[40:43]\n"
"v_add_u32 v250, 1, v250\n"
"v_mfma_f32_16x16x32_bf16 v[72:75], v[236:239], v[180:183], v[72:75]\n"
"v_mfma_f32_16x16x32_bf16 v[76:79], v[240:243], v[180:183], v[76:79]\n"
"ds_read_b64_tr_b16 v[232:233], v203 offset:40960\n"
"ds_read_b64_tr_b16 v[234:235], v203 offset:41984\n"
"v_mfma_f32_16x16x32_bf16 v[44:47], v[240:243], v[176:179], v[44:47]\n"
"v_mfma_f32_16x16x32_bf16 v[12:15], v[240:243], v[172:175], v[12:15]\n"
"s_waitcnt lgkmcnt(4)\n"
"v_cmp_gt_i32 vcc, v250, v251\n"
"v_mfma_f32_16x16x32_bf16 v[16:19], v[220:223], v[172:175], v[16:19]\n"
"s_cbranch_vccnz .LkLS9\n"
".LkLR9:\n"
"ds_read_b64_tr_b16 v[236:237], v196 offset:49152\n"
"v_mfma_f32_16x16x32_bf16 v[48:51], v[220:223], v[176:179], v[48:51]\n"
"ds_read_b64_tr_b16 v[238:239], v196 offset:50176\n"
"ds_read_b128 v[184:187], v205 offset:49152\n"
"v_mfma_f32_16x16x32_bf16 v[80:83], v[220:223], v[180:183], v[80:83]\n"
"ds_read_b128 v[188:191], v205 offset:51200\n"
"ds_read_b128 v[192:195], v205 offset:53248\n"
"v_mfma_f32_16x16x32_bf16 v[84:87], v[224:227], v[180:183], v[84:87]\n"
"ds_read_b64_tr_b16 v[240:241], v197 offset:49152\n"
"ds_read_b64_tr_b16 v[242:243], v197 offset:50176\n"
"v_mfma_f32_16x16x32_bf16 v[52:55], v[224:227], v[176:179], v[52:55]\n"
"v_mfma_f32_16x16x32_bf16 v[20:23], v[224:227], v[172:175], v[20:23]\n"
"s_waitcnt lgkmcnt(7)\n"
"v_mfma_f32_16x16x32_bf16 v[24:27], v[228:231], v[172:175], v[24:27]\n"
"ds_read_b64_tr_b16 v[220:221], v198 offset:49152\n"
"ds_read_b64_tr_b16 v[222:223], v198 offset:50176\n"
"v_mfma_f32_16x16x32_bf16 v[56:59], v[228:231], v[176:179], v[56:59]\n"
"v_mfma_f32_16x16x32_bf16 v[88:91], v[228:231], v[180:183], v[88:91]\n"
"v_mfma_f32_16x16x32_bf16 v[92:95], v[232:235], v[180:183], v[92:95]\n"
"ds_read_b64_tr_b16 v[224:225], v199 offset:49152\n"
"ds_read_b64_tr_b16 v[226:227], v199 offset:50176\n"
"v_mfma_f32_16x16x32_bf16 v[60:63], v[232:235], v[176:179], v[60:63]\n"
"v_mfma_f32_16x16x32_bf16 v[28:31], v[232:235], v[172:175], v[28:31]\n"
"s_waitcnt lgkmcnt(4)\n"
"v_mfma_f32_16x16x32_bf16 v[0:3], v[236:239], v[184:187], v[0:3]\n"
"ds_read_b64_tr_b16 v[228:229], v200 offset:49152\n"
"ds_read_b64_tr_b16 v[230:231], v200 offset:50176\n"
"v_mfma_f32_16x16x32_bf16 v[32:35], v[236:239], v[188:191], v[32:35]\n"
"v_mfma_f32_16x16x32_bf16 v[64:67], v[236:239], v[192:195], v[64:67]\n"
"v_mfma_f32_16x16x32_bf16 v[68:71], v[240:243], v[192:195], v[68:71]\n"
"ds_read_b64_tr_b16 v[232:233], v201 offset:49152\n"
"ds_read_b64_tr_b16 v[234:235], v201 offset:50176\n"
"v_mfma_f32_16x16x32_bf16 v[36:39], v[240:243], v[188:191], v[36:39]\n"
"ds_read_b32 v251, v252\n"
"v_mfma_f32_16x16x32_bf16 v[4:7], v[240:243], v[184:187], v[4:7]\n"
"s_waitcnt lgkmcnt(5)\n"
"v_mfma_f32_16x16x32_bf16 v[8:11], v[220:223], v[184:187], v[8:11]\n"
"ds_read_b64_tr_b16 v[236:237], v202 offset:49152\n"
"ds_read_b64_tr_b16 v[238:239], v202 offset:50176\n"
"v_mfma_f32_16x16x32_bf16 v[40:43], v[220:223], v[188:191], v[40:43]\n"
"v_add_u32 v250, 1, v250\n"
"v_mfma_f32_16x16x32_bf16 v[72:75], v[220:223], v[192:195], v[72:75]\n"
"v_mfma_f32_16x16x32_bf16 v[76:79], v[224:227], v[192:195], v[76:79]\n"
"ds_read_b64_tr_b16 v[240:241], v203 offset:49152\n"
"ds_read_b64_tr_b16 v[242:243], v203 offset:50176\n"
"v_mfma_f32_16x16x32_bf16 v[44:47], v[224:227], v[188:191], v[44:47]\n"
"v_mfma_f32_16x16x32_bf16 v[12:15], v[224:227], v[184:187], v[12:15]\n"
"s_waitcnt lgkmcnt(4)\n"
"v_cmp_gt_i32 vcc, v250, v251\n"
"v_mfma_f32_16x16x32_bf16 v[16:19], v[228:231], v[184:187], v[16:19]\n"
"s_cbranch_vccnz .LkLS10\n"
".LkLR10:\n"
"ds_read_b64_tr_b16 v[220:221], v196 offset:57344\n"
"v_mfma_f32_16x16x32_bf16 v[48:51], v[228:231], v[188:191], v[48:51]\n"
"ds_read_b64_tr_b16 v[222:223], v196 offset:58368\n"
"ds_read_b128 v[172:175], v204 offset:0\n"
"v_mfma_f32_16x16x32_bf16 v[80:83], v[228:231], v[192:195], v[80:83]\n"
"ds_read_b128 v[176:179], v204 offset:2048\n"
"ds_read_b128 v[180:183], v204 offset:4096\n"
"v_mfma_f32_16x16x32_bf16 v[84:87], v[232:235], v[192:195], v[84:87]\n"
"ds_read_b64_tr_b16 v[224:225], v197 offset:57344\n"
"ds_read_b64_tr_b16 v[226:227], v197 offset:58368\n"
"v_mfma_f32_16x16x32_bf16 v[52:55], v[232:235], v[188:191], v[52:55]\n"
"v_mfma_f32_16x16x32_bf16 v[20:23], v[232:235], v[184:187], v[20:23]\n"
"s_waitcnt lgkmcnt(7)\n"
"s_barrier\n"
"v_mfma_f32_16x16x32_bf16 v[24:27], v[236:239], v[184:187], v[24:27]\n"
"ds_read_b64_tr_b16 v[228:229], v198 offset:57344\n"
"ds_read_b64_tr_b16 v[230:231], v198 offset:58368\n"
"v_mfma_f32_16x16x32_bf16 v[56:59], v[236:239], v[188:191], v[56:59]\n"
"s_waitcnt vmcnt(0)\n"
"s_mov_b64 exec, s[76:77]\n"
"s_cbranch_execz .LkLCP2\n"
"buffer_store_dwordx2 v[128:129], v130, s[68:71], 0 offen sc1\n"
".LkLCP2:\n"
"s_mov_b64 exec, -1\n"
"v_mfma_f32_16x16x32_bf16 v[88:91], v[236:239], v[192:195], v[88:91]\n"
"s_mov_b64 exec, s[76:77]\n"
"s_cbranch_execz .LkLCP4\n"
"v_cmp_eq_u64 vcc, v[128:129], v[136:137]\n"
"s_nop 1\n"
"v_cndmask_b32 v138, v139, v138, vcc\n"
"ds_write_b32 v131, v138 offset:8\n"
".LkLCP4:\n"
"s_mov_b64 exec, -1\n"
"v_mfma_f32_16x16x32_bf16 v[92:95], v[240:243], v[192:195], v[92:95]\n"
"ds_read_b64_tr_b16 v[232:233], v199 offset:57344\n"
"ds_read_b64_tr_b16 v[234:235], v199 offset:58368\n"
"v_mfma_f32_16x16x32_bf16 v[60:63], v[240:243], v[188:191], v[60:63]\n"
"v_mfma_f32_16x16x32_bf16 v[28:31], v[240:243], v[184:187], v[28:31]\n"
"s_waitcnt lgkmcnt(4)\n"
"v_mfma_f32_16x16x32_bf16 v[0:3], v[220:223], v[172:175], v[0:3]\n"
"ds_read_b64_tr_b16 v[236:237], v200 offset:57344\n"
"ds_read_b64_tr_b16 v[238:239], v200 offset:58368\n"
"v_mfma_f32_16x16x32_bf16 v[32:35], v[220:223], v[176:179], v[32:35]\n"
"v_mfma_f32_16x16x32_bf16 v[64:67], v[220:223], v[180:183], v[64:67]\n"
"v_mfma_f32_16x16x32_bf16 v[68:71], v[224:227], v[180:183], v[68:71]\n"
"ds_read_b64_tr_b16 v[240:241], v201 offset:57344\n"
"ds_read_b64_tr_b16 v[242:243], v201 offset:58368\n"
"v_mfma_f32_16x16x32_bf16 v[36:39], v[224:227], v[176:179], v[36:39]\n"
"ds_read_b32 v251, v252\n"
"v_mfma_f32_16x16x32_bf16 v[4:7], v[224:227], v[172:175], v[4:7]\n"
"s_waitcnt lgkmcnt(5)\n"
"v_mfma_f32_16x16x32_bf16 v[8:11], v[228:231], v[172:175], v[8:11]\n"
"ds_read_b64_tr_b16 v[220:221], v202 offset:57344\n"
"ds_read_b64_tr_b16 v[222:223], v202 offset:58368\n"
"v_mfma_f32_16x16x32_bf16 v[40:43], v[228:231], v[176:179], v[40:43]\n"
"v_add_u32 v250, 1, v250\n"
"v_mfma_f32_16x16x32_bf16 v[72:75], v[228:231], v[180:183], v[72:75]\n"
"v_mfma_f32_16x16x32_bf16 v[76:79], v[232:235], v[180:183], v[76:79]\n"
"ds_read_b64_tr_b16 v[224:225], v203 offset:57344\n"
"ds_read_b64_tr_b16 v[226:227], v203 offset:58368\n"
"v_mfma_f32_16x16x32_bf16 v[44:47], v[232:235], v[176:179], v[44:47]\n"
"v_mfma_f32_16x16x32_bf16 v[12:15], v[232:235], v[172:175], v[12:15]\n"
"s_waitcnt lgkmcnt(4)\n"
"v_cmp_gt_i32 vcc, v250, v251\n"
"v_mfma_f32_16x16x32_bf16 v[16:19], v[236:239], v[172:175], v[16:19]\n"
"s_cbranch_vccnz .LkLS11\n"
".LkLR11:\n"
"ds_read_b64_tr_b16 v[228:229], v196 offset:24576\n"
"v_mfma_f32_16x16x32_bf16 v[48:51], v[236:239], v[176:179], v[48:51]\n"
"ds_read_b64_tr_b16 v[230:231], v196 offset:25600\n"
"ds_read_b128 v[184:187], v205 offset:0\n"
"v_mfma_f32_16x16x32_bf16 v[80:83], v[236:239], v[180:183], v[80:83]\n"
"ds_read_b128 v[188:191], v205 offset:2048\n"
"ds_read_b128 v[192:195], v205 offset:4096\n"
"v_mfma_f32_16x16x32_bf16 v[84:87], v[240:243], v[180:183], v[84:87]\n"
"ds_read_b64_tr_b16 v[232:233], v197 offset:24576\n"
"ds_read_b64_tr_b16 v[234:235], v197 offset:25600\n"
"v_mfma_f32_16x16x32_bf16 v[52:55], v[240:243], v[176:179], v[52:55]\n"
"v_mfma_f32_16x16x32_bf16 v[20:23], v[240:243], v[172:175], v[20:23]\n"
"s_waitcnt lgkmcnt(7)\n"
"v_mfma_f32_16x16x32_bf16 v[24:27], v[220:223], v[172:175], v[24:27]\n"
"ds_read_b64_tr_b16 v[236:237], v198 offset:24576\n"
"ds_read_b64_tr_b16 v[238:239], v198 offset:25600\n"
"v_mfma_f32_16x16x32_bf16 v[56:59], v[220:223], v[176:179], v[56:59]\n"
"v_mfma_f32_16x16x32_bf16 v[88:91], v[220:223], v[180:183], v[88:91]\n"
"v_mfma_f32_16x16x32_bf16 v[92:95], v[224:227], v[180:183], v[92:95]\n"
"ds_read_b64_tr_b16 v[240:241], v199 offset:24576\n"
"ds_read_b64_tr_b16 v[242:243], v199 offset:25600\n"
"v_mfma_f32_16x16x32_bf16 v[60:63], v[224:227], v[176:179], v[60:63]\n"
"v_mfma_f32_16x16x32_bf16 v[28:31], v[224:227], v[172:175], v[28:31]\n"
"s_waitcnt lgkmcnt(4)\n"
"v_mfma_f32_16x16x32_bf16 v[0:3], v[228:231], v[184:187], v[0:3]\n"
"ds_read_b64_tr_b16 v[220:221], v200 offset:24576\n"
"ds_read_b64_tr_b16 v[222:223], v200 offset:25600\n"
"v_mfma_f32_16x16x32_bf16 v[32:35], v[228:231], v[188:191], v[32:35]\n"
"v_mfma_f32_16x16x32_bf16 v[64:67], v[228:231], v[192:195], v[64:67]\n"
"v_mfma_f32_16x16x32_bf16 v[68:71], v[232:235], v[192:195], v[68:71]\n"
"ds_read_b64_tr_b16 v[224:225], v201 offset:24576\n"
"ds_read_b64_tr_b16 v[226:227], v201 offset:25600\n"
"v_mfma_f32_16x16x32_bf16 v[36:39], v[232:235], v[188:191], v[36:39]\n"
"ds_read_b32 v251, v252\n"
"v_mfma_f32_16x16x32_bf16 v[4:7], v[232:235], v[184:187], v[4:7]\n"
"s_waitcnt lgkmcnt(5)\n"
"v_mfma_f32_16x16x32_bf16 v[8:11], v[236:239], v[184:187], v[8:11]\n"
"ds_read_b64_tr_b16 v[228:229], v202 offset:24576\n"
"ds_read_b64_tr_b16 v[230:231], v202 offset:25600\n"
"v_mfma_f32_16x16x32_bf16 v[40:43], v[236:239], v[188:191], v[40:43]\n"
"v_add_u32 v250, 1, v250\n"
"v_mfma_f32_16x16x32_bf16 v[72:75], v[236:239], v[192:195], v[72:75]\n"
"v_mfma_f32_16x16x32_bf16 v[76:79], v[240:243], v[192:195], v[76:79]\n"
"ds_read_b64_tr_b16 v[232:233], v203 offset:24576\n"
"ds_read_b64_tr_b16 v[234:235], v203 offset:25600\n"
"v_mfma_f32_16x16x32_bf16 v[44:47], v[240:243], v[188:191], v[44:47]\n"
"v_mfma_f32_16x16x32_bf16 v[12:15], v[240:243], v[184:187], v[12:15]\n"
"s_waitcnt lgkmcnt(4)\n"
"v_cmp_gt_i32 vcc, v250, v251\n"
"v_mfma_f32_16x16x32_bf16 v[16:19], v[220:223], v[184:187], v[16:19]\n"
"s_cbranch_vccnz .LkLS12\n"
".LkLR12:\n"
"ds_read_b64_tr_b16 v[236:237], v196 offset:32768\n"
"v_mfma_f32_16x16x32_bf16 v[48:51], v[220:223], v[188:191], v[48:51]\n"
"ds_read_b64_tr_b16 v[238:239], v196 offset:33792\n"
"ds_read_b128 v[172:175], v204 offset:24576\n"
"v_mfma_f32_16x16x32_bf16 v[80:83], v[220:223], v[192:195], v[80:83]\n"
"ds_read_b128 v[176:179], v204 offset:26624\n"
"ds_read_b128 v[180:183], v204 offset:28672\n"
"v_mfma_f32_16x16x32_bf16 v[84:87], v[224:227], v[192:195], v[84:87]\n"
"ds_read_b64_tr_b16 v[240:241], v197 offset:32768\n"
"ds_read_b64_tr_b16 v[242:243], v197 offset:33792\n"
"v_mfma_f32_16x16x32_bf16 v[52:55], v[224:227], v[188:191], v[52:55]\n"
"v_mfma_f32_16x16x32_bf16 v[20:23], v[224:227], v[184:187], v[20:23]\n"
"s_waitcnt lgkmcnt(7)\n"
"s_barrier\n"
"v_mfma_f32_16x16x32_bf16 v[24:27], v[228:231], v[184:187], v[24:27]\n"
"ds_read_b64_tr_b16 v[220:221], v198 offset:32768\n"
"ds_read_b64_tr_b16 v[222:223], v198 offset:33792\n"
"v_mfma_f32_16x16x32_bf16 v[56:59], v[228:231], v[188:191], v[56:59]\n"
"v_mfma_f32_16x16x32_bf16 v[88:91], v[228:231], v[192:195], v[88:91]\n"
"v_mfma_f32_16x16x32_bf16 v[92:95], v[232:235], v[192:195], v[92:95]\n"
"ds_read_b64_tr_b16 v[224:225], v199 offset:32768\n"
"ds_read_b64_tr_b16 v[226:227], v199 offset:33792\n"
"v_mfma_f32_16x16x32_bf16 v[60:63], v[232:235], v[188:191], v[60:63]\n"
"v_mfma_f32_16x16x32_bf16 v[28:31], v[232:235], v[184:187], v[28:31]\n"
"s_waitcnt lgkmcnt(4)\n"
"v_mfma_f32_16x16x32_bf16 v[0:3], v[236:239], v[172:175], v[0:3]\n"
"ds_read_b64_tr_b16 v[228:229], v200 offset:32768\n"
"ds_read_b64_tr_b16 v[230:231], v200 offset:33792\n"
"v_mfma_f32_16x16x32_bf16 v[32:35], v[236:239], v[176:179], v[32:35]\n"
"v_mfma_f32_16x16x32_bf16 v[64:67], v[236:239], v[180:183], v[64:67]\n"
"v_mfma_f32_16x16x32_bf16 v[68:71], v[240:243], v[180:183], v[68:71]\n"
"ds_read_b64_tr_b16 v[232:233], v201 offset:32768\n"
"ds_read_b64_tr_b16 v[234:235], v201 offset:33792\n"
"v_mfma_f32_16x16x32_bf16 v[36:39], v[240:243], v[176:179], v[36:39]\n"
"ds_read_b32 v251, v252\n"
"v_mfma_f32_16x16x32_bf16 v[4:7], v[240:243], v[172:175], v[4:7]\n"
"s_waitcnt lgkmcnt(5)\n"
"v_mfma_f32_16x16x32_bf16 v[8:11], v[220:223], v[172:175], v[8:11]\n"
"ds_read_b64_tr_b16 v[236:237], v202 offset:32768\n"
"ds_read_b64_tr_b16 v[238:239], v202 offset:33792\n"
"v_mfma_f32_16x16x32_bf16 v[40:43], v[220:223], v[176:179], v[40:43]\n"
"v_add_u32 v250, 1, v250\n"
"v_mfma_f32_16x16x32_bf16 v[72:75], v[220:223], v[180:183], v[72:75]\n"
"v_mfma_f32_16x16x32_bf16 v[76:79], v[224:227], v[180:183], v[76:79]\n"
"ds_read_b64_tr_b16 v[240:241], v203 offset:32768\n"
"ds_read_b64_tr_b16 v[242:243], v203 offset:33792\n"
"v_mfma_f32_16x16x32_bf16 v[44:47], v[224:227], v[176:179], v[44:47]\n"
"v_mfma_f32_16x16x32_bf16 v[12:15], v[224:227], v[172:175], v[12:15]\n"
"s_waitcnt lgkmcnt(4)\n"
"v_cmp_gt_i32 vcc, v250, v251\n"
"v_mfma_f32_16x16x32_bf16 v[16:19], v[228:231], v[172:175], v[16:19]\n"
"s_cbranch_vccnz .LkLS13\n"
".LkLR13:\n"
"ds_read_b64_tr_b16 v[220:221], v196 offset:40960\n"
"v_mfma_f32_16x16x32_bf16 v[48:51], v[228:231], v[176:179], v[48:51]\n"
"ds_read_b64_tr_b16 v[222:223], v196 offset:41984\n"
"ds_read_b128 v[184:187], v205 offset:24576\n"
"v_mfma_f32_16x16x32_bf16 v[80:83], v[228:231], v[180:183], v[80:83]\n"
"ds_read_b128 v[188:191], v205 offset:26624\n"
"ds_read_b128 v[192:195], v205 offset:28672\n"
"v_mfma_f32_16x16x32_bf16 v[84:87], v[232:235], v[180:183], v[84:87]\n"
"ds_read_b64_tr_b16 v[224:225], v197 offset:40960\n"
"ds_read_b64_tr_b16 v[226:227], v197 offset:41984\n"
"v_mfma_f32_16x16x32_bf16 v[52:55], v[232:235], v[176:179], v[52:55]\n"
"v_mfma_f32_16x16x32_bf16 v[20:23], v[232:235], v[172:175], v[20:23]\n"
"s_waitcnt lgkmcnt(7)\n"
"v_mfma_f32_16x16x32_bf16 v[24:27], v[236:239], v[172:175], v[24:27]\n"
"ds_read_b64_tr_b16 v[228:229], v198 offset:40960\n"
"ds_read_b64_tr_b16 v[230:231], v198 offset:41984\n"
"v_mfma_f32_16x16x32_bf16 v[56:59], v[236:239], v[176:179], v[56:59]\n"
"v_mfma_f32_16x16x32_bf16 v[88:91], v[236:239], v[180:183], v[88:91]\n"
"v_mfma_f32_16x16x32_bf16 v[92:95], v[240:243], v[180:183], v[92:95]\n"
"ds_read_b64_tr_b16 v[232:233], v199 offset:40960\n"
"ds_read_b64_tr_b16 v[234:235], v199 offset:41984\n"
"v_mfma_f32_16x16x32_bf16 v[60:63], v[240:243], v[176:179], v[60:63]\n"
"v_mfma_f32_16x16x32_bf16 v[28:31], v[240:243], v[172:175], v[28:31]\n"
"s_waitcnt lgkmcnt(4)\n"
"v_mfma_f32_16x16x32_bf16 v[0:3], v[220:223], v[184:187], v[0:3]\n"
"ds_read_b64_tr_b16 v[236:237], v200 offset:40960\n"
"ds_read_b64_tr_b16 v[238:239], v200 offset:41984\n"
"v_mfma_f32_16x16x32_bf16 v[32:35], v[220:223], v[188:191], v[32:35]\n"
"v_mfma_f32_16x16x32_bf16 v[64:67], v[220:223], v[192:195], v[64:67]\n"
"v_mfma_f32_16x16x32_bf16 v[68:71], v[224:227], v[192:195], v[68:71]\n"
"ds_read_b64_tr_b16 v[240:241], v201 offset:40960\n"
"ds_read_b64_tr_b16 v[242:243], v201 offset:41984\n"
"v_mfma_f32_16x16x32_bf16 v[36:39], v[224:227], v[188:191], v[36:39]\n"
"ds_read_b32 v251, v252\n"
"v_mfma_f32_16x16x32_bf16 v[4:7], v[224:227], v[184:187], v[4:7]\n"
"s_waitcnt lgkmcnt(5)\n"
"v_mfma_f32_16x16x32_bf16 v[8:11], v[228:231], v[184:187], v[8:11]\n"
"ds_read_b64_tr_b16 v[220:221], v202 offset:40960\n"
"ds_read_b64_tr_b16 v[222:223], v202 offset:41984\n"
"v_mfma_f32_16x16x32_bf16 v[40:43], v[228:231], v[188:191], v[40:43]\n"
"v_add_u32 v250, 1, v250\n"
"v_mfma_f32_16x16x32_bf16 v[72:75], v[228:231], v[192:195], v[72:75]\n"
"v_mfma_f32_16x16x32_bf16 v[76:79], v[232:235], v[192:195], v[76:79]\n"
"ds_read_b64_tr_b16 v[224:225], v203 offset:40960\n"
"ds_read_b64_tr_b16 v[226:227], v203 offset:41984\n"
"v_mfma_f32_16x16x32_bf16 v[44:47], v[232:235], v[188:191], v[44:47]\n"
"v_mfma_f32_16x16x32_bf16 v[12:15], v[232:235], v[184:187], v[12:15]\n"
"s_waitcnt lgkmcnt(4)\n"
"v_cmp_gt_i32 vcc, v250, v251\n"
"v_mfma_f32_16x16x32_bf16 v[16:19], v[236:239], v[184:187], v[16:19]\n"
"s_cbranch_vccnz .LkLS14\n"
".LkLR14:\n"
"ds_read_b64_tr_b16 v[228:229], v196 offset:49152\n"
"v_mfma_f32_16x16x32_bf16 v[48:51], v[236:239], v[188:191], v[48:51]\n"
"ds_read_b64_tr_b16 v[230:231], v196 offset:50176\n"
"ds_read_b128 v[172:175], v204 offset:49152\n"
"v_mfma_f32_16x16x32_bf16 v[80:83], v[236:239], v[192:195], v[80:83]\n"
"ds_read_b128 v[176:179], v204 offset:51200\n"
"ds_read_b128 v[180:183], v204 offset:53248\n"
"v_mfma_f32_16x16x32_bf16 v[84:87], v[240:243], v[192:195], v[84:87]\n"
"ds_read_b64_tr_b16 v[232:233], v197 offset:49152\n"
"ds_read_b64_tr_b16 v[234:235], v197 offset:50176\n"
"v_mfma_f32_16x16x32_bf16 v[52:55], v[240:243], v[188:191], v[52:55]\n"
"ds_write_addtid_b32 v250 offset:0\n"
"v_mfma_f32_16x16x32_bf16 v[20:23], v[240:243], v[184:187], v[20:23]\n"
"s_waitcnt lgkmcnt(8)\n"
"v_mfma_f32_16x16x32_bf16 v[24:27], v[220:223], v[184:187], v[24:27]\n"
"ds_read_b64_tr_b16 v[236:237], v198 offset:49152\n"
"ds_read_b64_tr_b16 v[238:239], v198 offset:50176\n"
"v_mfma_f32_16x16x32_bf16 v[56:59], v[220:223], v[188:191], v[56:59]\n"
"v_mfma_f32_16x16x32_bf16 v[88:91], v[220:223], v[192:195], v[88:91]\n"
"v_mfma_f32_16x16x32_bf16 v[92:95], v[224:227], v[192:195], v[92:95]\n"
"ds_read_b64_tr_b16 v[240:241], v199 offset:49152\n"
"ds_read_b64_tr_b16 v[242:243], v199 offset:50176\n"
"v_mfma_f32_16x16x32_bf16 v[60:63], v[224:227], v[188:191], v[60:63]\n"
"v_mfma_f32_16x16x32_bf16 v[28:31], v[224:227], v[184:187], v[28:31]\n"
"s_waitcnt lgkmcnt(5)\n"
"v_mfma_f32_16x16x32_bf16 v[0:3], v[228:231], v[172:175], v[0:3]\n"
"ds_read_b64_tr_b16 v[220:221], v200 offset:49152\n"
"ds_read_b64_tr_b16 v[222:223], v200 offset:50176\n"
"v_mfma_f32_16x16x32_bf16 v[32:35], v[228:231], v[176:179], v[32:35]\n"
"v_mfma_f32_16x16x32_bf16 v[64:67], v[228:231], v[180:183], v[64:67]\n"
"v_mfma_f32_16x16x32_bf16 v[68:71], v[232:235], v[180:183], v[68:71]\n"
"ds_read_b64_tr_b16 v[224:225], v201 offset:49152\n"
"ds_read_b64_tr_b16 v[226:227], v201 offset:50176\n"
"v_mfma_f32_16x16x32_bf16 v[36:39], v[232:235], v[176:179], v[36:39]\n"
"ds_read_b32 v251, v252\n"
"v_mfma_f32_16x16x32_bf16 v[4:7], v[232:235], v[172:175], v[4:7]\n"
"s_waitcnt lgkmcnt(5)\n"
"v_mfma_f32_16x16x32_bf16 v[8:11], v[236:239], v[172:175], v[8:11]\n"
"ds_read_b64_tr_b16 v[228:229], v202 offset:49152\n"
"ds_read_b64_tr_b16 v[230:231], v202 offset:50176\n"
"v_mfma_f32_16x16x32_bf16 v[40:43], v[236:239], v[176:179], v[40:43]\n"
"v_add_u32 v250, 1, v250\n"
"v_mfma_f32_16x16x32_bf16 v[72:75], v[236:239], v[180:183], v[72:75]\n"
"v_mfma_f32_16x16x32_bf16 v[76:79], v[240:243], v[180:183], v[76:79]\n"
"ds_read_b64_tr_b16 v[232:233], v203 offset:49152\n"
"ds_read_b64_tr_b16 v[234:235], v203 offset:50176\n"
"v_mfma_f32_16x16x32_bf16 v[44:47], v[240:243], v[176:179], v[44:47]\n"
"v_mfma_f32_16x16x32_bf16 v[12:15], v[240:243], v[172:175], v[12:15]\n"
"s_waitcnt lgkmcnt(4)\n"
"v_cmp_gt_i32 vcc, v250, v251\n"
"v_mfma_f32_16x16x32_bf16 v[16:19], v[220:223], v[172:175], v[16:19]\n"
"s_cbranch_vccnz .LkLS15\n"
".LkLR15:\n"
"ds_read_b64_tr_b16 v[236:237], v196 offset:57344\n"
"v_mfma_f32_16x16x32_bf16 v[48:51], v[220:223], v[176:179], v[48:51]\n"
"ds_read_b64_tr_b16 v[238:239], v196 offset:58368\n"
"ds_read_b128 v[184:187], v205 offset:49152\n"
"v_mfma_f32_16x16x32_bf16 v[80:83], v[220:223], v[180:183], v[80:83]\n"
"ds_read_b128 v[188:191], v205 offset:51200\n"
"ds_read_b128 v[192:195], v205 offset:53248\n"
"v_mfma_f32_16x16x32_bf16 v[84:87], v[224:227], v[180:183], v[84:87]\n"
"ds_read_b64_tr_b16 v[240:241], v197 offset:57344\n"
"ds_read_b64_tr_b16 v[242:243], v197 offset:58368\n"
"v_mfma_f32_16x16x32_bf16 v[52:55], v[224:227], v[176:179], v[52:55]\n"
"v_mfma_f32_16x16x32_bf16 v[20:23], v[224:227], v[172:175], v[20:23]\n"
"s_waitcnt lgkmcnt(7)\n"
"v_mfma_f32_16x16x32_bf16 v[24:27], v[228:231], v[172:175], v[24:27]\n"
"ds_read_b64_tr_b16 v[220:221], v198 offset:57344\n"
"ds_read_b64_tr_b16 v[222:223], v198 offset:58368\n"
"v_mfma_f32_16x16x32_bf16 v[56:59], v[228:231], v[176:179], v[56:59]\n"
"v_mfma_f32_16x16x32_bf16 v[88:91], v[228:231], v[180:183], v[88:91]\n"
"v_mfma_f32_16x16x32_bf16 v[92:95], v[232:235], v[180:183], v[92:95]\n"
"ds_read_b64_tr_b16 v[224:225], v199 offset:57344\n"
"ds_read_b64_tr_b16 v[226:227], v199 offset:58368\n"
"v_mfma_f32_16x16x32_bf16 v[60:63], v[232:235], v[176:179], v[60:63]\n"
"v_mfma_f32_16x16x32_bf16 v[28:31], v[232:235], v[172:175], v[28:31]\n"
"s_waitcnt lgkmcnt(4)\n"
"v_mfma_f32_16x16x32_bf16 v[0:3], v[236:239], v[184:187], v[0:3]\n"
"ds_read_b64_tr_b16 v[228:229], v200 offset:57344\n"
"ds_read_b64_tr_b16 v[230:231], v200 offset:58368\n"
"v_mfma_f32_16x16x32_bf16 v[32:35], v[236:239], v[188:191], v[32:35]\n"
"v_mfma_f32_16x16x32_bf16 v[64:67], v[236:239], v[192:195], v[64:67]\n"
"v_mfma_f32_16x16x32_bf16 v[68:71], v[240:243], v[192:195], v[68:71]\n"
"ds_read_b64_tr_b16 v[232:233], v201 offset:57344\n"
"ds_read_b64_tr_b16 v[234:235], v201 offset:58368\n"
"v_mfma_f32_16x16x32_bf16 v[36:39], v[240:243], v[188:191], v[36:39]\n"
"ds_read_b32 v251, v252\n"
"v_mfma_f32_16x16x32_bf16 v[4:7], v[240:243], v[184:187], v[4:7]\n"
"s_waitcnt lgkmcnt(5)\n"
"v_mfma_f32_16x16x32_bf16 v[8:11], v[220:223], v[184:187], v[8:11]\n"
"ds_read_b64_tr_b16 v[236:237], v202 offset:57344\n"
"ds_read_b64_tr_b16 v[238:239], v202 offset:58368\n"
"v_mfma_f32_16x16x32_bf16 v[40:43], v[220:223], v[188:191], v[40:43]\n"
"v_add_u32 v250, 1, v250\n"
"v_mfma_f32_16x16x32_bf16 v[72:75], v[220:223], v[192:195], v[72:75]\n"
"v_mfma_f32_16x16x32_bf16 v[76:79], v[224:227], v[192:195], v[76:79]\n"
"ds_read_b64_tr_b16 v[240:241], v203 offset:57344\n"
"ds_read_b64_tr_b16 v[242:243], v203 offset:58368\n"
"v_mfma_f32_16x16x32_bf16 v[44:47], v[224:227], v[188:191], v[44:47]\n"
"v_mfma_f32_16x16x32_bf16 v[12:15], v[224:227], v[184:187], v[12:15]\n"
"s_waitcnt lgkmcnt(4)\n"
"v_cmp_gt_i32 vcc, v250, v251\n"
"v_mfma_f32_16x16x32_bf16 v[16:19], v[228:231], v[184:187], v[16:19]\n"
"s_cbranch_vccnz .LkLS16\n"
".LkLR16:\n"
"ds_read_b128 v[220:223], v244 offset:0\n"
"v_mfma_f32_16x16x32_bf16 v[48:51], v[228:231], v[188:191], v[48:51]\n"
"v_mfma_f32_16x16x32_bf16 v[80:83], v[228:231], v[192:195], v[80:83]\n"
"v_mfma_f32_16x16x32_bf16 v[84:87], v[232:235], v[192:195], v[84:87]\n"
"ds_read_b128 v[224:227], v245 offset:0\n"
"v_mfma_f32_16x16x32_bf16 v[52:55], v[232:235], v[188:191], v[52:55]\n"
"ds_write_addtid_b32 v250 offset:0\n"
"v_mfma_f32_16x16x32_bf16 v[20:23], v[232:235], v[184:187], v[20:23]\n"
"s_waitcnt lgkmcnt(3)\n"
"v_mfma_f32_16x16x32_bf16 v[24:27], v[236:239], v[184:187], v[24:27]\n"
"ds_read_b128 v[228:231], v244 offset:512\n"
"v_mfma_f32_16x16x32_bf16 v[56:59], v[236:239], v[188:191], v[56:59]\n"
"v_mfma_f32_16x16x32_bf16 v[88:91], v[236:239], v[192:195], v[88:91]\n"
"v_mfma_f32_16x16x32_bf16 v[92:95], v[240:243], v[192:195], v[92:95]\n"
"ds_read_b128 v[232:235], v245 offset:512\n"
"v_mfma_f32_16x16x32_bf16 v[60:63], v[240:243], v[188:191], v[60:63]\n"
"ds_read_b128 v[236:239], v244 offset:4096\n"
"v_mfma_f32_16x16x32_bf16 v[28:31], v[240:243], v[184:187], v[28:31]\n"
"v_pk_mul_f32 v[0:1], v[0:1], s[78:79]\n"
"v_pk_mul_f32 v[2:3], v[2:3], s[78:79]\n"
"v_pk_mul_f32 v[4:5], v[4:5], s[78:79]\n"
"v_pk_mul_f32 v[6:7], v[6:7], s[78:79]\n"
"v_cvt_pk_bf16_f32 v96, v0, v1\n"
"v_cvt_pk_bf16_f32 v97, v2, v3\n"
"v_cvt_pk_bf16_f32 v98, v4, v5\n"
"v_cvt_pk_bf16_f32 v99, v6, v7\n"
"v_pk_mul_f32 v[32:33], v[32:33], s[78:79]\n"
"v_pk_mul_f32 v[34:35], v[34:35], s[78:79]\n"
"v_pk_mul_f32 v[36:37], v[36:37], s[78:79]\n"
"v_pk_mul_f32 v[38:39], v[38:39], s[78:79]\n"
"v_cvt_pk_bf16_f32 v112, v32, v33\n"
"v_cvt_pk_bf16_f32 v113, v34, v35\n"
"v_cvt_pk_bf16_f32 v114, v36, v37\n"
"v_cvt_pk_bf16_f32 v115, v38, v39\n"
"v_pk_mul_f32 v[64:65], v[64:65], s[78:79]\n"
"v_pk_mul_f32 v[66:67], v[66:67], s[78:79]\n"
"v_pk_mul_f32 v[68:69], v[68:69], s[78:79]\n"
"v_pk_mul_f32 v[70:71], v[70:71], s[78:79]\n"
"v_cvt_pk_bf16_f32 v128, v64, v65\n"
"v_cvt_pk_bf16_f32 v129, v66, v67\n"
"v_cvt_pk_bf16_f32 v130, v68, v69\n"
"v_cvt_pk_bf16_f32 v131, v70, v71\n"
"v_pk_mul_f32 v[8:9], v[8:9], s[78:79]\n"
"v_pk_mul_f32 v[10:11], v[10:11], s[78:79]\n"
"v_pk_mul_f32 v[12:13], v[12:13], s[78:79]\n"
"v_pk_mul_f32 v[14:15], v[14:15], s[78:79]\n"
"v_cvt_pk_bf16_f32 v100, v8, v9\n"
"v_cvt_pk_bf16_f32 v101, v10, v11\n"
"v_cvt_pk_bf16_f32 v102, v12, v13\n"
"v_cvt_pk_bf16_f32 v103, v14, v15\n"
"v_pk_mul_f32 v[40:41], v[40:41], s[78:79]\n"
"v_pk_mul_f32 v[42:43], v[42:43], s[78:79]\n"
"v_pk_mul_f32 v[44:45], v[44:45], s[78:79]\n"
"v_pk_mul_f32 v[46:47], v[46:47], s[78:79]\n"
"v_cvt_pk_bf16_f32 v116, v40, v41\n"
"v_cvt_pk_bf16_f32 v117, v42, v43\n"
"v_cvt_pk_bf16_f32 v118, v44, v45\n"
"v_cvt_pk_bf16_f32 v119, v46, v47\n"
"v_pk_mul_f32 v[72:73], v[72:73], s[78:79]\n"
"v_pk_mul_f32 v[74:75], v[74:75], s[78:79]\n"
"v_pk_mul_f32 v[76:77], v[76:77], s[78:79]\n"
"v_pk_mul_f32 v[78:79], v[78:79], s[78:79]\n"
"v_cvt_pk_bf16_f32 v132, v72, v73\n"
"v_cvt_pk_bf16_f32 v133, v74, v75\n"
"v_cvt_pk_bf16_f32 v134, v76, v77\n"
"v_cvt_pk_bf16_f32 v135, v78, v79\n"
"v_pk_mul_f32 v[16:17], v[16:17], s[78:79]\n"
"v_pk_mul_f32 v[18:19], v[18:19], s[78:79]\n"
"v_pk_mul_f32 v[20:21], v[20:21], s[78:79]\n"
"v_pk_mul_f32 v[22:23], v[22:23], s[78:79]\n"
"v_cvt_pk_bf16_f32 v104, v16, v17\n"
"v_cvt_pk_bf16_f32 v105, v18, v19\n"
"v_cvt_pk_bf16_f32 v106, v20, v21\n"
"v_cvt_pk_bf16_f32 v107, v22, v23\n"
"v_pk_mul_f32 v[48:49], v[48:49], s[78:79]\n"
"v_pk_mul_f32 v[50:51], v[50:51], s[78:79]\n"
"v_pk_mul_f32 v[52:53], v[52:53], s[78:79]\n"
"v_pk_mul_f32 v[54:55], v[54:55], s[78:79]\n"
"v_cvt_pk_bf16_f32 v120, v48, v49\n"
"v_cvt_pk_bf16_f32 v121, v50, v51\n"
"v_cvt_pk_bf16_f32 v122, v52, v53\n"
"v_cvt_pk_bf16_f32 v123, v54, v55\n"
"v_pk_mul_f32 v[80:81], v[80:81], s[78:79]\n"
"v_pk_mul_f32 v[82:83], v[82:83], s[78:79]\n"
"v_pk_mul_f32 v[84:85], v[84:85], s[78:79]\n"
"v_pk_mul_f32 v[86:87], v[86:87], s[78:79]\n"
"v_cvt_pk_bf16_f32 v136, v80, v81\n"
"v_cvt_pk_bf16_f32 v137, v82, v83\n"
"v_cvt_pk_bf16_f32 v138, v84, v85\n"
"v_cvt_pk_bf16_f32 v139, v86, v87\n"
"v_pk_mul_f32 v[24:25], v[24:25], s[78:79]\n"
"v_pk_mul_f32 v[26:27], v[26:27], s[78:79]\n"
"v_pk_mul_f32 v[28:29], v[28:29], s[78:79]\n"
"v_pk_mul_f32 v[30:31], v[30:31], s[78:79]\n"
"v_cvt_pk_bf16_f32 v108, v24, v25\n"
"v_cvt_pk_bf16_f32 v109, v26, v27\n"
"v_cvt_pk_bf16_f32 v110, v28, v29\n"
"v_cvt_pk_bf16_f32 v111, v30, v31\n"
"v_pk_mul_f32 v[56:57], v[56:57], s[78:79]\n"
"v_pk_mul_f32 v[58:59], v[58:59], s[78:79]\n"
"v_pk_mul_f32 v[60:61], v[60:61], s[78:79]\n"
"v_pk_mul_f32 v[62:63], v[62:63], s[78:79]\n"
"v_cvt_pk_bf16_f32 v124, v56, v57\n"
"v_cvt_pk_bf16_f32 v125, v58, v59\n"
"v_cvt_pk_bf16_f32 v126, v60, v61\n"
"v_cvt_pk_bf16_f32 v127, v62, v63\n"
"v_pk_mul_f32 v[88:89], v[88:89], s[78:79]\n"
"v_pk_mul_f32 v[90:91], v[90:91], s[78:79]\n"
"v_pk_mul_f32 v[92:93], v[92:93], s[78:79]\n"
"v_pk_mul_f32 v[94:95], v[94:95], s[78:79]\n"
"v_cvt_pk_bf16_f32 v140, v88, v89\n"
"v_cvt_pk_bf16_f32 v141, v90, v91\n"
"v_cvt_pk_bf16_f32 v142, v92, v93\n"
"v_cvt_pk_bf16_f32 v143, v94, v95\n"
"s_mov_b32 exec_lo, 0xffff0000\n"
"s_mov_b32 exec_hi, 0xffff0000\n"
"v_swap_b32 v96, v98\n"
"v_swap_b32 v97, v99\n"
"v_swap_b32 v112, v114\n"
"v_swap_b32 v113, v115\n"
"v_swap_b32 v128, v130\n"
"v_swap_b32 v129, v131\n"
"v_swap_b32 v100, v102\n"
"v_swap_b32 v101, v103\n"
"v_swap_b32 v116, v118\n"
"v_swap_b32 v117, v119\n"
"v_swap_b32 v132, v134\n"
"v_swap_b32 v133, v135\n"
"v_swap_b32 v104, v106\n"
"v_swap_b32 v105, v107\n"
"v_swap_b32 v120, v122\n"
"v_swap_b32 v121, v123\n"
"v_swap_b32 v136, v138\n"
"v_swap_b32 v137, v139\n"
"v_swap_b32 v108, v110\n"
"v_swap_b32 v109, v111\n"
"v_swap_b32 v124, v126\n"
"v_swap_b32 v125, v127\n"
"v_swap_b32 v140, v142\n"
"v_swap_b32 v141, v143\n"
"s_mov_b64 exec, -1\n"
"s_waitcnt lgkmcnt(4)\n"
"v_mfma_f32_16x16x32_bf16 v[172:175], v[220:223], v[96:99], 0\n"
"v_mfma_f32_16x16x32_bf16 v[176:179], v[220:223], v[112:115], 0\n"
"v_mfma_f32_16x16x32_bf16 v[180:183], v[220:223], v[128:131], 0\n"
"ds_read_b128 v[220:223], v245 offset:4096\n"
"v_mfma_f32_16x16x32_bf16 v[172:175], v[224:227], v[100:103], v[172:175]\n"
"v_mfma_f32_16x16x32_bf16 v[176:179], v[224:227], v[116:119], v[176:179]\n"
"v_mfma_f32_16x16x32_bf16 v[180:183], v[224:227], v[132:135], v[180:183]\n"
"s_waitcnt lgkmcnt(2)\n"
"ds_read_b128 v[224:227], v244 offset:4608\n"
"v_mfma_f32_16x16x32_bf16 v[172:175], v[228:231], v[104:107], v[172:175]\n"
"v_mfma_f32_16x16x32_bf16 v[176:179], v[228:231], v[120:123], v[176:179]\n"
"v_mfma_f32_16x16x32_bf16 v[180:183], v[228:231], v[136:139], v[180:183]\n"
"ds_read_b128 v[228:231], v245 offset:4608\n"
"v_mfma_f32_16x16x32_bf16 v[172:175], v[232:235], v[108:111], v[172:175]\n"
"ds_read_b32 v251, v252\n"
"v_add_u32 v250, 1, v250\n"
"v_mfma_f32_16x16x32_bf16 v[176:179], v[232:235], v[124:127], v[176:179]\n"
"v_mfma_f32_16x16x32_bf16 v[180:183], v[232:235], v[140:143], v[180:183]\n"
"s_waitcnt lgkmcnt(3)\n"
"v_mfma_f32_16x16x32_bf16 v[184:187], v[236:239], v[96:99], 0\n"
"v_mfma_f32_16x16x32_bf16 v[188:191], v[236:239], v[112:115], 0\n"
"v_mfma_f32_16x16x32_bf16 v[192:195], v[236:239], v[128:131], 0\n"
"v_mfma_f32_16x16x32_bf16 v[184:187], v[220:223], v[100:103], v[184:187]\n"
"v_max3_f32 v0, v172, v173, v174\n"
"s_nop 0\n"
"v_max3_f32 v1, v176, v177, v178\n"
"v_mfma_f32_16x16x32_bf16 v[188:191], v[220:223], v[116:119], v[188:191]\n"
"v_max3_f32 v2, v180, v181, v182\n"
"v_max_f32 v0, v0, v175\n"
"v_mfma_f32_16x16x32_bf16 v[192:195], v[220:223], v[132:135], v[192:195]\n"
"s_waitcnt lgkmcnt(0)\n"
"v_max_f32 v1, v1, v179\n"
"v_mfma_f32_16x16x32_bf16 v[184:187], v[224:227], v[104:107], v[184:187]\n"
"v_max_f32 v2, v2, v183\n"
"v_mov_b32 v3, v0\n"
"v_mfma_f32_16x16x32_bf16 v[188:191], v[224:227], v[120:123], v[188:191]\n"
"v_mov_b32 v4, v1\n"
"v_mov_b32 v5, v2\n"
"v_mfma_f32_16x16x32_bf16 v[192:195], v[224:227], v[136:139], v[192:195]\n"
"v_permlane32_swap_b32 v0, v3\n"
"v_permlane32_swap_b32 v1, v4\n"
"v_mfma_f32_16x16x32_bf16 v[184:187], v[228:231], v[108:111], v[184:187]\n"
"v_permlane32_swap_b32 v2, v5\n"
"v_max_f32 v0, v0, v3\n"
"v_mfma_f32_16x16x32_bf16 v[188:191], v[228:231], v[124:127], v[188:191]\n"
"v_max_f32 v1, v1, v4\n"
"v_max_f32 v2, v2, v5\n"
"v_mfma_f32_16x16x32_bf16 v[192:195], v[228:231], v[140:143], v[192:195]\n"
"v_cmp_gt_i32 vcc, v250, v251\n"
"s_cbranch_vccnz .LkLS17\n"
".LkLR17:\n"
"ds_read_b128 v[220:223], v244 offset:8192\n"
"ds_read_b128 v[224:227], v245 offset:8192\n"
"ds_read_b128 v[228:231], v244 offset:8704\n"
"ds_read_b128 v[232:235], v245 offset:8704\n"
"ds_read_b128 v[236:239], v244 offset:12288\n"
"v_mov_b32 v3, v0\n"
"v_mov_b32 v4, v1\n"
"v_mov_b32 v5, v2\n"
"v_permlane16_swap_b32 v0, v3\n"
"v_permlane16_swap_b32 v1, v4\n"
"v_permlane16_swap_b32 v2, v5\n"
"v_min_f32_e64 v156, -v0, -v3\n"
"v_min_f32_e64 v157, -v0, -v3\n"
"v_min_f32_e64 v158, -v0, -v3\n"
"v_min_f32_e64 v159, -v0, -v3\n"
"v_min_f32_e64 v160, -v1, -v4\n"
"v_min_f32_e64 v161, -v1, -v4\n"
"v_min_f32_e64 v162, -v1, -v4\n"
"v_min_f32_e64 v163, -v1, -v4\n"
"v_min_f32_e64 v164, -v2, -v5\n"
"v_min_f32_e64 v165, -v2, -v5\n"
"v_min_f32_e64 v166, -v2, -v5\n"
"v_min_f32_e64 v167, -v2, -v5\n"
"v_add_f32 v172, v156, v172\n"
"v_add_f32 v173, v156, v173\n"
"v_add_f32 v174, v156, v174\n"
"v_add_f32 v175, v156, v175\n"
"v_add_f32 v176, v160, v176\n"
"v_add_f32 v177, v160, v177\n"
"v_add_f32 v178, v160, v178\n"
"v_add_f32 v179, v160, v179\n"
"v_add_f32 v180, v164, v180\n"
"v_add_f32 v181, v164, v181\n"
"v_add_f32 v182, v164, v182\n"
"v_add_f32 v183, v164, v183\n"
"v_add_f32 v184, v156, v184\n"
"v_add_f32 v185, v156, v185\n"
"v_add_f32 v186, v156, v186\n"
"v_add_f32 v187, v156, v187\n"
"v_add_f32 v188, v160, v188\n"
"v_add_f32 v189, v160, v189\n"
"v_add_f32 v190, v160, v190\n"
"v_add_f32 v191, v160, v191\n"
"v_add_f32 v192, v164, v192\n"
"v_add_f32 v193, v164, v193\n"
"v_add_f32 v194, v164, v194\n"
"v_add_f32 v195, v164, v195\n"
"v_exp_f32 v172, v172\n"
"v_exp_f32 v173, v173\n"
"v_exp_f32 v174, v174\n"
"v_exp_f32 v175, v175\n"
"v_exp_f32 v176, v176\n"
"v_exp_f32 v177, v177\n"
"v_exp_f32 v178, v178\n"
"v_exp_f32 v179, v179\n"
"v_exp_f32 v180, v180\n"
"v_exp_f32 v181, v181\n"
"v_exp_f32 v182, v182\n"
"v_exp_f32 v183, v183\n"
"v_exp_f32 v184, v184\n"
"v_exp_f32 v185, v185\n"
"v_exp_f32 v186, v186\n"
"v_exp_f32 v187, v187\n"
"v_exp_f32 v188, v188\n"
"v_exp_f32 v189, v189\n"
"v_exp_f32 v190, v190\n"
"v_exp_f32 v191, v191\n"
"v_cvt_pk_bf16_f32 v196, v172, v173\n"
"v_cvt_pk_bf16_f32 v197, v174, v175\n"
"v_cvt_pk_bf16_f32 v200, v176, v177\n"
"v_cvt_pk_bf16_f32 v201, v178, v179\n"
"v_cvt_pk_bf16_f32 v204, v180, v181\n"
"v_cvt_pk_bf16_f32 v205, v182, v183\n"
"s_waitcnt lgkmcnt(3)\n"
"v_mfma_f32_16x16x32_bf16 v[172:175], v[220:223], v[96:99], v[156:159]\n"
"v_exp_f32 v192, v192\n"
"v_mfma_f32_16x16x32_bf16 v[176:179], v[220:223], v[112:115], v[160:163]\n"
"v_exp_f32 v193, v193\n"
"v_mfma_f32_16x16x32_bf16 v[180:183], v[220:223], v[128:131], v[164:167]\n"
"ds_read_b128 v[220:223], v245 offset:12288\n"
"v_cvt_pk_bf16_f32 v198, v184, v185\n"
"v_mfma_f32_16x16x32_bf16 v[172:175], v[224:227], v[100:103], v[172:175]\n"
"v_exp_f32 v194, v194\n"
"v_mfma_f32_16x16x32_bf16 v[176:179], v[224:227], v[116:119], v[176:179]\n"
"v_exp_f32 v195, v195\n"
"v_mfma_f32_16x16x32_bf16 v[180:183], v[224:227], v[132:135], v[180:183]\n"
"s_waitcnt lgkmcnt(2)\n"
"ds_read_b128 v[224:227], v244 offset:12800\n"
"v_mfma_f32_16x16x32_bf16 v[172:175], v[228:231], v[104:107], v[172:175]\n"
"v_cvt_pk_bf16_f32 v199, v186, v187\n"
"v_cvt_pk_bf16_f32 v202, v188, v189\n"
"v_mfma_f32_16x16x32_bf16 v[176:179], v[228:231], v[120:123], v[176:179]\n"
"v_mfma_f32_16x16x32_bf16 v[180:183], v[228:231], v[136:139], v[180:183]\n"
"ds_read_b128 v[228:231], v245 offset:12800\n"
"v_cvt_pk_bf16_f32 v203, v190, v191\n"
"v_mfma_f32_16x16x32_bf16 v[172:175], v[232:235], v[108:111], v[172:175]\n"
"v_add_u32 v250, 1, v250\n"
"v_cvt_pk_bf16_f32 v206, v192, v193\n"
"v_mfma_f32_16x16x32_bf16 v[176:179], v[232:235], v[124:127], v[176:179]\n"
"v_mfma_f32_16x16x32_bf16 v[180:183], v[232:235], v[140:143], v[180:183]\n"
"s_waitcnt lgkmcnt(2)\n"
"v_cvt_pk_bf16_f32 v207, v194, v195\n"
"v_mfma_f32_16x16x32_bf16 v[184:187], v[236:239], v[96:99], v[156:159]\n"
"ds_read_b64_tr_b16 v[232:233], v246 offset:0\n"
"ds_read_b64_tr_b16 v[234:235], v246 offset:4096\n"
"v_mfma_f32_16x16x32_bf16 v[188:191], v[236:239], v[112:115], v[160:163]\n"
"v_exp_f32 v172, v172\n"
"v_mfma_f32_16x16x32_bf16 v[192:195], v[236:239], v[128:131], v[164:167]\n"
"v_exp_f32 v173, v173\n"
"v_mfma_f32_16x16x32_bf16 v[184:187], v[220:223], v[100:103], v[184:187]\n"
"ds_read_b64_tr_b16 v[236:237], v247 offset:0\n"
"ds_read_b64_tr_b16 v[238:239], v247 offset:4096\n"
"v_mfma_f32_16x16x32_bf16 v[188:191], v[220:223], v[116:119], v[188:191]\n"
"v_exp_f32 v174, v174\n"
"v_mfma_f32_16x16x32_bf16 v[192:195], v[220:223], v[132:135], v[192:195]\n"
"s_waitcnt lgkmcnt(4)\n"
"v_cvt_pk_bf16_f32 v208, v172, v173\n"
"v_mfma_f32_16x16x32_bf16 v[184:187], v[224:227], v[104:107], v[184:187]\n"
"ds_read_b64_tr_b16 v[220:221], v248 offset:0\n"
"ds_read_b64_tr_b16 v[222:223], v248 offset:4096\n"
"v_mfma_f32_16x16x32_bf16 v[188:191], v[224:227], v[120:123], v[188:191]\n"
"v_exp_f32 v175, v175\n"
"v_mfma_f32_16x16x32_bf16 v[192:195], v[224:227], v[136:139], v[192:195]\n"
"v_exp_f32 v176, v176\n"
"v_mfma_f32_16x16x32_bf16 v[184:187], v[228:231], v[108:111], v[184:187]\n"
"ds_read_b64_tr_b16 v[224:225], v249 offset:0\n"
"ds_read_b64_tr_b16 v[226:227], v249 offset:4096\n"
"v_mfma_f32_16x16x32_bf16 v[188:191], v[228:231], v[124:127], v[188:191]\n"
"v_exp_f32 v177, v177\n"
"v_mfma_f32_16x16x32_bf16 v[192:195], v[228:231], v[140:143], v[192:195]\n"
"v_exp_f32 v178, v178\n"
"v_mfma_f32_16x16x32_bf16 v[144:147], v[168:171], v[196:199], 0\n"
"ds_read_b64_tr_b16 v[228:229], v246 offset:512\n"
"ds_read_b64_tr_b16 v[230:231], v246 offset:4608\n"
"v_mfma_f32_16x16x32_bf16 v[148:151], v[168:171], v[200:203], 0\n"
"v_exp_f32 v179, v179\n"
"v_mfma_f32_16x16x32_bf16 v[152:155], v[168:171], v[204:207], 0\n"
"s_waitcnt lgkmcnt(6)\n"
"v_cvt_pk_bf16_f32 v209, v174, v175\n"
"v_mfma_f32_16x16x32_bf16 v[64:67], v[232:235], v[204:207], 0\n"
"v_exp_f32 v180, v180\n"
"v_mfma_f32_16x16x32_bf16 v[32:35], v[232:235], v[200:203], 0\n"
"v_exp_f32 v181, v181\n"
"v_mfma_f32_16x16x32_bf16 v[0:3], v[232:235], v[196:199], 0\n"
"ds_read_b64_tr_b16 v[232:233], v247 offset:512\n"
"ds_read_b64_tr_b16 v[234:235], v247 offset:4608\n"
"v_mfma_f32_16x16x32_bf16 v[4:7], v[236:239], v[196:199], 0\n"
"ds_read_b32 v251, v252\n"
"v_cvt_pk_bf16_f32 v212, v176, v177\n"
"v_mfma_f32_16x16x32_bf16 v[36:39], v[236:239], v[200:203], 0\n"
"v_exp_f32 v182, v182\n"
"v_mfma_f32_16x16x32_bf16 v[68:71], v[236:239], v[204:207], 0\n"
"s_waitcnt lgkmcnt(5)\n"
"v_cvt_pk_bf16_f32 v213, v178, v179\n"
"v_mfma_f32_16x16x32_bf16 v[72:75], v[220:223], v[204:207], 0\n"
"ds_read_b64_tr_b16 v[236:237], v248 offset:512\n"
"ds_read_b64_tr_b16 v[238:239], v248 offset:4608\n"
"v_mfma_f32_16x16x32_bf16 v[40:43], v[220:223], v[200:203], 0\n"
"v_exp_f32 v183, v183\n"
"v_mfma_f32_16x16x32_bf16 v[8:11], v[220:223], v[196:199], 0\n"
"ds_read_b64_tr_b16 v[240:241], v249 offset:512\n"
"ds_read_b64_tr_b16 v[242:243], v249 offset:4608\n"
"v_mfma_f32_16x16x32_bf16 v[12:15], v[224:227], v[196:199], 0\n"
"ds_write_addtid_b32 v250 offset:0\n"
"v_exp_f32 v184, v184\n"
"v_mfma_f32_16x16x32_bf16 v[44:47], v[224:227], v[200:203], 0\n"
"v_exp_f32 v185, v185\n"
"v_mfma_f32_16x16x32_bf16 v[76:79], v[224:227], v[204:207], 0\n"
"s_waitcnt lgkmcnt(5)\n"
"v_cmp_gt_i32 vcc, v250, v251\n"
"v_mfma_f32_16x16x32_bf16 v[80:83], v[228:231], v[204:207], 0\n"
"s_cbranch_vccnz .LkLS18\n"
".LkLR18:\n"
"ds_read_b128 v[220:223], v244 offset:16384\n"
"v_mfma_f32_16x16x32_bf16 v[48:51], v[228:231], v[200:203], 0\n"
"v_exp_f32 v186, v186\n"
"v_mfma_f32_16x16x32_bf16 v[16:19], v[228:231], v[196:199], 0\n"
"ds_read_b128 v[224:227], v245 offset:16384\n"
"ds_read_b128 v[228:231], v244 offset:16896\n"
"v_mfma_f32_16x16x32_bf16 v[20:23], v[232:235], v[196:199], 0\n"
"v_cvt_pk_bf16_f32 v216, v180, v181\n"
"v_mfma_f32_16x16x32_bf16 v[52:55], v[232:235], v[200:203], 0\n"
"v_exp_f32 v187, v187\n"
"v_mfma_f32_16x16x32_bf16 v[84:87], v[232:235], v[204:207], 0\n"
"s_waitcnt lgkmcnt(4)\n"
"ds_read_b128 v[232:235], v245 offset:16896\n"
"v_mfma_f32_16x16x32_bf16 v[88:91], v[236:239], v[204:207], 0\n"
"v_exp_f32 v188, v188\n"
"v_mfma_f32_16x16x32_bf16 v[56:59], v[236:239], v[200:203], 0\n"
"v_exp_f32 v189, v189\n"
"v_mfma_f32_16x16x32_bf16 v[24:27], v[236:239], v[196:199], 0\n"
"ds_read_b128 v[236:239], v244 offset:20480\n"
"v_cvt_pk_bf16_f32 v217, v182, v183\n"
"v_mfma_f32_16x16x32_bf16 v[28:31], v[240:243], v[196:199], 0\n"
"v_exp_f32 v190, v190\n"
"v_mfma_f32_16x16x32_bf16 v[60:63], v[240:243], v[200:203], 0\n"
"v_exp_f32 v191, v191\n"
"v_mfma_f32_16x16x32_bf16 v[92:95], v[240:243], v[204:207], 0\n"
"s_waitcnt lgkmcnt(3)\n"
"s_mov_b32 s87, 3\n"
"s_branch .LkLU2\n"
".LkLS0:\n"
"s_mov_b32 s86, 0x10000\n"
".LkLSL0:\n"
"ds_read_b32 v251, v252\n"
"v_mfma_f32_16x16x32_bf16 v[0:3], v[144:147], v[152:155], 0\n"
"v_mfma_f32_16x16x32_bf16 v[4:7], v[148:151], v[156:159], 0\n"
"v_mfma_f32_16x16x32_bf16 v[8:11], v[144:147], v[156:159], 0\n"
"v_mfma_f32_16x16x32_bf16 v[12:15], v[148:151], v[152:155], 0\n"
"s_waitcnt lgkmcnt(0)\n"
"v_cmp_gt_i32 vcc, v250, v251\n"
"s_cbranch_vccz .LkLR0\n"
"s_sub_u32 s86, s86, 1\n"
"s_cbranch_scc0 .LkLSL0\n"
"s_branch .LkLR0\n"
".LkLS1:\n"
"s_mov_b32 s86, 0x10000\n"
".LkLSL1:\n"
"ds_read_b32 v251, v252\n"
"s_waitcnt lgkmcnt(0)\n"
"v_cmp_gt_i32 vcc, v250, v251\n"
"s_cbranch_vccz .LkLR1\n"
"s_sub_u32 s86, s86, 1\n"
"s_cbranch_scc0 .LkLSL1\n"
"s_branch .LkLR1\n"
".LkLS2:\n"
"s_mov_b32 s86, 0x10000\n"
".LkLSL2:\n"
"ds_read_b32 v251, v252\n"
"s_waitcnt lgkmcnt(0)\n"
"v_cmp_gt_i32 vcc, v250, v251\n"
"s_cbranch_vccz .LkLR2\n"
"s_sub_u32 s86, s86, 1\n"
"s_cbranch_scc0 .LkLSL2\n"
"s_branch .LkLR2\n"
".LkLS3:\n"
"s_mov_b32 s86, 0x10000\n"
".LkLSL3:\n"
"ds_read_b32 v251, v252\n"
"s_waitcnt lgkmcnt(0)\n"
"v_cmp_gt_i32 vcc, v250, v251\n"
"s_cbranch_vccz .LkLR3\n"
"s_sub_u32 s86, s86, 1\n"
"s_cbranch_scc0 .LkLSL3\n"
"s_branch .LkLR3\n"
".LkLS4:\n"
"s_mov_b32 s86, 0x10000\n"
".LkLSL4:\n"
"ds_read_b32 v251, v252\n"
"s_waitcnt lgkmcnt(0)\n"
"v_cmp_gt_i32 vcc, v250, v251\n"
"s_cbranch_vccz .LkLR4\n"
"s_sub_u32 s86, s86, 1\n"
"s_cbranch_scc0 .LkLSL4\n"
"s_branch .LkLR4\n"
".LkLS5:\n"
"s_mov_b32 s86, 0x10000\n"
".LkLSL5:\n"
"ds_read_b32 v251, v252\n"
"s_waitcnt lgkmcnt(0)\n"
"v_cmp_gt_i32 vcc, v250, v251\n"
"s_cbranch_vccz .LkLR5\n"
"s_sub_u32 s86, s86, 1\n"
"s_cbranch_scc0 .LkLSL5\n"
"s_branch .LkLR5\n"
".LkLS6:\n"
"s_mov_b32 s86, 0x10000\n"
".LkLSL6:\n"
"ds_read_b32 v251, v252\n"
"s_waitcnt lgkmcnt(0)\n"
"v_cmp_gt_i32 vcc, v250, v251\n"
"s_cbranch_vccz .LkLR6\n"
"s_sub_u32 s86, s86, 1\n"
"s_cbranch_scc0 .LkLSL6\n"
"s_branch .LkLR6\n"
".LkLS7:\n"
"s_mov_b32 s86, 0x10000\n"
".LkLSL7:\n"
"ds_read_b32 v251, v252\n"
"s_waitcnt lgkmcnt(0)\n"
"v_cmp_gt_i32 vcc, v250, v251\n"
"s_cbranch_vccz .LkLR7\n"
"s_sub_u32 s86, s86, 1\n"
"s_cbranch_scc0 .LkLSL7\n"
"s_branch .LkLR7\n"
".LkLS8:\n"
"s_mov_b32 s86, 0x10000\n"
".LkLSL8:\n"
"ds_read_b32 v251, v252\n"
"s_waitcnt lgkmcnt(0)\n"
"v_cmp_gt_i32 vcc, v250, v251\n"
"s_cbranch_vccz .LkLR8\n"
"s_sub_u32 s86, s86, 1\n"
"s_cbranch_scc0 .LkLSL8\n"
"s_branch .LkLR8\n"
".LkLS9:\n"
"s_mov_b32 s86, 0x10000\n"
".LkLSL9:\n"
"ds_read_b32 v251, v252\n"
"s_waitcnt lgkmcnt(0)\n"
"v_cmp_gt_i32 vcc, v250, v251\n"
"s_cbranch_vccz .LkLR9\n"
"s_sub_u32 s86, s86, 1\n"
"s_cbranch_scc0 .LkLSL9\n"
"s_branch .LkLR9\n"
".LkLS10:\n"
"s_mov_b32 s86, 0x10000\n"
".LkLSL10:\n"
"ds_read_b32 v251, v252\n"
"s_waitcnt lgkmcnt(0)\n"
"v_cmp_gt_i32 vcc, v250, v251\n"
"s_cbranch_vccz .LkLR10\n"
"s_sub_u32 s86, s86, 1\n"
"s_cbranch_scc0 .LkLSL10\n"
"s_branch .LkLR10\n"
".LkLS11:\n"
"s_mov_b32 s86, 0x10000\n"
".LkLSL11:\n"
"ds_read_b32 v251, v252\n"
"s_waitcnt lgkmcnt(0)\n"
"v_cmp_gt_i32 vcc, v250, v251\n"
"s_cbranch_vccz .LkLR11\n"
"s_sub_u32 s86, s86, 1\n"
"s_cbranch_scc0 .LkLSL11\n"
"s_branch .LkLR11\n"
".LkLS12:\n"
"s_mov_b32 s86, 0x10000\n"
".LkLSL12:\n"
"ds_read_b32 v251, v252\n"
"s_waitcnt lgkmcnt(0)\n"
"v_cmp_gt_i32 vcc, v250, v251\n"
"s_cbranch_vccz .LkLR12\n"
"s_sub_u32 s86, s86, 1\n"
"s_cbranch_scc0 .LkLSL12\n"
"s_branch .LkLR12\n"
".LkLS13:\n"
"s_mov_b32 s86, 0x10000\n"
".LkLSL13:\n"
"ds_read_b32 v251, v252\n"
"s_waitcnt lgkmcnt(0)\n"
"v_cmp_gt_i32 vcc, v250, v251\n"
"s_cbranch_vccz .LkLR13\n"
"s_sub_u32 s86, s86, 1\n"
"s_cbranch_scc0 .LkLSL13\n"
"s_branch .LkLR13\n"
".LkLS14:\n"
"s_mov_b32 s86, 0x10000\n"
".LkLSL14:\n"
"ds_read_b32 v251, v252\n"
"s_waitcnt lgkmcnt(0)\n"
"v_cmp_gt_i32 vcc, v250, v251\n"
"s_cbranch_vccz .LkLR14\n"
"s_sub_u32 s86, s86, 1\n"
"s_cbranch_scc0 .LkLSL14\n"
"s_branch .LkLR14\n"
".LkLS15:\n"
"s_mov_b32 s86, 0x10000\n"
".LkLSL15:\n"
"ds_read_b32 v251, v252\n"
"s_waitcnt lgkmcnt(0)\n"
"v_cmp_gt_i32 vcc, v250, v251\n"
"s_cbranch_vccz .LkLR15\n"
"s_sub_u32 s86, s86, 1\n"
"s_cbranch_scc0 .LkLSL15\n"
"s_branch .LkLR15\n"
".LkLS16:\n"
"s_mov_b32 s86, 0x10000\n"
".LkLSL16:\n"
"ds_read_b32 v251, v252\n"
"s_waitcnt lgkmcnt(0)\n"
"v_cmp_gt_i32 vcc, v250, v251\n"
"s_cbranch_vccz .LkLR16\n"
"s_sub_u32 s86, s86, 1\n"
"s_cbranch_scc0 .LkLSL16\n"
"s_branch .LkLR16\n"
".LkLS17:\n"
"s_mov_b32 s86, 0x10000\n"
".LkLSL17:\n"
"ds_read_b32 v251, v252\n"
"s_waitcnt lgkmcnt(0)\n"
"v_cmp_gt_i32 vcc, v250, v251\n"
"s_cbranch_vccz .LkLR17\n"
"s_sub_u32 s86, s86, 1\n"
"s_cbranch_scc0 .LkLSL17\n"
"s_branch .LkLR17\n"
".LkLS18:\n"
"s_mov_b32 s86, 0x10000\n"
".LkLSL18:\n"
"ds_read_b32 v251, v252\n"
"s_waitcnt lgkmcnt(0)\n"
"v_cmp_gt_i32 vcc, v250, v251\n"
"s_cbranch_vccz .LkLR18\n"
"s_sub_u32 s86, s86, 1\n"
"s_cbranch_scc0 .LkLSL18\n"
"s_branch .LkLR18\n"
".LkLS19:\n"
"s_mov_b32 s86, 0x10000\n"
".LkLSL19:\n"
"ds_read_b32 v251, v252\n"
"s_waitcnt lgkmcnt(0)\n"
"v_cmp_gt_i32 vcc, v250, v251\n"
"s_cbranch_vccz .LkLR19\n"
"s_sub_u32 s86, s86, 1\n"
"s_cbranch_scc0 .LkLSL19\n"
"s_branch .LkLR19\n"
".LkLS20:\n"
"s_mov_b32 s86, 0x10000\n"
".LkLSL20:\n"
"ds_read_b32 v251, v252\n"
"s_waitcnt lgkmcnt(0)\n"
"v_cmp_gt_i32 vcc, v250, v251\n"
"s_cbranch_vccz .LkLR20\n"
"s_sub_u32 s86, s86, 1\n"
"s_cbranch_scc0 .LkLSL20\n"
"s_branch .LkLR20\n"
".LkLS21:\n"
"s_mov_b32 s86, 0x10000\n"
".LkLSL21:\n"
"ds_read_b32 v251, v252\n"
"s_waitcnt lgkmcnt(0)\n"
"v_cmp_gt_i32 vcc, v250, v251\n"
"s_cbranch_vccz .LkLR21\n"
"s_sub_u32 s86, s86, 1\n"
"s_cbranch_scc0 .LkLSL21\n"
"s_branch .LkLR21\n"
".LkLS22:\n"
"s_mov_b32 s86, 0x10000\n"
".LkLSL22:\n"
"ds_read_b32 v251, v252\n"
"s_waitcnt lgkmcnt(0)\n"
"v_cmp_gt_i32 vcc, v250, v251\n"
"s_cbranch_vccz .LkLR22\n"
"s_sub_u32 s86, s86, 1\n"
"s_cbranch_scc0 .LkLSL22\n"
"s_branch .LkLR22\n"
".LkLS23:\n"
"s_mov_b32 s86, 0x10000\n"
".LkLSL23:\n"
"ds_read_b32 v251, v252\n"
"s_waitcnt lgkmcnt(0)\n"
"v_cmp_gt_i32 vcc, v250, v251\n"
"s_cbranch_vccz .LkLR23\n"
"s_sub_u32 s86, s86, 1\n"
"s_cbranch_scc0 .LkLSL23\n"
"s_branch .LkLR23\n"
".LkLS24:\n"
"s_mov_b32 s86, 0x10000\n"
".LkLSL24:\n"
"ds_read_b32 v251, v252\n"
"s_waitcnt lgkmcnt(0)\n"
"v_cmp_gt_i32 vcc, v250, v251\n"
"s_cbranch_vccz .LkLR24\n"
"s_sub_u32 s86, s86, 1\n"
"s_cbranch_scc0 .LkLSL24\n"
"s_branch .LkLR24\n"
".LkLS25:\n"
"s_mov_b32 s86, 0x10000\n"
".LkLSL25:\n"
"ds_read_b32 v251, v252\n"
"s_waitcnt lgkmcnt(0)\n"
"v_cmp_gt_i32 vcc, v250, v251\n"
"s_cbranch_vccz .LkLR25\n"
"s_sub_u32 s86, s86, 1\n"
"s_cbranch_scc0 .LkLSL25\n"
"s_branch .LkLR25\n"
".LkLS26:\n"
"s_mov_b32 s86, 0x10000\n"
".LkLSL26:\n"
"ds_read_b32 v251, v252\n"
"s_waitcnt lgkmcnt(0)\n"
"v_cmp_gt_i32 vcc, v250, v251\n"
"s_cbranch_vccz .LkLR26\n"
"s_sub_u32 s86, s86, 1\n"
"s_cbranch_scc0 .LkLSL26\n"
"s_branch .LkLR26\n"
".LkLU0:\n"
"v_mfma_f32_16x16x32_bf16 v[172:175], v[220:223], v[96:99], v[156:159]\n"
"v_exp_f32 v192, v192\n"
"v_mfma_f32_16x16x32_bf16 v[176:179], v[220:223], v[112:115], v[160:163]\n"
"v_exp_f32 v193, v193\n"
"v_mfma_f32_16x16x32_bf16 v[180:183], v[220:223], v[128:131], v[164:167]\n"
"ds_read_b128 v[220:223], v245 offset:4096\n"
"v_cvt_pk_bf16_f32 v210, v184, v185\n"
"v_mfma_f32_16x16x32_bf16 v[172:175], v[224:227], v[100:103], v[172:175]\n"
"v_exp_f32 v194, v194\n"
"v_mfma_f32_16x16x32_bf16 v[176:179], v[224:227], v[116:119], v[176:179]\n"
"v_exp_f32 v195, v195\n"
"v_mfma_f32_16x16x32_bf16 v[180:183], v[224:227], v[132:135], v[180:183]\n"
"s_waitcnt lgkmcnt(2)\n"
"ds_read_b128 v[224:227], v244 offset:4608\n"
"v_mfma_f32_16x16x32_bf16 v[172:175], v[228:231], v[104:107], v[172:175]\n"
"v_cvt_pk_bf16_f32 v211, v186, v187\n"
"v_cvt_pk_bf16_f32 v214, v188, v189\n"
"v_mfma_f32_16x16x32_bf16 v[176:179], v[228:231], v[120:123], v[176:179]\n"
"v_mfma_f32_16x16x32_bf16 v[180:183], v[228:231], v[136:139], v[180:183]\n"
"ds_read_b128 v[228:231], v245 offset:4608\n"
"v_cvt_pk_bf16_f32 v215, v190, v191\n"
"v_mfma_f32_16x16x32_bf16 v[172:175], v[232:235], v[108:111], v[172:175]\n"
"v_add_u32 v250, 1, v250\n"
"v_cvt_pk_bf16_f32 v218, v192, v193\n"
"v_mfma_f32_16x16x32_bf16 v[176:179], v[232:235], v[124:127], v[176:179]\n"
"v_mfma_f32_16x16x32_bf16 v[180:183], v[232:235], v[140:143], v[180:183]\n"
"s_waitcnt lgkmcnt(2)\n"
"v_cvt_pk_bf16_f32 v219, v194, v195\n"
"v_mfma_f32_16x16x32_bf16 v[184:187], v[236:239], v[96:99], v[156:159]\n"
"ds_read_b64_tr_b16 v[232:233], v246 offset:57344\n"
"ds_read_b64_tr_b16 v[234:235], v246 offset:61440\n"
"v_mfma_f32_16x16x32_bf16 v[188:191], v[236:239], v[112:115], v[160:163]\n"
"v_exp_f32 v172, v172\n"
"v_mfma_f32_16x16x32_bf16 v[192:195], v[236:239], v[128:131], v[164:167]\n"
"v_exp_f32 v173, v173\n"
"v_mfma_f32_16x16x32_bf16 v[184:187], v[220:223], v[100:103], v[184:187]\n"
"ds_read_b64_tr_b16 v[236:237], v247 offset:57344\n"
"ds_read_b64_tr_b16 v[238:239], v247 offset:61440\n"
"v_mfma_f32_16x16x32_bf16 v[188:191], v[220:223], v[116:119], v[188:191]\n"
"v_exp_f32 v174, v174\n"
"v_mfma_f32_16x16x32_bf16 v[192:195], v[220:223], v[132:135], v[192:195]\n"
"s_waitcnt lgkmcnt(4)\n"
"v_cvt_pk_bf16_f32 v196, v172, v173\n"
"v_mfma_f32_16x16x32_bf16 v[184:187], v[224:227], v[104:107], v[184:187]\n"
"ds_read_b64_tr_b16 v[220:221], v248 offset:57344\n"
"ds_read_b64_tr_b16 v[222:223], v248 offset:61440\n"
"v_mfma_f32_16x16x32_bf16 v[188:191], v[224:227], v[120:123], v[188:191]\n"
"v_exp_f32 v175, v175\n"
"v_mfma_f32_16x16x32_bf16 v[192:195], v[224:227], v[136:139], v[192:195]\n"
"v_exp_f32 v176, v176\n"
"v_mfma_f32_16x16x32_bf16 v[184:187], v[228:231], v[108:111], v[184:187]\n"
"ds_read_b64_tr_b16 v[224:225], v249 offset:57344\n"
"ds_read_b64_tr_b16 v[226:227], v249 offset:61440\n"
"v_mfma_f32_16x16x32_bf16 v[188:191], v[228:231], v[124:127], v[188:191]\n"
"v_exp_f32 v177, v177\n"
"v_mfma_f32_16x16x32_bf16 v[192:195], v[228:231], v[140:143], v[192:195]\n"
"v_exp_f32 v178, v178\n"
"v_mfma_f32_16x16x32_bf16 v[144:147], v[168:171], v[208:211], v[144:147]\n"
"ds_read_b64_tr_b16 v[228:229], v246 offset:57856\n"
"ds_read_b64_tr_b16 v[230:231], v246 offset:61952\n"
"v_mfma_f32_16x16x32_bf16 v[148:151], v[168:171], v[212:215], v[148:151]\n"
"v_exp_f32 v179, v179\n"
"v_mfma_f32_16x16x32_bf16 v[152:155], v[168:171], v[216:219], v[152:155]\n"
"s_waitcnt lgkmcnt(6)\n"
"v_cvt_pk_bf16_f32 v197, v174, v175\n"
"v_mfma_f32_16x16x32_bf16 v[64:67], v[232:235], v[216:219], v[64:67]\n"
"v_exp_f32 v180, v180\n"
"v_mfma_f32_16x16x32_bf16 v[32:35], v[232:235], v[212:215], v[32:35]\n"
"v_exp_f32 v181, v181\n"
"v_mfma_f32_16x16x32_bf16 v[0:3], v[232:235], v[208:211], v[0:3]\n"
"ds_read_b64_tr_b16 v[232:233], v247 offset:57856\n"
"ds_read_b64_tr_b16 v[234:235], v247 offset:61952\n"
"v_mfma_f32_16x16x32_bf16 v[4:7], v[236:239], v[208:211], v[4:7]\n"
"ds_read_b32 v251, v252\n"
"v_cvt_pk_bf16_f32 v200, v176, v177\n"
"v_mfma_f32_16x16x32_bf16 v[36:39], v[236:239], v[212:215], v[36:39]\n"
"v_exp_f32 v182, v182\n"
"v_mfma_f32_16x16x32_bf16 v[68:71], v[236:239], v[216:219], v[68:71]\n"
"s_waitcnt lgkmcnt(5)\n"
"v_cvt_pk_bf16_f32 v201, v178, v179\n"
"v_mfma_f32_16x16x32_bf16 v[72:75], v[220:223], v[216:219], v[72:75]\n"
"ds_read_b64_tr_b16 v[236:237], v248 offset:57856\n"
"ds_read_b64_tr_b16 v[238:239], v248 offset:61952\n"
"v_mfma_f32_16x16x32_bf16 v[40:43], v[220:223], v[212:215], v[40:43]\n"
"v_exp_f32 v183, v183\n"
"v_mfma_f32_16x16x32_bf16 v[8:11], v[220:223], v[208:211], v[8:11]\n"
"ds_read_b64_tr_b16 v[240:241], v249 offset:57856\n"
"ds_read_b64_tr_b16 v[242:243], v249 offset:61952\n"
"v_mfma_f32_16x16x32_bf16 v[12:15], v[224:227], v[208:211], v[12:15]\n"
"v_exp_f32 v184, v184\n"
"v_mfma_f32_16x16x32_bf16 v[44:47], v[224:227], v[212:215], v[44:47]\n"
"v_exp_f32 v185, v185\n"
"v_mfma_f32_16x16x32_bf16 v[76:79], v[224:227], v[216:219], v[76:79]\n"
"s_waitcnt lgkmcnt(4)\n"
"v_cmp_gt_i32 vcc, v250, v251\n"
"v_mfma_f32_16x16x32_bf16 v[80:83], v[228:231], v[216:219], v[80:83]\n"
"s_cbranch_vccnz .LkLS19\n"
".LkLR19:\n"
"ds_read_b128 v[220:223], v244 offset:8192\n"
"v_mfma_f32_16x16x32_bf16 v[48:51], v[228:231], v[212:215], v[48:51]\n"
"v_exp_f32 v186, v186\n"
"v_mfma_f32_16x16x32_bf16 v[16:19], v[228:231], v[208:211], v[16:19]\n"
"ds_read_b128 v[224:227], v245 offset:8192\n"
"ds_read_b128 v[228:231], v244 offset:8704\n"
"v_mfma_f32_16x16x32_bf16 v[20:23], v[232:235], v[208:211], v[20:23]\n"
"v_cvt_pk_bf16_f32 v204, v180, v181\n"
"v_mfma_f32_16x16x32_bf16 v[52:55], v[232:235], v[212:215], v[52:55]\n"
"v_exp_f32 v187, v187\n"
"v_mfma_f32_16x16x32_bf16 v[84:87], v[232:235], v[216:219], v[84:87]\n"
"s_waitcnt lgkmcnt(3)\n"
"ds_read_b128 v[232:235], v245 offset:8704\n"
"v_mfma_f32_16x16x32_bf16 v[88:91], v[236:239], v[216:219], v[88:91]\n"
"v_exp_f32 v188, v188\n"
"v_mfma_f32_16x16x32_bf16 v[56:59], v[236:239], v[212:215], v[56:59]\n"
"v_exp_f32 v189, v189\n"
"v_mfma_f32_16x16x32_bf16 v[24:27], v[236:239], v[208:211], v[24:27]\n"
"ds_read_b128 v[236:239], v244 offset:12288\n"
"v_cvt_pk_bf16_f32 v205, v182, v183\n"
"v_mfma_f32_16x16x32_bf16 v[28:31], v[240:243], v[208:211], v[28:31]\n"
"v_exp_f32 v190, v190\n"
"v_mfma_f32_16x16x32_bf16 v[60:63], v[240:243], v[212:215], v[60:63]\n"
"v_exp_f32 v191, v191\n"
"v_mfma_f32_16x16x32_bf16 v[92:95], v[240:243], v[216:219], v[92:95]\n"
"s_waitcnt lgkmcnt(3)\n"
".LkLU1:\n"
"v_mfma_f32_16x16x32_bf16 v[172:175], v[220:223], v[96:99], v[156:159]\n"
"v_exp_f32 v192, v192\n"
"v_mfma_f32_16x16x32_bf16 v[176:179], v[220:223], v[112:115], v[160:163]\n"
"v_exp_f32 v193, v193\n"
"v_mfma_f32_16x16x32_bf16 v[180:183], v[220:223], v[128:131], v[164:167]\n"
"ds_read_b128 v[220:223], v245 offset:12288\n"
"v_cvt_pk_bf16_f32 v198, v184, v185\n"
"v_mfma_f32_16x16x32_bf16 v[172:175], v[224:227], v[100:103], v[172:175]\n"
"v_exp_f32 v194, v194\n"
"v_mfma_f32_16x16x32_bf16 v[176:179], v[224:227], v[116:119], v[176:179]\n"
"v_exp_f32 v195, v195\n"
"v_mfma_f32_16x16x32_bf16 v[180:183], v[224:227], v[132:135], v[180:183]\n"
"s_waitcnt lgkmcnt(2)\n"
"ds_read_b128 v[224:227], v244 offset:12800\n"
"v_mfma_f32_16x16x32_bf16 v[172:175], v[228:231], v[104:107], v[172:175]\n"
"v_cvt_pk_bf16_f32 v199, v186, v187\n"
"v_cvt_pk_bf16_f32 v202, v188, v189\n"
"v_mfma_f32_16x16x32_bf16 v[176:179], v[228:231], v[120:123], v[176:179]\n"
"v_mfma_f32_16x16x32_bf16 v[180:183], v[228:231], v[136:139], v[180:183]\n"
"ds_read_b128 v[228:231], v245 offset:12800\n"
"v_cvt_pk_bf16_f32 v203, v190, v191\n"
"v_mfma_f32_16x16x32_bf16 v[172:175], v[232:235], v[108:111], v[172:175]\n"
"v_add_u32 v250, 1, v250\n"
"v_cvt_pk_bf16_f32 v206, v192, v193\n"
"v_mfma_f32_16x16x32_bf16 v[176:179], v[232:235], v[124:127], v[176:179]\n"
"v_mfma_f32_16x16x32_bf16 v[180:183], v[232:235], v[140:143], v[180:183]\n"
"s_waitcnt lgkmcnt(2)\n"
"v_cvt_pk_bf16_f32 v207, v194, v195\n"
"v_mfma_f32_16x16x32_bf16 v[184:187], v[236:239], v[96:99], v[156:159]\n"
"ds_read_b64_tr_b16 v[232:233], v246 offset:0\n"
"ds_read_b64_tr_b16 v[234:235], v246 offset:4096\n"
"v_mfma_f32_16x16x32_bf16 v[188:191], v[236:239], v[112:115], v[160:163]\n"
"v_exp_f32 v172, v172\n"
"v_mfma_f32_16x16x32_bf16 v[192:195], v[236:239], v[128:131], v[164:167]\n"
"v_exp_f32 v173, v173\n"
"v_mfma_f32_16x16x32_bf16 v[184:187], v[220:223], v[100:103], v[184:187]\n"
"ds_read_b64_tr_b16 v[236:237], v247 offset:0\n"
"ds_read_b64_tr_b16 v[238:239], v247 offset:4096\n"
"v_mfma_f32_16x16x32_bf16 v[188:191], v[220:223], v[116:119], v[188:191]\n"
"v_exp_f32 v174, v174\n"
"v_mfma_f32_16x16x32_bf16 v[192:195], v[220:223], v[132:135], v[192:195]\n"
"s_waitcnt lgkmcnt(4)\n"
"v_cvt_pk_bf16_f32 v208, v172, v173\n"
"v_mfma_f32_16x16x32_bf16 v[184:187], v[224:227], v[104:107], v[184:187]\n"
"ds_read_b64_tr_b16 v[220:221], v248 offset:0\n"
"ds_read_b64_tr_b16 v[222:223], v248 offset:4096\n"
"v_mfma_f32_16x16x32_bf16 v[188:191], v[224:227], v[120:123], v[188:191]\n"
"v_exp_f32 v175, v175\n"
"v_mfma_f32_16x16x32_bf16 v[192:195], v[224:227], v[136:139], v[192:195]\n"
"v_exp_f32 v176, v176\n"
"v_mfma_f32_16x16x32_bf16 v[184:187], v[228:231], v[108:111], v[184:187]\n"
"ds_read_b64_tr_b16 v[224:225], v249 offset:0\n"
"ds_read_b64_tr_b16 v[226:227], v249 offset:4096\n"
"v_mfma_f32_16x16x32_bf16 v[188:191], v[228:231], v[124:127], v[188:191]\n"
"v_exp_f32 v177, v177\n"
"v_mfma_f32_16x16x32_bf16 v[192:195], v[228:231], v[140:143], v[192:195]\n"
"v_exp_f32 v178, v178\n"
"v_mfma_f32_16x16x32_bf16 v[144:147], v[168:171], v[196:199], v[144:147]\n"
"ds_read_b64_tr_b16 v[228:229], v246 offset:512\n"
"ds_read_b64_tr_b16 v[230:231], v246 offset:4608\n"
"v_mfma_f32_16x16x32_bf16 v[148:151], v[168:171], v[200:203], v[148:151]\n"
"v_exp_f32 v179, v179\n"
"v_mfma_f32_16x16x32_bf16 v[152:155], v[168:171], v[204:207], v[152:155]\n"
"s_waitcnt lgkmcnt(6)\n"
"v_cvt_pk_bf16_f32 v209, v174, v175\n"
"v_mfma_f32_16x16x32_bf16 v[64:67], v[232:235], v[204:207], v[64:67]\n"
"v_exp_f32 v180, v180\n"
"v_mfma_f32_16x16x32_bf16 v[32:35], v[232:235], v[200:203], v[32:35]\n"
"v_exp_f32 v181, v181\n"
"v_mfma_f32_16x16x32_bf16 v[0:3], v[232:235], v[196:199], v[0:3]\n"
"ds_read_b64_tr_b16 v[232:233], v247 offset:512\n"
"ds_read_b64_tr_b16 v[234:235], v247 offset:4608\n"
"v_mfma_f32_16x16x32_bf16 v[4:7], v[236:239], v[196:199], v[4:7]\n"
"ds_read_b32 v251, v252\n"
"v_cvt_pk_bf16_f32 v212, v176, v177\n"
"v_mfma_f32_16x16x32_bf16 v[36:39], v[236:239], v[200:203], v[36:39]\n"
"v_exp_f32 v182, v182\n"
"v_mfma_f32_16x16x32_bf16 v[68:71], v[236:239], v[204:207], v[68:71]\n"
"s_waitcnt lgkmcnt(5)\n"
"v_cvt_pk_bf16_f32 v213, v178, v179\n"
"v_mfma_f32_16x16x32_bf16 v[72:75], v[220:223], v[204:207], v[72:75]\n"
"ds_read_b64_tr_b16 v[236:237], v248 offset:512\n"
"ds_read_b64_tr_b16 v[238:239], v248 offset:4608\n"
"v_mfma_f32_16x16x32_bf16 v[40:43], v[220:223], v[200:203], v[40:43]\n"
"v_exp_f32 v183, v183\n"
"v_mfma_f32_16x16x32_bf16 v[8:11], v[220:223], v[196:199], v[8:11]\n"
"ds_read_b64_tr_b16 v[240:241], v249 offset:512\n"
"ds_read_b64_tr_b16 v[242:243], v249 offset:4608\n"
"v_mfma_f32_16x16x32_bf16 v[12:15], v[224:227], v[196:199], v[12:15]\n"
"ds_write_addtid_b32 v250 offset:0\n"
"v_exp_f32 v184, v184\n"
"v_mfma_f32_16x16x32_bf16 v[44:47], v[224:227], v[200:203], v[44:47]\n"
"v_exp_f32 v185, v185\n"
"v_mfma_f32_16x16x32_bf16 v[76:79], v[224:227], v[204:207], v[76:79]\n"
"s_waitcnt lgkmcnt(5)\n"
"v_cmp_gt_i32 vcc, v250, v251\n"
"v_mfma_f32_16x16x32_bf16 v[80:83], v[228:231], v[204:207], v[80:83]\n"
"s_cbranch_vccnz .LkLS20\n"
".LkLR20:\n"
"ds_read_b128 v[220:223], v244 offset:16384\n"
"v_mfma_f32_16x16x32_bf16 v[48:51], v[228:231], v[200:203], v[48:51]\n"
"v_exp_f32 v186, v186\n"
"v_mfma_f32_16x16x32_bf16 v[16:19], v[228:231], v[196:199], v[16:19]\n"
"ds_read_b128 v[224:227], v245 offset:16384\n"
"ds_read_b128 v[228:231], v244 offset:16896\n"
"v_mfma_f32_16x16x32_bf16 v[20:23], v[232:235], v[196:199], v[20:23]\n"
"v_cvt_pk_bf16_f32 v216, v180, v181\n"
"v_mfma_f32_16x16x32_bf16 v[52:55], v[232:235], v[200:203], v[52:55]\n"
"v_exp_f32 v187, v187\n"
"v_mfma_f32_16x16x32_bf16 v[84:87], v[232:235], v[204:207], v[84:87]\n"
"s_waitcnt lgkmcnt(4)\n"
"ds_read_b128 v[232:235], v245 offset:16896\n"
"v_mfma_f32_16x16x32_bf16 v[88:91], v[236:239], v[204:207], v[88:91]\n"
"v_exp_f32 v188, v188\n"
"v_mfma_f32_16x16x32_bf16 v[56:59], v[236:239], v[200:203], v[56:59]\n"
"v_exp_f32 v189, v189\n"
"v_mfma_f32_16x16x32_bf16 v[24:27], v[236:239], v[196:199], v[24:27]\n"
"ds_read_b128 v[236:239], v244 offset:20480\n"
"v_cvt_pk_bf16_f32 v217, v182, v183\n"
"v_mfma_f32_16x16x32_bf16 v[28:31], v[240:243], v[196:199], v[28:31]\n"
"v_exp_f32 v190, v190\n"
"v_mfma_f32_16x16x32_bf16 v[60:63], v[240:243], v[200:203], v[60:63]\n"
"v_exp_f32 v191, v191\n"
"v_mfma_f32_16x16x32_bf16 v[92:95], v[240:243], v[204:207], v[92:95]\n"
"s_waitcnt lgkmcnt(3)\n"
".LkLU2:\n"
"v_mfma_f32_16x16x32_bf16 v[172:175], v[220:223], v[96:99], v[156:159]\n"
"v_exp_f32 v192, v192\n"
"v_mfma_f32_16x16x32_bf16 v[176:179], v[220:223], v[112:115], v[160:163]\n"
"v_exp_f32 v193, v193\n"
"v_mfma_f32_16x16x32_bf16 v[180:183], v[220:223], v[128:131], v[164:167]\n"
"ds_read_b128 v[220:223], v245 offset:20480\n"
"v_cvt_pk_bf16_f32 v210, v184, v185\n"
"v_mfma_f32_16x16x32_bf16 v[172:175], v[224:227], v[100:103], v[172:175]\n"
"v_exp_f32 v194, v194\n"
"v_mfma_f32_16x16x32_bf16 v[176:179], v[224:227], v[116:119], v[176:179]\n"
"v_exp_f32 v195, v195\n"
"v_mfma_f32_16x16x32_bf16 v[180:183], v[224:227], v[132:135], v[180:183]\n"
"s_waitcnt lgkmcnt(2)\n"
"ds_read_b128 v[224:227], v244 offset:20992\n"
"v_mfma_f32_16x16x32_bf16 v[172:175], v[228:231], v[104:107], v[172:175]\n"
"v_cvt_pk_bf16_f32 v211, v186, v187\n"
"v_cvt_pk_bf16_f32 v214, v188, v189\n"
"v_mfma_f32_16x16x32_bf16 v[176:179], v[228:231], v[120:123], v[176:179]\n"
"v_mfma_f32_16x16x32_bf16 v[180:183], v[228:231], v[136:139], v[180:183]\n"
"ds_read_b128 v[228:231], v245 offset:20992\n"
"v_cvt_pk_bf16_f32 v215, v190, v191\n"
"v_mfma_f32_16x16x32_bf16 v[172:175], v[232:235], v[108:111], v[172:175]\n"
"v_add_u32 v250, 1, v250\n"
"v_cvt_pk_bf16_f32 v218, v192, v193\n"
"v_mfma_f32_16x16x32_bf16 v[176:179], v[232:235], v[124:127], v[176:179]\n"
"v_mfma_f32_16x16x32_bf16 v[180:183], v[232:235], v[140:143], v[180:183]\n"
"s_waitcnt lgkmcnt(2)\n"
"v_cvt_pk_bf16_f32 v219, v194, v195\n"
"v_mfma_f32_16x16x32_bf16 v[184:187], v[236:239], v[96:99], v[156:159]\n"
"ds_read_b64_tr_b16 v[232:233], v246 offset:8192\n"
"ds_read_b64_tr_b16 v[234:235], v246 offset:12288\n"
"v_mfma_f32_16x16x32_bf16 v[188:191], v[236:239], v[112:115], v[160:163]\n"
"v_exp_f32 v172, v172\n"
"v_mfma_f32_16x16x32_bf16 v[192:195], v[236:239], v[128:131], v[164:167]\n"
"v_exp_f32 v173, v173\n"
"v_mfma_f32_16x16x32_bf16 v[184:187], v[220:223], v[100:103], v[184:187]\n"
"ds_read_b64_tr_b16 v[236:237], v247 offset:8192\n"
"ds_read_b64_tr_b16 v[238:239], v247 offset:12288\n"
"v_mfma_f32_16x16x32_bf16 v[188:191], v[220:223], v[116:119], v[188:191]\n"
"v_exp_f32 v174, v174\n"
"v_mfma_f32_16x16x32_bf16 v[192:195], v[220:223], v[132:135], v[192:195]\n"
"s_waitcnt lgkmcnt(4)\n"
"v_cvt_pk_bf16_f32 v196, v172, v173\n"
"v_mfma_f32_16x16x32_bf16 v[184:187], v[224:227], v[104:107], v[184:187]\n"
"ds_read_b64_tr_b16 v[220:221], v248 offset:8192\n"
"ds_read_b64_tr_b16 v[222:223], v248 offset:12288\n"
"v_mfma_f32_16x16x32_bf16 v[188:191], v[224:227], v[120:123], v[188:191]\n"
"v_exp_f32 v175, v175\n"
"v_mfma_f32_16x16x32_bf16 v[192:195], v[224:227], v[136:139], v[192:195]\n"
"v_exp_f32 v176, v176\n"
"v_mfma_f32_16x16x32_bf16 v[184:187], v[228:231], v[108:111], v[184:187]\n"
"ds_read_b64_tr_b16 v[224:225], v249 offset:8192\n"
"ds_read_b64_tr_b16 v[226:227], v249 offset:12288\n"
"v_mfma_f32_16x16x32_bf16 v[188:191], v[228:231], v[124:127], v[188:191]\n"
"v_exp_f32 v177, v177\n"
"v_mfma_f32_16x16x32_bf16 v[192:195], v[228:231], v[140:143], v[192:195]\n"
"v_exp_f32 v178, v178\n"
"v_mfma_f32_16x16x32_bf16 v[144:147], v[168:171], v[208:211], v[144:147]\n"
"ds_read_b64_tr_b16 v[228:229], v246 offset:8704\n"
"ds_read_b64_tr_b16 v[230:231], v246 offset:12800\n"
"v_mfma_f32_16x16x32_bf16 v[148:151], v[168:171], v[212:215], v[148:151]\n"
"v_exp_f32 v179, v179\n"
"v_mfma_f32_16x16x32_bf16 v[152:155], v[168:171], v[216:219], v[152:155]\n"
"s_waitcnt lgkmcnt(6)\n"
"v_cvt_pk_bf16_f32 v197, v174, v175\n"
"v_mfma_f32_16x16x32_bf16 v[64:67], v[232:235], v[216:219], v[64:67]\n"
"v_exp_f32 v180, v180\n"
"v_mfma_f32_16x16x32_bf16 v[32:35], v[232:235], v[212:215], v[32:35]\n"
"v_exp_f32 v181, v181\n"
"v_mfma_f32_16x16x32_bf16 v[0:3], v[232:235], v[208:211], v[0:3]\n"
"ds_read_b64_tr_b16 v[232:233], v247 offset:8704\n"
"ds_read_b64_tr_b16 v[234:235], v247 offset:12800\n"
"v_mfma_f32_16x16x32_bf16 v[4:7], v[236:239], v[208:211], v[4:7]\n"
"ds_read_b32 v251, v252\n"
"v_cvt_pk_bf16_f32 v200, v176, v177\n"
"v_mfma_f32_16x16x32_bf16 v[36:39], v[236:239], v[212:215], v[36:39]\n"
"v_exp_f32 v182, v182\n"
"v_mfma_f32_16x16x32_bf16 v[68:71], v[236:239], v[216:219], v[68:71]\n"
"s_waitcnt lgkmcnt(5)\n"
"v_cvt_pk_bf16_f32 v201, v178, v179\n"
"v_mfma_f32_16x16x32_bf16 v[72:75], v[220:223], v[216:219], v[72:75]\n"
"ds_read_b64_tr_b16 v[236:237], v248 offset:8704\n"
"ds_read_b64_tr_b16 v[238:239], v248 offset:12800\n"
"v_mfma_f32_16x16x32_bf16 v[40:43], v[220:223], v[212:215], v[40:43]\n"
"v_exp_f32 v183, v183\n"
"v_mfma_f32_16x16x32_bf16 v[8:11], v[220:223], v[208:211], v[8:11]\n"
"ds_read_b64_tr_b16 v[240:241], v249 offset:8704\n"
"ds_read_b64_tr_b16 v[242:243], v249 offset:12800\n"
"v_mfma_f32_16x16x32_bf16 v[12:15], v[224:227], v[208:211], v[12:15]\n"
"v_exp_f32 v184, v184\n"
"v_mfma_f32_16x16x32_bf16 v[44:47], v[224:227], v[212:215], v[44:47]\n"
"v_exp_f32 v185, v185\n"
"v_mfma_f32_16x16x32_bf16 v[76:79], v[224:227], v[216:219], v[76:79]\n"
"s_waitcnt lgkmcnt(4)\n"
"v_cmp_gt_i32 vcc, v250, v251\n"
"v_mfma_f32_16x16x32_bf16 v[80:83], v[228:231], v[216:219], v[80:83]\n"
"s_cbranch_vccnz .LkLS21\n"
".LkLR21:\n"
"ds_read_b128 v[220:223], v244 offset:24576\n"
"v_mfma_f32_16x16x32_bf16 v[48:51], v[228:231], v[212:215], v[48:51]\n"
"v_exp_f32 v186, v186\n"
"v_mfma_f32_16x16x32_bf16 v[16:19], v[228:231], v[208:211], v[16:19]\n"
"ds_read_b128 v[224:227], v245 offset:24576\n"
"ds_read_b128 v[228:231], v244 offset:25088\n"
"v_mfma_f32_16x16x32_bf16 v[20:23], v[232:235], v[208:211], v[20:23]\n"
"v_cvt_pk_bf16_f32 v204, v180, v181\n"
"v_mfma_f32_16x16x32_bf16 v[52:55], v[232:235], v[212:215], v[52:55]\n"
"v_exp_f32 v187, v187\n"
"v_mfma_f32_16x16x32_bf16 v[84:87], v[232:235], v[216:219], v[84:87]\n"
"s_waitcnt lgkmcnt(3)\n"
"ds_read_b128 v[232:235], v245 offset:25088\n"
"v_mfma_f32_16x16x32_bf16 v[88:91], v[236:239], v[216:219], v[88:91]\n"
"v_exp_f32 v188, v188\n"
"v_mfma_f32_16x16x32_bf16 v[56:59], v[236:239], v[212:215], v[56:59]\n"
"v_exp_f32 v189, v189\n"
"v_mfma_f32_16x16x32_bf16 v[24:27], v[236:239], v[208:211], v[24:27]\n"
"ds_read_b128 v[236:239], v244 offset:28672\n"
"v_cvt_pk_bf16_f32 v205, v182, v183\n"
"v_mfma_f32_16x16x32_bf16 v[28:31], v[240:243], v[208:211], v[28:31]\n"
"v_exp_f32 v190, v190\n"
"v_mfma_f32_16x16x32_bf16 v[60:63], v[240:243], v[212:215], v[60:63]\n"
"v_exp_f32 v191, v191\n"
"v_mfma_f32_16x16x32_bf16 v[92:95], v[240:243], v[216:219], v[92:95]\n"
"s_waitcnt lgkmcnt(3)\n"
".LkLU3:\n"
"v_mfma_f32_16x16x32_bf16 v[172:175], v[220:223], v[96:99], v[156:159]\n"
"v_exp_f32 v192, v192\n"
"v_mfma_f32_16x16x32_bf16 v[176:179], v[220:223], v[112:115], v[160:163]\n"
"v_exp_f32 v193, v193\n"
"v_mfma_f32_16x16x32_bf16 v[180:183], v[220:223], v[128:131], v[164:167]\n"
"ds_read_b128 v[220:223], v245 offset:28672\n"
"v_cvt_pk_bf16_f32 v198, v184, v185\n"
"v_mfma_f32_16x16x32_bf16 v[172:175], v[224:227], v[100:103], v[172:175]\n"
"v_exp_f32 v194, v194\n"
"v_mfma_f32_16x16x32_bf16 v[176:179], v[224:227], v[116:119], v[176:179]\n"
"v_exp_f32 v195, v195\n"
"v_mfma_f32_16x16x32_bf16 v[180:183], v[224:227], v[132:135], v[180:183]\n"
"s_waitcnt lgkmcnt(2)\n"
"ds_read_b128 v[224:227], v244 offset:29184\n"
"v_mfma_f32_16x16x32_bf16 v[172:175], v[228:231], v[104:107], v[172:175]\n"
"v_cvt_pk_bf16_f32 v199, v186, v187\n"
"v_cvt_pk_bf16_f32 v202, v188, v189\n"
"v_mfma_f32_16x16x32_bf16 v[176:179], v[228:231], v[120:123], v[176:179]\n"
"v_mfma_f32_16x16x32_bf16 v[180:183], v[228:231], v[136:139], v[180:183]\n"
"ds_read_b128 v[228:231], v245 offset:29184\n"
"v_cvt_pk_bf16_f32 v203, v190, v191\n"
"v_mfma_f32_16x16x32_bf16 v[172:175], v[232:235], v[108:111], v[172:175]\n"
"v_add_u32 v250, 1, v250\n"
"v_cvt_pk_bf16_f32 v206, v192, v193\n"
"v_mfma_f32_16x16x32_bf16 v[176:179], v[232:235], v[124:127], v[176:179]\n"
"v_mfma_f32_16x16x32_bf16 v[180:183], v[232:235], v[140:143], v[180:183]\n"
"s_waitcnt lgkmcnt(2)\n"
"v_cvt_pk_bf16_f32 v207, v194, v195\n"
"v_mfma_f32_16x16x32_bf16 v[184:187], v[236:239], v[96:99], v[156:159]\n"
"ds_read_b64_tr_b16 v[232:233], v246 offset:16384\n"
"ds_read_b64_tr_b16 v[234:235], v246 offset:20480\n"
"v_mfma_f32_16x16x32_bf16 v[188:191], v[236:239], v[112:115], v[160:163]\n"
"v_exp_f32 v172, v172\n"
"v_mfma_f32_16x16x32_bf16 v[192:195], v[236:239], v[128:131], v[164:167]\n"
"v_exp_f32 v173, v173\n"
"v_mfma_f32_16x16x32_bf16 v[184:187], v[220:223], v[100:103], v[184:187]\n"
"ds_read_b64_tr_b16 v[236:237], v247 offset:16384\n"
"ds_read_b64_tr_b16 v[238:239], v247 offset:20480\n"
"v_mfma_f32_16x16x32_bf16 v[188:191], v[220:223], v[116:119], v[188:191]\n"
"v_exp_f32 v174, v174\n"
"v_mfma_f32_16x16x32_bf16 v[192:195], v[220:223], v[132:135], v[192:195]\n"
"s_waitcnt lgkmcnt(4)\n"
"v_cvt_pk_bf16_f32 v208, v172, v173\n"
"v_mfma_f32_16x16x32_bf16 v[184:187], v[224:227], v[104:107], v[184:187]\n"
"ds_read_b64_tr_b16 v[220:221], v248 offset:16384\n"
"ds_read_b64_tr_b16 v[222:223], v248 offset:20480\n"
"v_mfma_f32_16x16x32_bf16 v[188:191], v[224:227], v[120:123], v[188:191]\n"
"v_exp_f32 v175, v175\n"
"v_mfma_f32_16x16x32_bf16 v[192:195], v[224:227], v[136:139], v[192:195]\n"
"v_exp_f32 v176, v176\n"
"v_mfma_f32_16x16x32_bf16 v[184:187], v[228:231], v[108:111], v[184:187]\n"
"ds_read_b64_tr_b16 v[224:225], v249 offset:16384\n"
"ds_read_b64_tr_b16 v[226:227], v249 offset:20480\n"
"v_mfma_f32_16x16x32_bf16 v[188:191], v[228:231], v[124:127], v[188:191]\n"
"v_exp_f32 v177, v177\n"
"v_mfma_f32_16x16x32_bf16 v[192:195], v[228:231], v[140:143], v[192:195]\n"
"v_exp_f32 v178, v178\n"
"v_mfma_f32_16x16x32_bf16 v[144:147], v[168:171], v[196:199], v[144:147]\n"
"ds_read_b64_tr_b16 v[228:229], v246 offset:16896\n"
"ds_read_b64_tr_b16 v[230:231], v246 offset:20992\n"
"v_mfma_f32_16x16x32_bf16 v[148:151], v[168:171], v[200:203], v[148:151]\n"
"v_exp_f32 v179, v179\n"
"v_mfma_f32_16x16x32_bf16 v[152:155], v[168:171], v[204:207], v[152:155]\n"
"s_waitcnt lgkmcnt(6)\n"
"v_cvt_pk_bf16_f32 v209, v174, v175\n"
"v_mfma_f32_16x16x32_bf16 v[64:67], v[232:235], v[204:207], v[64:67]\n"
"v_exp_f32 v180, v180\n"
"v_mfma_f32_16x16x32_bf16 v[32:35], v[232:235], v[200:203], v[32:35]\n"
"v_exp_f32 v181, v181\n"
"v_mfma_f32_16x16x32_bf16 v[0:3], v[232:235], v[196:199], v[0:3]\n"
"ds_read_b64_tr_b16 v[232:233], v247 offset:16896\n"
"ds_read_b64_tr_b16 v[234:235], v247 offset:20992\n"
"v_mfma_f32_16x16x32_bf16 v[4:7], v[236:239], v[196:199], v[4:7]\n"
"ds_read_b32 v251, v252\n"
"v_cvt_pk_bf16_f32 v212, v176, v177\n"
"v_mfma_f32_16x16x32_bf16 v[36:39], v[236:239], v[200:203], v[36:39]\n"
"v_exp_f32 v182, v182\n"
"v_mfma_f32_16x16x32_bf16 v[68:71], v[236:239], v[204:207], v[68:71]\n"
"s_waitcnt lgkmcnt(5)\n"
"v_cvt_pk_bf16_f32 v213, v178, v179\n"
"v_mfma_f32_16x16x32_bf16 v[72:75], v[220:223], v[204:207], v[72:75]\n"
"ds_read_b64_tr_b16 v[236:237], v248 offset:16896\n"
"ds_read_b64_tr_b16 v[238:239], v248 offset:20992\n"
"v_mfma_f32_16x16x32_bf16 v[40:43], v[220:223], v[200:203], v[40:43]\n"
"v_exp_f32 v183, v183\n"
"v_mfma_f32_16x16x32_bf16 v[8:11], v[220:223], v[196:199], v[8:11]\n"
"ds_read_b64_tr_b16 v[240:241], v249 offset:16896\n"
"ds_read_b64_tr_b16 v[242:243], v249 offset:20992\n"
"v_mfma_f32_16x16x32_bf16 v[12:15], v[224:227], v[196:199], v[12:15]\n"
"ds_write_addtid_b32 v250 offset:0\n"
"v_exp_f32 v184, v184\n"
"v_mfma_f32_16x16x32_bf16 v[44:47], v[224:227], v[200:203], v[44:47]\n"
"v_exp_f32 v185, v185\n"
"v_mfma_f32_16x16x32_bf16 v[76:79], v[224:227], v[204:207], v[76:79]\n"
"s_waitcnt lgkmcnt(5)\n"
"v_cmp_gt_i32 vcc, v250, v251\n"
"v_mfma_f32_16x16x32_bf16 v[80:83], v[228:231], v[204:207], v[80:83]\n"
"s_cbranch_vccnz .LkLS22\n"
".LkLR22:\n"
"ds_read_b128 v[220:223], v244 offset:32768\n"
"v_mfma_f32_16x16x32_bf16 v[48:51], v[228:231], v[200:203], v[48:51]\n"
"v_exp_f32 v186, v186\n"
"v_mfma_f32_16x16x32_bf16 v[16:19], v[228:231], v[196:199], v[16:19]\n"
"ds_read_b128 v[224:227], v245 offset:32768\n"
"ds_read_b128 v[228:231], v244 offset:33280\n"
"v_mfma_f32_16x16x32_bf16 v[20:23], v[232:235], v[196:199], v[20:23]\n"
"v_cvt_pk_bf16_f32 v216, v180, v181\n"
"v_mfma_f32_16x16x32_bf16 v[52:55], v[232:235], v[200:203], v[52:55]\n"
"v_exp_f32 v187, v187\n"
"v_mfma_f32_16x16x32_bf16 v[84:87], v[232:235], v[204:207], v[84:87]\n"
"s_waitcnt lgkmcnt(4)\n"
"ds_read_b128 v[232:235], v245 offset:33280\n"
"v_mfma_f32_16x16x32_bf16 v[88:91], v[236:239], v[204:207], v[88:91]\n"
"v_exp_f32 v188, v188\n"
"v_mfma_f32_16x16x32_bf16 v[56:59], v[236:239], v[200:203], v[56:59]\n"
"v_exp_f32 v189, v189\n"
"v_mfma_f32_16x16x32_bf16 v[24:27], v[236:239], v[196:199], v[24:27]\n"
"ds_read_b128 v[236:239], v244 offset:36864\n"
"v_cvt_pk_bf16_f32 v217, v182, v183\n"
"v_mfma_f32_16x16x32_bf16 v[28:31], v[240:243], v[196:199], v[28:31]\n"
"v_exp_f32 v190, v190\n"
"v_mfma_f32_16x16x32_bf16 v[60:63], v[240:243], v[200:203], v[60:63]\n"
"v_exp_f32 v191, v191\n"
"v_mfma_f32_16x16x32_bf16 v[92:95], v[240:243], v[204:207], v[92:95]\n"
"s_waitcnt lgkmcnt(3)\n"
".LkLU4:\n"
"v_mfma_f32_16x16x32_bf16 v[172:175], v[220:223], v[96:99], v[156:159]\n"
"v_exp_f32 v192, v192\n"
"v_mfma_f32_16x16x32_bf16 v[176:179], v[220:223], v[112:115], v[160:163]\n"
"v_exp_f32 v193, v193\n"
"v_mfma_f32_16x16x32_bf16 v[180:183], v[220:223], v[128:131], v[164:167]\n"
"ds_read_b128 v[220:223], v245 offset:36864\n"
"v_cvt_pk_bf16_f32 v210, v184, v185\n"
"v_mfma_f32_16x16x32_bf16 v[172:175], v[224:227], v[100:103], v[172:175]\n"
"v_exp_f32 v194, v194\n"
"v_mfma_f32_16x16x32_bf16 v[176:179], v[224:227], v[116:119], v[176:179]\n"
"v_exp_f32 v195, v195\n"
"v_mfma_f32_16x16x32_bf16 v[180:183], v[224:227], v[132:135], v[180:183]\n"
"s_waitcnt lgkmcnt(2)\n"
"ds_read_b128 v[224:227], v244 offset:37376\n"
"v_mfma_f32_16x16x32_bf16 v[172:175], v[228:231], v[104:107], v[172:175]\n"
"v_cvt_pk_bf16_f32 v211, v186, v187\n"
"v_cvt_pk_bf16_f32 v214, v188, v189\n"
"v_mfma_f32_16x16x32_bf16 v[176:179], v[228:231], v[120:123], v[176:179]\n"
"v_mfma_f32_16x16x32_bf16 v[180:183], v[228:231], v[136:139], v[180:183]\n"
"ds_read_b128 v[228:231], v245 offset:37376\n"
"v_cvt_pk_bf16_f32 v215, v190, v191\n"
"v_mfma_f32_16x16x32_bf16 v[172:175], v[232:235], v[108:111], v[172:175]\n"
"v_add_u32 v250, 1, v250\n"
"v_cvt_pk_bf16_f32 v218, v192, v193\n"
"v_mfma_f32_16x16x32_bf16 v[176:179], v[232:235], v[124:127], v[176:179]\n"
"v_mfma_f32_16x16x32_bf16 v[180:183], v[232:235], v[140:143], v[180:183]\n"
"s_waitcnt lgkmcnt(2)\n"
"v_cvt_pk_bf16_f32 v219, v194, v195\n"
"v_mfma_f32_16x16x32_bf16 v[184:187], v[236:239], v[96:99], v[156:159]\n"
"ds_read_b64_tr_b16 v[232:233], v246 offset:24576\n"
"ds_read_b64_tr_b16 v[234:235], v246 offset:28672\n"
"v_mfma_f32_16x16x32_bf16 v[188:191], v[236:239], v[112:115], v[160:163]\n"
"v_exp_f32 v172, v172\n"
"v_mfma_f32_16x16x32_bf16 v[192:195], v[236:239], v[128:131], v[164:167]\n"
"v_exp_f32 v173, v173\n"
"v_mfma_f32_16x16x32_bf16 v[184:187], v[220:223], v[100:103], v[184:187]\n"
"ds_read_b64_tr_b16 v[236:237], v247 offset:24576\n"
"ds_read_b64_tr_b16 v[238:239], v247 offset:28672\n"
"v_mfma_f32_16x16x32_bf16 v[188:191], v[220:223], v[116:119], v[188:191]\n"
"v_exp_f32 v174, v174\n"
"v_mfma_f32_16x16x32_bf16 v[192:195], v[220:223], v[132:135], v[192:195]\n"
"s_waitcnt lgkmcnt(4)\n"
"v_cvt_pk_bf16_f32 v196, v172, v173\n"
"v_mfma_f32_16x16x32_bf16 v[184:187], v[224:227], v[104:107], v[184:187]\n"
"ds_read_b64_tr_b16 v[220:221], v248 offset:24576\n"
"ds_read_b64_tr_b16 v[222:223], v248 offset:28672\n"
"v_mfma_f32_16x16x32_bf16 v[188:191], v[224:227], v[120:123], v[188:191]\n"
"v_exp_f32 v175, v175\n"
"v_mfma_f32_16x16x32_bf16 v[192:195], v[224:227], v[136:139], v[192:195]\n"
"v_exp_f32 v176, v176\n"
"v_mfma_f32_16x16x32_bf16 v[184:187], v[228:231], v[108:111], v[184:187]\n"
"ds_read_b64_tr_b16 v[224:225], v249 offset:24576\n"
"ds_read_b64_tr_b16 v[226:227], v249 offset:28672\n"
"v_mfma_f32_16x16x32_bf16 v[188:191], v[228:231], v[124:127], v[188:191]\n"
"v_exp_f32 v177, v177\n"
"v_mfma_f32_16x16x32_bf16 v[192:195], v[228:231], v[140:143], v[192:195]\n"
"v_exp_f32 v178, v178\n"
"v_mfma_f32_16x16x32_bf16 v[144:147], v[168:171], v[208:211], v[144:147]\n"
"ds_read_b64_tr_b16 v[228:229], v246 offset:25088\n"
"ds_read_b64_tr_b16 v[230:231], v246 offset:29184\n"
"v_mfma_f32_16x16x32_bf16 v[148:151], v[168:171], v[212:215], v[148:151]\n"
"v_exp_f32 v179, v179\n"
"v_mfma_f32_16x16x32_bf16 v[152:155], v[168:171], v[216:219], v[152:155]\n"
"s_waitcnt lgkmcnt(6)\n"
"v_cvt_pk_bf16_f32 v197, v174, v175\n"
"v_mfma_f32_16x16x32_bf16 v[64:67], v[232:235], v[216:219], v[64:67]\n"
"v_exp_f32 v180, v180\n"
"v_mfma_f32_16x16x32_bf16 v[32:35], v[232:235], v[212:215], v[32:35]\n"
"v_exp_f32 v181, v181\n"
"v_mfma_f32_16x16x32_bf16 v[0:3], v[232:235], v[208:211], v[0:3]\n"
"ds_read_b64_tr_b16 v[232:233], v247 offset:25088\n"
"ds_read_b64_tr_b16 v[234:235], v247 offset:29184\n"
"v_mfma_f32_16x16x32_bf16 v[4:7], v[236:239], v[208:211], v[4:7]\n"
"ds_read_b32 v251, v252\n"
"v_cvt_pk_bf16_f32 v200, v176, v177\n"
"v_mfma_f32_16x16x32_bf16 v[36:39], v[236:239], v[212:215], v[36:39]\n"
"v_exp_f32 v182, v182\n"
"v_mfma_f32_16x16x32_bf16 v[68:71], v[236:239], v[216:219], v[68:71]\n"
"s_waitcnt lgkmcnt(5)\n"
"v_cvt_pk_bf16_f32 v201, v178, v179\n"
"v_mfma_f32_16x16x32_bf16 v[72:75], v[220:223], v[216:219], v[72:75]\n"
"ds_read_b64_tr_b16 v[236:237], v248 offset:25088\n"
"ds_read_b64_tr_b16 v[238:239], v248 offset:29184\n"
"v_mfma_f32_16x16x32_bf16 v[40:43], v[220:223], v[212:215], v[40:43]\n"
"v_exp_f32 v183, v183\n"
"v_mfma_f32_16x16x32_bf16 v[8:11], v[220:223], v[208:211], v[8:11]\n"
"ds_read_b64_tr_b16 v[240:241], v249 offset:25088\n"
"ds_read_b64_tr_b16 v[242:243], v249 offset:29184\n"
"v_mfma_f32_16x16x32_bf16 v[12:15], v[224:227], v[208:211], v[12:15]\n"
"v_exp_f32 v184, v184\n"
"v_mfma_f32_16x16x32_bf16 v[44:47], v[224:227], v[212:215], v[44:47]\n"
"v_exp_f32 v185, v185\n"
"v_mfma_f32_16x16x32_bf16 v[76:79], v[224:227], v[216:219], v[76:79]\n"
"s_waitcnt lgkmcnt(4)\n"
"v_cmp_gt_i32 vcc, v250, v251\n"
"v_mfma_f32_16x16x32_bf16 v[80:83], v[228:231], v[216:219], v[80:83]\n"
"s_cbranch_vccnz .LkLS23\n"
".LkLR23:\n"
"ds_read_b128 v[220:223], v244 offset:40960\n"
"v_mfma_f32_16x16x32_bf16 v[48:51], v[228:231], v[212:215], v[48:51]\n"
"v_exp_f32 v186, v186\n"
"v_mfma_f32_16x16x32_bf16 v[16:19], v[228:231], v[208:211], v[16:19]\n"
"ds_read_b128 v[224:227], v245 offset:40960\n"
"ds_read_b128 v[228:231], v244 offset:41472\n"
"v_mfma_f32_16x16x32_bf16 v[20:23], v[232:235], v[208:211], v[20:23]\n"
"v_cvt_pk_bf16_f32 v204, v180, v181\n"
"v_mfma_f32_16x16x32_bf16 v[52:55], v[232:235], v[212:215], v[52:55]\n"
"v_exp_f32 v187, v187\n"
"v_mfma_f32_16x16x32_bf16 v[84:87], v[232:235], v[216:219], v[84:87]\n"
"s_waitcnt lgkmcnt(3)\n"
"ds_read_b128 v[232:235], v245 offset:41472\n"
"v_mfma_f32_16x16x32_bf16 v[88:91], v[236:239], v[216:219], v[88:91]\n"
"v_exp_f32 v188, v188\n"
"v_mfma_f32_16x16x32_bf16 v[56:59], v[236:239], v[212:215], v[56:59]\n"
"v_exp_f32 v189, v189\n"
"v_mfma_f32_16x16x32_bf16 v[24:27], v[236:239], v[208:211], v[24:27]\n"
"ds_read_b128 v[236:239], v244 offset:45056\n"
"v_cvt_pk_bf16_f32 v205, v182, v183\n"
"v_mfma_f32_16x16x32_bf16 v[28:31], v[240:243], v[208:211], v[28:31]\n"
"v_exp_f32 v190, v190\n"
"v_mfma_f32_16x16x32_bf16 v[60:63], v[240:243], v[212:215], v[60:63]\n"
"v_exp_f32 v191, v191\n"
"v_mfma_f32_16x16x32_bf16 v[92:95], v[240:243], v[216:219], v[92:95]\n"
"s_waitcnt lgkmcnt(3)\n"
".LkLU5:\n"
"v_mfma_f32_16x16x32_bf16 v[172:175], v[220:223], v[96:99], v[156:159]\n"
"v_exp_f32 v192, v192\n"
"v_mfma_f32_16x16x32_bf16 v[176:179], v[220:223], v[112:115], v[160:163]\n"
"v_exp_f32 v193, v193\n"
"v_mfma_f32_16x16x32_bf16 v[180:183], v[220:223], v[128:131], v[164:167]\n"
"ds_read_b128 v[220:223], v245 offset:45056\n"
"v_cvt_pk_bf16_f32 v198, v184, v185\n"
"v_mfma_f32_16x16x32_bf16 v[172:175], v[224:227], v[100:103], v[172:175]\n"
"v_exp_f32 v194, v194\n"
"v_mfma_f32_16x16x32_bf16 v[176:179], v[224:227], v[116:119], v[176:179]\n"
"v_exp_f32 v195, v195\n"
"v_mfma_f32_16x16x32_bf16 v[180:183], v[224:227], v[132:135], v[180:183]\n"
"s_waitcnt lgkmcnt(2)\n"
"ds_read_b128 v[224:227], v244 offset:45568\n"
"v_mfma_f32_16x16x32_bf16 v[172:175], v[228:231], v[104:107], v[172:175]\n"
"v_cvt_pk_bf16_f32 v199, v186, v187\n"
"v_cvt_pk_bf16_f32 v202, v188, v189\n"
"v_mfma_f32_16x16x32_bf16 v[176:179], v[228:231], v[120:123], v[176:179]\n"
"v_mfma_f32_16x16x32_bf16 v[180:183], v[228:231], v[136:139], v[180:183]\n"
"ds_read_b128 v[228:231], v245 offset:45568\n"
"v_cvt_pk_bf16_f32 v203, v190, v191\n"
"v_mfma_f32_16x16x32_bf16 v[172:175], v[232:235], v[108:111], v[172:175]\n"
"v_add_u32 v250, 1, v250\n"
"v_cvt_pk_bf16_f32 v206, v192, v193\n"
"v_mfma_f32_16x16x32_bf16 v[176:179], v[232:235], v[124:127], v[176:179]\n"
"v_mfma_f32_16x16x32_bf16 v[180:183], v[232:235], v[140:143], v[180:183]\n"
"s_waitcnt lgkmcnt(2)\n"
"v_cvt_pk_bf16_f32 v207, v194, v195\n"
"v_mfma_f32_16x16x32_bf16 v[184:187], v[236:239], v[96:99], v[156:159]\n"
"ds_read_b64_tr_b16 v[232:233], v246 offset:32768\n"
"ds_read_b64_tr_b16 v[234:235], v246 offset:36864\n"
"v_mfma_f32_16x16x32_bf16 v[188:191], v[236:239], v[112:115], v[160:163]\n"
"v_exp_f32 v172, v172\n"
"v_mfma_f32_16x16x32_bf16 v[192:195], v[236:239], v[128:131], v[164:167]\n"
"v_exp_f32 v173, v173\n"
"v_mfma_f32_16x16x32_bf16 v[184:187], v[220:223], v[100:103], v[184:187]\n"
"ds_read_b64_tr_b16 v[236:237], v247 offset:32768\n"
"ds_read_b64_tr_b16 v[238:239], v247 offset:36864\n"
"v_mfma_f32_16x16x32_bf16 v[188:191], v[220:223], v[116:119], v[188:191]\n"
"v_exp_f32 v174, v174\n"
"v_mfma_f32_16x16x32_bf16 v[192:195], v[220:223], v[132:135], v[192:195]\n"
"s_waitcnt lgkmcnt(4)\n"
"v_cvt_pk_bf16_f32 v208, v172, v173\n"
"v_mfma_f32_16x16x32_bf16 v[184:187], v[224:227], v[104:107], v[184:187]\n"
"ds_read_b64_tr_b16 v[220:221], v248 offset:32768\n"
"ds_read_b64_tr_b16 v[222:223], v248 offset:36864\n"
"v_mfma_f32_16x16x32_bf16 v[188:191], v[224:227], v[120:123], v[188:191]\n"
"v_exp_f32 v175, v175\n"
"v_mfma_f32_16x16x32_bf16 v[192:195], v[224:227], v[136:139], v[192:195]\n"
"v_exp_f32 v176, v176\n"
"v_mfma_f32_16x16x32_bf16 v[184:187], v[228:231], v[108:111], v[184:187]\n"
"ds_read_b64_tr_b16 v[224:225], v249 offset:32768\n"
"ds_read_b64_tr_b16 v[226:227], v249 offset:36864\n"
"v_mfma_f32_16x16x32_bf16 v[188:191], v[228:231], v[124:127], v[188:191]\n"
"v_exp_f32 v177, v177\n"
"v_mfma_f32_16x16x32_bf16 v[192:195], v[228:231], v[140:143], v[192:195]\n"
"v_exp_f32 v178, v178\n"
"v_mfma_f32_16x16x32_bf16 v[144:147], v[168:171], v[196:199], v[144:147]\n"
"ds_read_b64_tr_b16 v[228:229], v246 offset:33280\n"
"ds_read_b64_tr_b16 v[230:231], v246 offset:37376\n"
"v_mfma_f32_16x16x32_bf16 v[148:151], v[168:171], v[200:203], v[148:151]\n"
"v_exp_f32 v179, v179\n"
"v_mfma_f32_16x16x32_bf16 v[152:155], v[168:171], v[204:207], v[152:155]\n"
"s_waitcnt lgkmcnt(6)\n"
"v_cvt_pk_bf16_f32 v209, v174, v175\n"
"v_mfma_f32_16x16x32_bf16 v[64:67], v[232:235], v[204:207], v[64:67]\n"
"v_exp_f32 v180, v180\n"
"v_mfma_f32_16x16x32_bf16 v[32:35], v[232:235], v[200:203], v[32:35]\n"
"v_exp_f32 v181, v181\n"
"v_mfma_f32_16x16x32_bf16 v[0:3], v[232:235], v[196:199], v[0:3]\n"
"ds_read_b64_tr_b16 v[232:233], v247 offset:33280\n"
"ds_read_b64_tr_b16 v[234:235], v247 offset:37376\n"
"v_mfma_f32_16x16x32_bf16 v[4:7], v[236:239], v[196:199], v[4:7]\n"
"ds_read_b32 v251, v252\n"
"v_cvt_pk_bf16_f32 v212, v176, v177\n"
"v_mfma_f32_16x16x32_bf16 v[36:39], v[236:239], v[200:203], v[36:39]\n"
"v_exp_f32 v182, v182\n"
"v_mfma_f32_16x16x32_bf16 v[68:71], v[236:239], v[204:207], v[68:71]\n"
"s_waitcnt lgkmcnt(5)\n"
"v_cvt_pk_bf16_f32 v213, v178, v179\n"
"v_mfma_f32_16x16x32_bf16 v[72:75], v[220:223], v[204:207], v[72:75]\n"
"ds_read_b64_tr_b16 v[236:237], v248 offset:33280\n"
"ds_read_b64_tr_b16 v[238:239], v248 offset:37376\n"
"v_mfma_f32_16x16x32_bf16 v[40:43], v[220:223], v[200:203], v[40:43]\n"
"v_exp_f32 v183, v183\n"
"v_mfma_f32_16x16x32_bf16 v[8:11], v[220:223], v[196:199], v[8:11]\n"
"ds_read_b64_tr_b16 v[240:241], v249 offset:33280\n"
"ds_read_b64_tr_b16 v[242:243], v249 offset:37376\n"
"v_mfma_f32_16x16x32_bf16 v[12:15], v[224:227], v[196:199], v[12:15]\n"
"ds_write_addtid_b32 v250 offset:0\n"
"v_exp_f32 v184, v184\n"
"v_mfma_f32_16x16x32_bf16 v[44:47], v[224:227], v[200:203], v[44:47]\n"
"v_exp_f32 v185, v185\n"
"v_mfma_f32_16x16x32_bf16 v[76:79], v[224:227], v[204:207], v[76:79]\n"
"s_waitcnt lgkmcnt(5)\n"
"v_cmp_gt_i32 vcc, v250, v251\n"
"v_mfma_f32_16x16x32_bf16 v[80:83], v[228:231], v[204:207], v[80:83]\n"
"s_cbranch_vccnz .LkLS24\n"
".LkLR24:\n"
"ds_read_b128 v[220:223], v244 offset:49152\n"
"v_mfma_f32_16x16x32_bf16 v[48:51], v[228:231], v[200:203], v[48:51]\n"
"v_exp_f32 v186, v186\n"
"v_mfma_f32_16x16x32_bf16 v[16:19], v[228:231], v[196:199], v[16:19]\n"
"ds_read_b128 v[224:227], v245 offset:49152\n"
"ds_read_b128 v[228:231], v244 offset:49664\n"
"v_mfma_f32_16x16x32_bf16 v[20:23], v[232:235], v[196:199], v[20:23]\n"
"v_cvt_pk_bf16_f32 v216, v180, v181\n"
"v_mfma_f32_16x16x32_bf16 v[52:55], v[232:235], v[200:203], v[52:55]\n"
"v_exp_f32 v187, v187\n"
"v_mfma_f32_16x16x32_bf16 v[84:87], v[232:235], v[204:207], v[84:87]\n"
"s_waitcnt lgkmcnt(4)\n"
"ds_read_b128 v[232:235], v245 offset:49664\n"
"v_mfma_f32_16x16x32_bf16 v[88:91], v[236:239], v[204:207], v[88:91]\n"
"v_exp_f32 v188, v188\n"
"v_mfma_f32_16x16x32_bf16 v[56:59], v[236:239], v[200:203], v[56:59]\n"
"v_exp_f32 v189, v189\n"
"v_mfma_f32_16x16x32_bf16 v[24:27], v[236:239], v[196:199], v[24:27]\n"
"ds_read_b128 v[236:239], v244 offset:53248\n"
"v_cvt_pk_bf16_f32 v217, v182, v183\n"
"v_mfma_f32_16x16x32_bf16 v[28:31], v[240:243], v[196:199], v[28:31]\n"
"v_exp_f32 v190, v190\n"
"v_mfma_f32_16x16x32_bf16 v[60:63], v[240:243], v[200:203], v[60:63]\n"
"v_exp_f32 v191, v191\n"
"v_mfma_f32_16x16x32_bf16 v[92:95], v[240:243], v[204:207], v[92:95]\n"
"s_waitcnt lgkmcnt(3)\n"
".LkLU6:\n"
"v_mfma_f32_16x16x32_bf16 v[172:175], v[220:223], v[96:99], v[156:159]\n"
"v_exp_f32 v192, v192\n"
"v_mfma_f32_16x16x32_bf16 v[176:179], v[220:223], v[112:115], v[160:163]\n"
"v_exp_f32 v193, v193\n"
"v_mfma_f32_16x16x32_bf16 v[180:183], v[220:223], v[128:131], v[164:167]\n"
"ds_read_b128 v[220:223], v245 offset:53248\n"
"v_cvt_pk_bf16_f32 v210, v184, v185\n"
"v_mfma_f32_16x16x32_bf16 v[172:175], v[224:227], v[100:103], v[172:175]\n"
"v_exp_f32 v194, v194\n"
"v_mfma_f32_16x16x32_bf16 v[176:179], v[224:227], v[116:119], v[176:179]\n"
"v_exp_f32 v195, v195\n"
"v_mfma_f32_16x16x32_bf16 v[180:183], v[224:227], v[132:135], v[180:183]\n"
"s_waitcnt lgkmcnt(2)\n"
"ds_read_b128 v[224:227], v244 offset:53760\n"
"v_mfma_f32_16x16x32_bf16 v[172:175], v[228:231], v[104:107], v[172:175]\n"
"v_cvt_pk_bf16_f32 v211, v186, v187\n"
"v_cvt_pk_bf16_f32 v214, v188, v189\n"
"v_mfma_f32_16x16x32_bf16 v[176:179], v[228:231], v[120:123], v[176:179]\n"
"v_mfma_f32_16x16x32_bf16 v[180:183], v[228:231], v[136:139], v[180:183]\n"
"ds_read_b128 v[228:231], v245 offset:53760\n"
"v_cvt_pk_bf16_f32 v215, v190, v191\n"
"v_mfma_f32_16x16x32_bf16 v[172:175], v[232:235], v[108:111], v[172:175]\n"
"v_add_u32 v250, 1, v250\n"
"v_cvt_pk_bf16_f32 v218, v192, v193\n"
"v_mfma_f32_16x16x32_bf16 v[176:179], v[232:235], v[124:127], v[176:179]\n"
"v_mfma_f32_16x16x32_bf16 v[180:183], v[232:235], v[140:143], v[180:183]\n"
"s_waitcnt lgkmcnt(2)\n"
"v_cvt_pk_bf16_f32 v219, v194, v195\n"
"v_mfma_f32_16x16x32_bf16 v[184:187], v[236:239], v[96:99], v[156:159]\n"
"ds_read_b64_tr_b16 v[232:233], v246 offset:40960\n"
"ds_read_b64_tr_b16 v[234:235], v246 offset:45056\n"
"v_mfma_f32_16x16x32_bf16 v[188:191], v[236:239], v[112:115], v[160:163]\n"
"v_exp_f32 v172, v172\n"
"v_mfma_f32_16x16x32_bf16 v[192:195], v[236:239], v[128:131], v[164:167]\n"
"v_exp_f32 v173, v173\n"
"v_mfma_f32_16x16x32_bf16 v[184:187], v[220:223], v[100:103], v[184:187]\n"
"ds_read_b64_tr_b16 v[236:237], v247 offset:40960\n"
"ds_read_b64_tr_b16 v[238:239], v247 offset:45056\n"
"v_mfma_f32_16x16x32_bf16 v[188:191], v[220:223], v[116:119], v[188:191]\n"
"v_exp_f32 v174, v174\n"
"v_mfma_f32_16x16x32_bf16 v[192:195], v[220:223], v[132:135], v[192:195]\n"
"s_waitcnt lgkmcnt(4)\n"
"v_cvt_pk_bf16_f32 v196, v172, v173\n"
"v_mfma_f32_16x16x32_bf16 v[184:187], v[224:227], v[104:107], v[184:187]\n"
"ds_read_b64_tr_b16 v[220:221], v248 offset:40960\n"
"ds_read_b64_tr_b16 v[222:223], v248 offset:45056\n"
"v_mfma_f32_16x16x32_bf16 v[188:191], v[224:227], v[120:123], v[188:191]\n"
"v_exp_f32 v175, v175\n"
"v_mfma_f32_16x16x32_bf16 v[192:195], v[224:227], v[136:139], v[192:195]\n"
"v_exp_f32 v176, v176\n"
"v_mfma_f32_16x16x32_bf16 v[184:187], v[228:231], v[108:111], v[184:187]\n"
"ds_read_b64_tr_b16 v[224:225], v249 offset:40960\n"
"ds_read_b64_tr_b16 v[226:227], v249 offset:45056\n"
"v_mfma_f32_16x16x32_bf16 v[188:191], v[228:231], v[124:127], v[188:191]\n"
"v_exp_f32 v177, v177\n"
"v_mfma_f32_16x16x32_bf16 v[192:195], v[228:231], v[140:143], v[192:195]\n"
"v_exp_f32 v178, v178\n"
"v_mfma_f32_16x16x32_bf16 v[144:147], v[168:171], v[208:211], v[144:147]\n"
"ds_read_b64_tr_b16 v[228:229], v246 offset:41472\n"
"ds_read_b64_tr_b16 v[230:231], v246 offset:45568\n"
"v_mfma_f32_16x16x32_bf16 v[148:151], v[168:171], v[212:215], v[148:151]\n"
"v_exp_f32 v179, v179\n"
"v_mfma_f32_16x16x32_bf16 v[152:155], v[168:171], v[216:219], v[152:155]\n"
"s_waitcnt lgkmcnt(6)\n"
"v_cvt_pk_bf16_f32 v197, v174, v175\n"
"v_mfma_f32_16x16x32_bf16 v[64:67], v[232:235], v[216:219], v[64:67]\n"
"v_exp_f32 v180, v180\n"
"v_mfma_f32_16x16x32_bf16 v[32:35], v[232:235], v[212:215], v[32:35]\n"
"v_exp_f32 v181, v181\n"
"v_mfma_f32_16x16x32_bf16 v[0:3], v[232:235], v[208:211], v[0:3]\n"
"ds_read_b64_tr_b16 v[232:233], v247 offset:41472\n"
"ds_read_b64_tr_b16 v[234:235], v247 offset:45568\n"
"v_mfma_f32_16x16x32_bf16 v[4:7], v[236:239], v[208:211], v[4:7]\n"
"ds_read_b32 v251, v252\n"
"v_cvt_pk_bf16_f32 v200, v176, v177\n"
"v_mfma_f32_16x16x32_bf16 v[36:39], v[236:239], v[212:215], v[36:39]\n"
"v_exp_f32 v182, v182\n"
"v_mfma_f32_16x16x32_bf16 v[68:71], v[236:239], v[216:219], v[68:71]\n"
"s_waitcnt lgkmcnt(5)\n"
"v_cvt_pk_bf16_f32 v201, v178, v179\n"
"v_mfma_f32_16x16x32_bf16 v[72:75], v[220:223], v[216:219], v[72:75]\n"
"ds_read_b64_tr_b16 v[236:237], v248 offset:41472\n"
"ds_read_b64_tr_b16 v[238:239], v248 offset:45568\n"
"v_mfma_f32_16x16x32_bf16 v[40:43], v[220:223], v[212:215], v[40:43]\n"
"v_exp_f32 v183, v183\n"
"v_mfma_f32_16x16x32_bf16 v[8:11], v[220:223], v[208:211], v[8:11]\n"
"ds_read_b64_tr_b16 v[240:241], v249 offset:41472\n"
"ds_read_b64_tr_b16 v[242:243], v249 offset:45568\n"
"v_mfma_f32_16x16x32_bf16 v[12:15], v[224:227], v[208:211], v[12:15]\n"
"v_exp_f32 v184, v184\n"
"v_mfma_f32_16x16x32_bf16 v[44:47], v[224:227], v[212:215], v[44:47]\n"
"v_exp_f32 v185, v185\n"
"v_mfma_f32_16x16x32_bf16 v[76:79], v[224:227], v[216:219], v[76:79]\n"
"s_waitcnt lgkmcnt(4)\n"
"v_cmp_gt_i32 vcc, v250, v251\n"
"v_mfma_f32_16x16x32_bf16 v[80:83], v[228:231], v[216:219], v[80:83]\n"
"s_cbranch_vccnz .LkLS25\n"
".LkLR25:\n"
"ds_read_b128 v[220:223], v244 offset:57344\n"
"v_mfma_f32_16x16x32_bf16 v[48:51], v[228:231], v[212:215], v[48:51]\n"
"v_exp_f32 v186, v186\n"
"v_mfma_f32_16x16x32_bf16 v[16:19], v[228:231], v[208:211], v[16:19]\n"
"ds_read_b128 v[224:227], v245 offset:57344\n"
"ds_read_b128 v[228:231], v244 offset:57856\n"
"v_mfma_f32_16x16x32_bf16 v[20:23], v[232:235], v[208:211], v[20:23]\n"
"v_cvt_pk_bf16_f32 v204, v180, v181\n"
"v_mfma_f32_16x16x32_bf16 v[52:55], v[232:235], v[212:215], v[52:55]\n"
"v_exp_f32 v187, v187\n"
"v_mfma_f32_16x16x32_bf16 v[84:87], v[232:235], v[216:219], v[84:87]\n"
"s_waitcnt lgkmcnt(3)\n"
"ds_read_b128 v[232:235], v245 offset:57856\n"
"v_mfma_f32_16x16x32_bf16 v[88:91], v[236:239], v[216:219], v[88:91]\n"
"v_exp_f32 v188, v188\n"
"v_mfma_f32_16x16x32_bf16 v[56:59], v[236:239], v[212:215], v[56:59]\n"
"v_exp_f32 v189, v189\n"
"v_mfma_f32_16x16x32_bf16 v[24:27], v[236:239], v[208:211], v[24:27]\n"
"ds_read_b128 v[236:239], v244 offset:61440\n"
"v_cvt_pk_bf16_f32 v205, v182, v183\n"
"v_mfma_f32_16x16x32_bf16 v[28:31], v[240:243], v[208:211], v[28:31]\n"
"v_exp_f32 v190, v190\n"
"v_mfma_f32_16x16x32_bf16 v[60:63], v[240:243], v[212:215], v[60:63]\n"
"v_exp_f32 v191, v191\n"
"v_mfma_f32_16x16x32_bf16 v[92:95], v[240:243], v[216:219], v[92:95]\n"
"s_waitcnt lgkmcnt(3)\n"
".LkLU7:\n"
"v_mfma_f32_16x16x32_bf16 v[172:175], v[220:223], v[96:99], v[156:159]\n"
"v_exp_f32 v192, v192\n"
"v_mfma_f32_16x16x32_bf16 v[176:179], v[220:223], v[112:115], v[160:163]\n"
"v_exp_f32 v193, v193\n"
"v_mfma_f32_16x16x32_bf16 v[180:183], v[220:223], v[128:131], v[164:167]\n"
"ds_read_b128 v[220:223], v245 offset:61440\n"
"v_cvt_pk_bf16_f32 v198, v184, v185\n"
"v_mfma_f32_16x16x32_bf16 v[172:175], v[224:227], v[100:103], v[172:175]\n"
"v_exp_f32 v194, v194\n"
"v_mfma_f32_16x16x32_bf16 v[176:179], v[224:227], v[116:119], v[176:179]\n"
"v_exp_f32 v195, v195\n"
"v_mfma_f32_16x16x32_bf16 v[180:183], v[224:227], v[132:135], v[180:183]\n"
"s_waitcnt lgkmcnt(2)\n"
"ds_read_b128 v[224:227], v244 offset:61952\n"
"v_mfma_f32_16x16x32_bf16 v[172:175], v[228:231], v[104:107], v[172:175]\n"
"v_cvt_pk_bf16_f32 v199, v186, v187\n"
"v_cvt_pk_bf16_f32 v202, v188, v189\n"
"v_mfma_f32_16x16x32_bf16 v[176:179], v[228:231], v[120:123], v[176:179]\n"
"v_mfma_f32_16x16x32_bf16 v[180:183], v[228:231], v[136:139], v[180:183]\n"
"ds_read_b128 v[228:231], v245 offset:61952\n"
"v_cvt_pk_bf16_f32 v203, v190, v191\n"
"v_mfma_f32_16x16x32_bf16 v[172:175], v[232:235], v[108:111], v[172:175]\n"
"v_add_u32 v250, 1, v250\n"
"v_cvt_pk_bf16_f32 v206, v192, v193\n"
"v_mfma_f32_16x16x32_bf16 v[176:179], v[232:235], v[124:127], v[176:179]\n"
"v_mfma_f32_16x16x32_bf16 v[180:183], v[232:235], v[140:143], v[180:183]\n"
"s_waitcnt lgkmcnt(2)\n"
"v_cvt_pk_bf16_f32 v207, v194, v195\n"
"v_mfma_f32_16x16x32_bf16 v[184:187], v[236:239], v[96:99], v[156:159]\n"
"ds_read_b64_tr_b16 v[232:233], v246 offset:49152\n"
"ds_read_b64_tr_b16 v[234:235], v246 offset:53248\n"
"v_mfma_f32_16x16x32_bf16 v[188:191], v[236:239], v[112:115], v[160:163]\n"
"v_exp_f32 v172, v172\n"
"v_mfma_f32_16x16x32_bf16 v[192:195], v[236:239], v[128:131], v[164:167]\n"
"v_exp_f32 v173, v173\n"
"v_mfma_f32_16x16x32_bf16 v[184:187], v[220:223], v[100:103], v[184:187]\n"
"ds_read_b64_tr_b16 v[236:237], v247 offset:49152\n"
"ds_read_b64_tr_b16 v[238:239], v247 offset:53248\n"
"v_mfma_f32_16x16x32_bf16 v[188:191], v[220:223], v[116:119], v[188:191]\n"
"v_exp_f32 v174, v174\n"
"v_mfma_f32_16x16x32_bf16 v[192:195], v[220:223], v[132:135], v[192:195]\n"
"s_waitcnt lgkmcnt(4)\n"
"v_cvt_pk_bf16_f32 v208, v172, v173\n"
"v_mfma_f32_16x16x32_bf16 v[184:187], v[224:227], v[104:107], v[184:187]\n"
"ds_read_b64_tr_b16 v[220:221], v248 offset:49152\n"
"ds_read_b64_tr_b16 v[222:223], v248 offset:53248\n"
"v_mfma_f32_16x16x32_bf16 v[188:191], v[224:227], v[120:123], v[188:191]\n"
"v_exp_f32 v175, v175\n"
"v_mfma_f32_16x16x32_bf16 v[192:195], v[224:227], v[136:139], v[192:195]\n"
"v_exp_f32 v176, v176\n"
"v_mfma_f32_16x16x32_bf16 v[184:187], v[228:231], v[108:111], v[184:187]\n"
"ds_read_b64_tr_b16 v[224:225], v249 offset:49152\n"
"ds_read_b64_tr_b16 v[226:227], v249 offset:53248\n"
"v_mfma_f32_16x16x32_bf16 v[188:191], v[228:231], v[124:127], v[188:191]\n"
"v_exp_f32 v177, v177\n"
"v_mfma_f32_16x16x32_bf16 v[192:195], v[228:231], v[140:143], v[192:195]\n"
"v_exp_f32 v178, v178\n"
"v_mfma_f32_16x16x32_bf16 v[144:147], v[168:171], v[196:199], v[144:147]\n"
"ds_read_b64_tr_b16 v[228:229], v246 offset:49664\n"
"ds_read_b64_tr_b16 v[230:231], v246 offset:53760\n"
"v_mfma_f32_16x16x32_bf16 v[148:151], v[168:171], v[200:203], v[148:151]\n"
"v_exp_f32 v179, v179\n"
"v_mfma_f32_16x16x32_bf16 v[152:155], v[168:171], v[204:207], v[152:155]\n"
"s_waitcnt lgkmcnt(6)\n"
"v_cvt_pk_bf16_f32 v209, v174, v175\n"
"v_mfma_f32_16x16x32_bf16 v[64:67], v[232:235], v[204:207], v[64:67]\n"
"v_exp_f32 v180, v180\n"
"v_mfma_f32_16x16x32_bf16 v[32:35], v[232:235], v[200:203], v[32:35]\n"
"v_exp_f32 v181, v181\n"
"v_mfma_f32_16x16x32_bf16 v[0:3], v[232:235], v[196:199], v[0:3]\n"
"ds_read_b64_tr_b16 v[232:233], v247 offset:49664\n"
"ds_read_b64_tr_b16 v[234:235], v247 offset:53760\n"
"v_mfma_f32_16x16x32_bf16 v[4:7], v[236:239], v[196:199], v[4:7]\n"
"ds_read_b32 v251, v252\n"
"v_cvt_pk_bf16_f32 v212, v176, v177\n"
"v_mfma_f32_16x16x32_bf16 v[36:39], v[236:239], v[200:203], v[36:39]\n"
"v_exp_f32 v182, v182\n"
"v_mfma_f32_16x16x32_bf16 v[68:71], v[236:239], v[204:207], v[68:71]\n"
"s_waitcnt lgkmcnt(5)\n"
"v_cvt_pk_bf16_f32 v213, v178, v179\n"
"v_mfma_f32_16x16x32_bf16 v[72:75], v[220:223], v[204:207], v[72:75]\n"
"ds_read_b64_tr_b16 v[236:237], v248 offset:49664\n"
"ds_read_b64_tr_b16 v[238:239], v248 offset:53760\n"
"v_mfma_f32_16x16x32_bf16 v[40:43], v[220:223], v[200:203], v[40:43]\n"
"v_exp_f32 v183, v183\n"
"v_mfma_f32_16x16x32_bf16 v[8:11], v[220:223], v[196:199], v[8:11]\n"
"ds_read_b64_tr_b16 v[240:241], v249 offset:49664\n"
"ds_read_b64_tr_b16 v[242:243], v249 offset:53760\n"
"v_mfma_f32_16x16x32_bf16 v[12:15], v[224:227], v[196:199], v[12:15]\n"
"ds_write_addtid_b32 v250 offset:0\n"
"v_exp_f32 v184, v184\n"
"v_mfma_f32_16x16x32_bf16 v[44:47], v[224:227], v[200:203], v[44:47]\n"
"v_exp_f32 v185, v185\n"
"v_mfma_f32_16x16x32_bf16 v[76:79], v[224:227], v[204:207], v[76:79]\n"
"s_waitcnt lgkmcnt(5)\n"
"v_cmp_gt_i32 vcc, v250, v251\n"
"v_mfma_f32_16x16x32_bf16 v[80:83], v[228:231], v[204:207], v[80:83]\n"
"s_cbranch_vccnz .LkLS26\n"
".LkLR26:\n"
"ds_read_b128 v[220:223], v244 offset:0\n"
"v_mfma_f32_16x16x32_bf16 v[48:51], v[228:231], v[200:203], v[48:51]\n"
"v_exp_f32 v186, v186\n"
"v_mfma_f32_16x16x32_bf16 v[16:19], v[228:231], v[196:199], v[16:19]\n"
"ds_read_b128 v[224:227], v245 offset:0\n"
"ds_read_b128 v[228:231], v244 offset:512\n"
"v_mfma_f32_16x16x32_bf16 v[20:23], v[232:235], v[196:199], v[20:23]\n"
"v_cvt_pk_bf16_f32 v216, v180, v181\n"
"v_mfma_f32_16x16x32_bf16 v[52:55], v[232:235], v[200:203], v[52:55]\n"
"v_exp_f32 v187, v187\n"
"s_sub_u32 s87, s87, 1\n"
"v_mfma_f32_16x16x32_bf16 v[84:87], v[232:235], v[204:207], v[84:87]\n"
"s_waitcnt lgkmcnt(4)\n"
"ds_read_b128 v[232:235], v245 offset:512\n"
"v_mfma_f32_16x16x32_bf16 v[88:91], v[236:239], v[204:207], v[88:91]\n"
"v_exp_f32 v188, v188\n"
"v_mfma_f32_16x16x32_bf16 v[56:59], v[236:239], v[200:203], v[56:59]\n"
"v_exp_f32 v189, v189\n"
"v_mfma_f32_16x16x32_bf16 v[24:27], v[236:239], v[196:199], v[24:27]\n"
"ds_read_b128 v[236:239], v244 offset:4096\n"
"v_cvt_pk_bf16_f32 v217, v182, v183\n"
"v_mfma_f32_16x16x32_bf16 v[28:31], v[240:243], v[196:199], v[28:31]\n"
"v_exp_f32 v190, v190\n"
"v_mfma_f32_16x16x32_bf16 v[60:63], v[240:243], v[200:203], v[60:63]\n"
"v_exp_f32 v191, v191\n"
"v_mfma_f32_16x16x32_bf16 v[92:95], v[240:243], v[204:207], v[92:95]\n"
"s_waitcnt lgkmcnt(3)\n"
"s_cbranch_scc0 .LkLU0\n"
"ds_read_b64_tr_b16 v[220:221], v246 offset:57344\n"
"ds_read_b64_tr_b16 v[222:223], v246 offset:61440\n"
"ds_read_b64_tr_b16 v[224:225], v247 offset:57344\n"
"ds_read_b64_tr_b16 v[226:227], v247 offset:61440\n"
"ds_read_b64_tr_b16 v[228:229], v248 offset:57344\n"
"ds_read_b64_tr_b16 v[230:231], v248 offset:61440\n"
"ds_read_b64_tr_b16 v[232:233], v249 offset:57344\n"
"ds_read_b64_tr_b16 v[234:235], v249 offset:61440\n"
"ds_read_b64_tr_b16 v[236:237], v246 offset:57856\n"
"ds_read_b64_tr_b16 v[238:239], v246 offset:61952\n"
"ds_read_b64_tr_b16 v[240:241], v247 offset:57856\n"
"ds_read_b64_tr_b16 v[242:243], v247 offset:61952\n"
"ds_read_b64_tr_b16 v[172:173], v248 offset:57856\n"
"ds_read_b64_tr_b16 v[174:175], v248 offset:61952\n"
"ds_read_b64_tr_b16 v[176:177], v249 offset:57856\n"
"ds_read_b64_tr_b16 v[178:179], v249 offset:61952\n"
"v_cvt_pk_bf16_f32 v210, v184, v185\n"
"v_cvt_pk_bf16_f32 v211, v186, v187\n"
"v_lshlrev_b32 v252, 4, v255\n"
"s_waitcnt vmcnt(0)\n"
"v_mov_b32 v253, 163824\n"
"ds_read_b64 v[244:245], v253\n"
"v_mfma_f32_16x16x32_bf16 v[144:147], v[168:171], v[208:211], v[144:147]\n"
"s_waitcnt lgkmcnt(13)\n"
"v_mfma_f32_16x16x32_bf16 v[0:3], v[220:223], v[208:211], v[0:3]\n"
"v_cvt_pk_bf16_f32 v214, v188, v189\n"
"v_cvt_pk_bf16_f32 v215, v190, v191\n"
"v_mfma_f32_16x16x32_bf16 v[4:7], v[224:227], v[208:211], v[4:7]\n"
"s_waitcnt lgkmcnt(9)\n"
"v_mfma_f32_16x16x32_bf16 v[8:11], v[228:231], v[208:211], v[8:11]\n"
"v_exp_f32 v192, v192\n"
"v_mfma_f32_16x16x32_bf16 v[12:15], v[232:235], v[208:211], v[12:15]\n"
"s_waitcnt lgkmcnt(5)\n"
"v_mfma_f32_16x16x32_bf16 v[16:19], v[236:239], v[208:211], v[16:19]\n"
"v_exp_f32 v193, v193\n"
"v_mfma_f32_16x16x32_bf16 v[20:23], v[240:243], v[208:211], v[20:23]\n"
"s_waitcnt lgkmcnt(1)\n"
"v_mfma_f32_16x16x32_bf16 v[24:27], v[172:175], v[208:211], v[24:27]\n"
"v_exp_f32 v194, v194\n"
"v_mfma_f32_16x16x32_bf16 v[28:31], v[176:179], v[208:211], v[28:31]\n"
"v_exp_f32 v195, v195\n"
"v_mfma_f32_16x16x32_bf16 v[148:151], v[168:171], v[212:215], v[148:151]\n"
"v_cvt_pk_bf16_f32 v218, v192, v193\n"
"v_cvt_pk_bf16_f32 v219, v194, v195\n"
"v_mfma_f32_16x16x32_bf16 v[32:35], v[220:223], v[212:215], v[32:35]\n"
"v_rcp_f32 v146, v144\n"
"s_nop 0\n"
"v_pk_mul_f32 v[0:1], v[0:1], v[146:147] op_sel_hi:[1,0]\n"
"v_mfma_f32_16x16x32_bf16 v[36:39], v[224:227], v[212:215], v[36:39]\n"
"v_pk_mul_f32 v[2:3], v[2:3], v[146:147] op_sel_hi:[1,0]\n"
"v_pk_mul_f32 v[4:5], v[4:5], v[146:147] op_sel_hi:[1,0]\n"
"v_mfma_f32_16x16x32_bf16 v[40:43], v[228:231], v[212:215], v[40:43]\n"
"v_pk_mul_f32 v[6:7], v[6:7], v[146:147] op_sel_hi:[1,0]\n"
"v_cvt_pk_bf16_f32 v0, v0, v1\n"
"v_mfma_f32_16x16x32_bf16 v[44:47], v[232:235], v[212:215], v[44:47]\n"
"v_cvt_pk_bf16_f32 v1, v2, v3\n"
"v_cvt_pk_bf16_f32 v2, v4, v5\n"
"v_mfma_f32_16x16x32_bf16 v[48:51], v[236:239], v[212:215], v[48:51]\n"
"v_cvt_pk_bf16_f32 v3, v6, v7\n"
"buffer_store_dwordx4 v[0:3], v252, s[64:67], 0 offen offset:0 sc1\n"
"v_mfma_f32_16x16x32_bf16 v[52:55], v[240:243], v[212:215], v[52:55]\n"
"v_pk_mul_f32 v[8:9], v[8:9], v[146:147] op_sel_hi:[1,0]\n"
"v_pk_mul_f32 v[10:11], v[10:11], v[146:147] op_sel_hi:[1,0]\n"
"v_mfma_f32_16x16x32_bf16 v[56:59], v[172:175], v[212:215], v[56:59]\n"
"v_pk_mul_f32 v[12:13], v[12:13], v[146:147] op_sel_hi:[1,0]\n"
"v_pk_mul_f32 v[14:15], v[14:15], v[146:147] op_sel_hi:[1,0]\n"
"v_mfma_f32_16x16x32_bf16 v[60:63], v[176:179], v[212:215], v[60:63]\n"
"v_cvt_pk_bf16_f32 v8, v8, v9\n"
"v_cvt_pk_bf16_f32 v9, v10, v11\n"
"v_mfma_f32_16x16x32_bf16 v[152:155], v[168:171], v[216:219], v[152:155]\n"
"v_cvt_pk_bf16_f32 v10, v12, v13\n"
"v_cvt_pk_bf16_f32 v11, v14, v15\n"
"v_mfma_f32_16x16x32_bf16 v[64:67], v[220:223], v[216:219], v[64:67]\n"
"buffer_store_dwordx4 v[8:11], v252, s[64:67], 0 offen offset:3072 sc1\n"
"v_pk_mul_f32 v[16:17], v[16:17], v[146:147] op_sel_hi:[1,0]\n"
"v_mfma_f32_16x16x32_bf16 v[68:71], v[224:227], v[216:219], v[68:71]\n"
"v_pk_mul_f32 v[18:19], v[18:19], v[146:147] op_sel_hi:[1,0]\n"
"v_pk_mul_f32 v[20:21], v[20:21], v[146:147] op_sel_hi:[1,0]\n"
"v_mfma_f32_16x16x32_bf16 v[72:75], v[228:231], v[216:219], v[72:75]\n"
"v_pk_mul_f32 v[22:23], v[22:23], v[146:147] op_sel_hi:[1,0]\n"
"v_cvt_pk_bf16_f32 v16, v16, v17\n"
"v_mfma_f32_16x16x32_bf16 v[76:79], v[232:235], v[216:219], v[76:79]\n"
"v_cvt_pk_bf16_f32 v17, v18, v19\n"
"v_cvt_pk_bf16_f32 v18, v20, v21\n"
"v_mfma_f32_16x16x32_bf16 v[80:83], v[236:239], v[216:219], v[80:83]\n"
"v_cvt_pk_bf16_f32 v19, v22, v23\n"
"buffer_store_dwordx4 v[16:19], v252, s[64:67], s81 offen offset:2048 sc1\n"
"v_mfma_f32_16x16x32_bf16 v[84:87], v[240:243], v[216:219], v[84:87]\n"
"v_pk_mul_f32 v[24:25], v[24:25], v[146:147] op_sel_hi:[1,0]\n"
"v_pk_mul_f32 v[26:27], v[26:27], v[146:147] op_sel_hi:[1,0]\n"
"v_mfma_f32_16x16x32_bf16 v[88:91], v[172:175], v[216:219], v[88:91]\n"
"v_pk_mul_f32 v[28:29], v[28:29], v[146:147] op_sel_hi:[1,0]\n"
"v_pk_mul_f32 v[30:31], v[30:31], v[146:147] op_sel_hi:[1,0]\n"
"v_mfma_f32_16x16x32_bf16 v[92:95], v[176:179], v[216:219], v[92:95]\n"
"v_cvt_pk_bf16_f32 v24, v24, v25\n"
"v_cvt_pk_bf16_f32 v25, v26, v27\n"
"v_cvt_pk_bf16_f32 v26, v28, v29\n"
"v_cvt_pk_bf16_f32 v27, v30, v31\n"
"buffer_store_dwordx4 v[24:27], v252, s[64:67], s82 offen offset:1024 sc1\n"
"v_rcp_f32 v150, v148\n"
"s_nop 0\n"
"v_pk_mul_f32 v[32:33], v[32:33], v[150:151] op_sel_hi:[1,0]\n"
"v_pk_mul_f32 v[34:35], v[34:35], v[150:151] op_sel_hi:[1,0]\n"
"v_pk_mul_f32 v[36:37], v[36:37], v[150:151] op_sel_hi:[1,0]\n"
"v_pk_mul_f32 v[38:39], v[38:39], v[150:151] op_sel_hi:[1,0]\n"
"v_cvt_pk_bf16_f32 v32, v32, v33\n"
"v_cvt_pk_bf16_f32 v33, v34, v35\n"
"v_cvt_pk_bf16_f32 v34, v36, v37\n"
"v_cvt_pk_bf16_f32 v35, v38, v39\n"
"buffer_store_dwordx4 v[32:35], v252, s[64:67], 0 offen offset:1024 sc1\n"
"v_pk_mul_f32 v[40:41], v[40:41], v[150:151] op_sel_hi:[1,0]\n"
"v_pk_mul_f32 v[42:43], v[42:43], v[150:151] op_sel_hi:[1,0]\n"
"v_pk_mul_f32 v[44:45], v[44:45], v[150:151] op_sel_hi:[1,0]\n"
"v_pk_mul_f32 v[46:47], v[46:47], v[150:151] op_sel_hi:[1,0]\n"
"v_cvt_pk_bf16_f32 v40, v40, v41\n"
"v_cvt_pk_bf16_f32 v41, v42, v43\n"
"v_cvt_pk_bf16_f32 v42, v44, v45\n"
"v_cvt_pk_bf16_f32 v43, v46, v47\n"
"buffer_store_dwordx4 v[40:43], v252, s[64:67], s81 offen offset:0 sc1\n"
"v_pk_mul_f32 v[48:49], v[48:49], v[150:151] op_sel_hi:[1,0]\n"
"v_pk_mul_f32 v[50:51], v[50:51], v[150:151] op_sel_hi:[1,0]\n"
"v_pk_mul_f32 v[52:53], v[52:53], v[150:151] op_sel_hi:[1,0]\n"
"v_pk_mul_f32 v[54:55], v[54:55], v[150:151] op_sel_hi:[1,0]\n"
"v_cvt_pk_bf16_f32 v48, v48, v49\n"
"v_cvt_pk_bf16_f32 v49, v50, v51\n"
"v_cvt_pk_bf16_f32 v50, v52, v53\n"
"v_cvt_pk_bf16_f32 v51, v54, v55\n"
"buffer_store_dwordx4 v[48:51], v252, s[64:67], s81 offen offset:3072 sc1\n"
"v_pk_mul_f32 v[56:57], v[56:57], v[150:151] op_sel_hi:[1,0]\n"
"v_pk_mul_f32 v[58:59], v[58:59], v[150:151] op_sel_hi:[1,0]\n"
"v_pk_mul_f32 v[60:61], v[60:61], v[150:151] op_sel_hi:[1,0]\n"
"v_pk_mul_f32 v[62:63], v[62:63], v[150:151] op_sel_hi:[1,0]\n"
"v_cvt_pk_bf16_f32 v56, v56, v57\n"
"v_cvt_pk_bf16_f32 v57, v58, v59\n"
"v_cvt_pk_bf16_f32 v58, v60, v61\n"
"v_cvt_pk_bf16_f32 v59, v62, v63\n"
"buffer_store_dwordx4 v[56:59], v252, s[64:67], s82 offen offset:2048 sc1\n"
"v_rcp_f32 v154, v152\n"
"s_nop 0\n"
"v_pk_mul_f32 v[64:65], v[64:65], v[154:155] op_sel_hi:[1,0]\n"
"v_pk_mul_f32 v[66:67], v[66:67], v[154:155] op_sel_hi:[1,0]\n"
"v_pk_mul_f32 v[68:69], v[68:69], v[154:155] op_sel_hi:[1,0]\n"
"v_pk_mul_f32 v[70:71], v[70:71], v[154:155] op_sel_hi:[1,0]\n"
"v_cvt_pk_bf16_f32 v64, v64, v65\n"
"v_cvt_pk_bf16_f32 v65, v66, v67\n"
"v_cvt_pk_bf16_f32 v66, v68, v69\n"
"v_cvt_pk_bf16_f32 v67, v70, v71\n"
"buffer_store_dwordx4 v[64:67], v252, s[64:67], 0 offen offset:2048 sc1\n"
"v_pk_mul_f32 v[72:73], v[72:73], v[154:155] op_sel_hi:[1,0]\n"
"v_pk_mul_f32 v[74:75], v[74:75], v[154:155] op_sel_hi:[1,0]\n"
"v_pk_mul_f32 v[76:77], v[76:77], v[154:155] op_sel_hi:[1,0]\n"
"v_pk_mul_f32 v[78:79], v[78:79], v[154:155] op_sel_hi:[1,0]\n"
"v_cvt_pk_bf16_f32 v72, v72, v73\n"
"v_cvt_pk_bf16_f32 v73, v74, v75\n"
"v_cvt_pk_bf16_f32 v74, v76, v77\n"
"v_cvt_pk_bf16_f32 v75, v78, v79\n"
"buffer_store_dwordx4 v[72:75], v252, s[64:67], s81 offen offset:1024 sc1\n"
"v_pk_mul_f32 v[80:81], v[80:81], v[154:155] op_sel_hi:[1,0]\n"
"v_pk_mul_f32 v[82:83], v[82:83], v[154:155] op_sel_hi:[1,0]\n"
"v_pk_mul_f32 v[84:85], v[84:85], v[154:155] op_sel_hi:[1,0]\n"
"v_pk_mul_f32 v[86:87], v[86:87], v[154:155] op_sel_hi:[1,0]\n"
"v_cvt_pk_bf16_f32 v80, v80, v81\n"
"v_cvt_pk_bf16_f32 v81, v82, v83\n"
"v_cvt_pk_bf16_f32 v82, v84, v85\n"
"v_cvt_pk_bf16_f32 v83, v86, v87\n"
"buffer_store_dwordx4 v[80:83], v252, s[64:67], s82 offen offset:0 sc1\n"
"v_pk_mul_f32 v[88:89], v[88:89], v[154:155] op_sel_hi:[1,0]\n"
"v_pk_mul_f32 v[90:91], v[90:91], v[154:155] op_sel_hi:[1,0]\n"
"v_pk_mul_f32 v[92:93], v[92:93], v[154:155] op_sel_hi:[1,0]\n"
"v_pk_mul_f32 v[94:95], v[94:95], v[154:155] op_sel_hi:[1,0]\n"
"v_cvt_pk_bf16_f32 v88, v88, v89\n"
"v_cvt_pk_bf16_f32 v89, v90, v91\n"
"v_cvt_pk_bf16_f32 v90, v92, v93\n"
"v_cvt_pk_bf16_f32 v91, v94, v95\n"
"buffer_store_dwordx4 v[88:91], v252, s[64:67], s82 offen offset:3072 sc1\n"
"s_waitcnt lgkmcnt(0)\n"
"v_readfirstlane_b32 s20, v244\n"
"v_readfirstlane_b32 s21, v245\n"
"v_mov_b32 v4, 0\n"
"v_mov_b32 v5, 0\n"
"v_mov_b32 v6, 0\n"
"v_mov_b32 v7, 0\n"
"s_nop 1\n"
"v_mfma_f32_16x16x32_bf16 v[12:15], v[4:7], v[0:3], v[144:147]\n"
"v_mfma_f32_16x16x32_bf16 v[20:23], v[4:7], v[32:35], v[148:151]\n"
"v_mfma_f32_16x16x32_bf16 v[28:31], v[4:7], v[64:67], v[152:155]\n"
"v_mfma_f32_16x16x32_bf16 v[12:15], v[4:7], v[8:11], v[12:15]\n"
"v_mfma_f32_16x16x32_bf16 v[20:23], v[4:7], v[40:43], v[20:23]\n"
"v_mfma_f32_16x16x32_bf16 v[28:31], v[4:7], v[72:75], v[28:31]\n"
"v_mfma_f32_16x16x32_bf16 v[12:15], v[4:7], v[16:19], v[12:15]\n"
"v_mfma_f32_16x16x32_bf16 v[20:23], v[4:7], v[48:51], v[20:23]\n"
"v_mfma_f32_16x16x32_bf16 v[28:31], v[4:7], v[80:83], v[28:31]\n"
"v_mfma_f32_16x16x32_bf16 v[12:15], v[4:7], v[24:27], v[12:15]\n"
"v_mfma_f32_16x16x32_bf16 v[20:23], v[4:7], v[56:59], v[20:23]\n"
"v_mfma_f32_16x16x32_bf16 v[28:31], v[4:7], v[88:91], v[28:31]\n"
"s_nop 6\n"
"v_add_f32 v12, v12, v20\n"
"v_add_f32 v12, v12, v28\n"
"v_cmp_ngt_f32 vcc, 0x53800000, v12\n"
"s_cbranch_vccnz .LkLSLOW\n"
".LkLPUB:\n"
"v_mov_b32 v76, v252\n"
"v_mov_b32 v4, s20\n"
"v_mov_b32 v5, s21\n"
"v_mov_b32 v6, s80\n"
"v_mov_b32 v79, 0xb0000034\n"
"s_lshl_b32 s81, s38, 8\n"
"s_add_u32 s81, s81, 1024\n"
"v_mov_b32 v77, s81\n"
"s_cmp_eq_u32 s41, 3\n"
"s_cselect_b32 s83, 4, 32\n"
"s_cselect_b32 s29, 0, 7168\n"
"s_lshr_b32 s84, s38, 1\n"
"s_mul_i32 s84, s84, 57344\n"
"s_lshl_b32 s85, s38, 12\n"
"s_add_u32 s84, s84, s85\n"
"s_add_u32 s84, s84, 2048\n"
"s_mul_i32 s85, s38, 6144\n"
"s_add_u32 s85, s85, 133120\n"
"v_and_b32 v48, 1, v255\n"
"v_and_b32 v49, 3, v255\n"
"v_lshlrev_b32 v50, 1, v49\n"
"v_bfe_u32 v51, v255, 3, 2\n"
"v_xor_b32 v52, v50, v51\n"
"v_bfe_u32 v53, v255, 2, 2\n"
"v_lshrrev_b32 v54, 4, v255\n"
"v_lshlrev_b32 v55, 9, v54\n"
"v_lshl_add_u32 v56, v53, 7, v55\n"
"v_lshl_add_u32 v57, v52, 4, v56\n"
"v_lshl_add_u32 v28, v48, 3, v57\n"
"v_lshrrev_b32 v58, 3, v255\n"
"v_lshlrev_b32 v21, 5, v58\n"
"v_and_b32 v59, 7, v255\n"
"v_xor_b32 v60, v59, v54\n"
"v_lshlrev_b32 v22, 4, v60\n"
"v_mad_u32_u24 v20, v21, s83, v22\n"
"v_add_u32 v21, s29, v20\n"
"v_add_u32 v22, s29, v21\n"
"v_add_u32 v23, s29, v22\n"
"v_xor_b32 v29, 8, v28\n"
"v_xor_b32 v30, 16, v28\n"
"v_xor_b32 v31, 24, v28\n"
"v_add_u32 v24, s84, v28\n"
"v_add_u32 v25, s84, v29\n"
"v_add_u32 v26, s84, v30\n"
"v_add_u32 v27, s84, v31\n"
"v_add_u32 v28, s85, v28\n"
"v_add_u32 v29, s85, v29\n"
"v_add_u32 v30, s85, v30\n"
"v_add_u32 v31, s85, v31\n"
"s_mov_b32 s86, 0x40000\n"
".LkLCY:\n"
"ds_read_b32 v78, v77\n"
"s_waitcnt lgkmcnt(0)\n"
"v_cmp_gt_i32 vcc, 0xb0000032, v78\n"
"s_cbranch_vccz .LkLCYX\n"
"s_sub_u32 s86, s86, 1\n"
"s_cbranch_scc0 .LkLCY\n"
".LkLCYX:\n"
"ds_read_b64_tr_b16 v[32:33], v24 offset:0\n"
"ds_read_b64_tr_b16 v[34:35], v24 offset:2048\n"
"ds_read_b64_tr_b16 v[36:37], v25 offset:0\n"
"ds_read_b64_tr_b16 v[38:39], v25 offset:2048\n"
"ds_read_b64_tr_b16 v[40:41], v26 offset:0\n"
"ds_read_b64_tr_b16 v[42:43], v26 offset:2048\n"
"ds_read_b64_tr_b16 v[44:45], v27 offset:0\n"
"ds_read_b64_tr_b16 v[46:47], v27 offset:2048\n"
"ds_read_b64_tr_b16 v[80:81], v24 offset:8192\n"
"ds_read_b64_tr_b16 v[82:83], v24 offset:10240\n"
"ds_read_b64_tr_b16 v[84:85], v25 offset:8192\n"
"ds_read_b64_tr_b16 v[86:87], v25 offset:10240\n"
"ds_read_b64_tr_b16 v[88:89], v26 offset:8192\n"
"ds_read_b64_tr_b16 v[90:91], v26 offset:10240\n"
"ds_read_b64_tr_b16 v[92:93], v27 offset:8192\n"
"ds_read_b64_tr_b16 v[94:95], v27 offset:10240\n"
"ds_read_b64_tr_b16 v[96:97], v24 offset:16384\n"
"ds_read_b64_tr_b16 v[98:99], v24 offset:18432\n"
"ds_read_b64_tr_b16 v[100:101], v25 offset:16384\n"
"ds_read_b64_tr_b16 v[102:103], v25 offset:18432\n"
"ds_read_b64_tr_b16 v[104:105], v26 offset:16384\n"
"ds_read_b64_tr_b16 v[106:107], v26 offset:18432\n"
"ds_read_b64_tr_b16 v[108:109], v27 offset:16384\n"
"ds_read_b64_tr_b16 v[110:111], v27 offset:18432\n"
"ds_read_b64_tr_b16 v[112:113], v24 offset:24576\n"
"ds_read_b64_tr_b16 v[114:115], v24 offset:26624\n"
"ds_read_b64_tr_b16 v[116:117], v25 offset:24576\n"
"ds_read_b64_tr_b16 v[118:119], v25 offset:26624\n"
"ds_read_b64_tr_b16 v[120:121], v26 offset:24576\n"
"ds_read_b64_tr_b16 v[122:123], v26 offset:26624\n"
"ds_read_b64_tr_b16 v[124:125], v27 offset:24576\n"
"ds_read_b64_tr_b16 v[126:127], v27 offset:26624\n"
"ds_read_b64_tr_b16 v[128:129], v24 offset:32768\n"
"ds_read_b64_tr_b16 v[130:131], v24 offset:34816\n"
"ds_read_b64_tr_b16 v[132:133], v25 offset:32768\n"
"ds_read_b64_tr_b16 v[134:135], v25 offset:34816\n"
"ds_read_b64_tr_b16 v[136:137], v26 offset:32768\n"
"ds_read_b64_tr_b16 v[138:139], v26 offset:34816\n"
"ds_read_b64_tr_b16 v[140:141], v27 offset:32768\n"
"ds_read_b64_tr_b16 v[142:143], v27 offset:34816\n"
"ds_read_b64_tr_b16 v[144:145], v24 offset:40960\n"
"ds_read_b64_tr_b16 v[146:147], v24 offset:43008\n"
"ds_read_b64_tr_b16 v[148:149], v25 offset:40960\n"
"ds_read_b64_tr_b16 v[150:151], v25 offset:43008\n"
"ds_read_b64_tr_b16 v[152:153], v26 offset:40960\n"
"ds_read_b64_tr_b16 v[154:155], v26 offset:43008\n"
"ds_read_b64_tr_b16 v[156:157], v27 offset:40960\n"
"ds_read_b64_tr_b16 v[158:159], v27 offset:43008\n"
"ds_read_b64_tr_b16 v[160:161], v24 offset:49152\n"
"ds_read_b64_tr_b16 v[162:163], v24 offset:51200\n"
"ds_read_b64_tr_b16 v[164:165], v25 offset:49152\n"
"ds_read_b64_tr_b16 v[166:167], v25 offset:51200\n"
"ds_read_b64_tr_b16 v[168:169], v26 offset:49152\n"
"ds_read_b64_tr_b16 v[170:171], v26 offset:51200\n"
"ds_read_b64_tr_b16 v[172:173], v27 offset:49152\n"
"ds_read_b64_tr_b16 v[174:175], v27 offset:51200\n"
"ds_read_b64_tr_b16 v[176:177], v28 offset:0\n"
"ds_read_b64_tr_b16 v[178:179], v28 offset:2048\n"
"ds_read_b64_tr_b16 v[180:181], v29 offset:0\n"
"ds_read_b64_tr_b16 v[182:183], v29 offset:2048\n"
"ds_read_b64_tr_b16 v[184:185], v30 offset:0\n"
"ds_read_b64_tr_b16 v[186:187], v30 offset:2048\n"
"ds_read_b64_tr_b16 v[188:189], v31 offset:0\n"
"ds_read_b64_tr_b16 v[190:191], v31 offset:2048\n"
"s_mov_b64 exec, 1\n"
"s_waitcnt vmcnt(0)\n"
"buffer_store_dwordx2 v[4:5], v6, s[68:71], 0 offen sc1\n"
"s_mov_b64 exec, -1\n"
"s_setprio 0\n"
"s_waitcnt lgkmcnt(0)\n"
"s_lshl_b32 m0, s38, 8\n"
"s_nop 0\n"
"ds_write_addtid_b32 v79 offset:0\n"
"s_add_u32 m0, s84, 0\n"
"s_xor_b32 s29, s28, 16384\n"
"s_add_u32 s29, s29, s26\n"
"s_and_b32 s29, s29, 0x7e00\n"
"s_mul_i32 s29, s29, s83\n"
"buffer_load_dwordx4 v20, s[60:63], s29 offen sc1 lds\n"
"buffer_load_dwordx4 v21, s[60:63], s29 offen offset:1024 sc1 lds\n"
"buffer_load_dwordx4 v22, s[60:63], s29 offen offset:2048 sc1 lds\n"
"buffer_load_dwordx4 v23, s[60:63], s29 offen offset:3072 sc1 lds\n"
"s_add_u32 m0, s84, 8192\n"
"s_xor_b32 s29, s28, 17408\n"
"s_add_u32 s29, s29, s26\n"
"s_and_b32 s29, s29, 0x7e00\n"
"s_mul_i32 s29, s29, s83\n"
"buffer_load_dwordx4 v20, s[60:63], s29 offen sc1 lds\n"
"buffer_load_dwordx4 v21, s[60:63], s29 offen offset:1024 sc1 lds\n"
"buffer_load_dwordx4 v22, s[60:63], s29 offen offset:2048 sc1 lds\n"
"buffer_load_dwordx4 v23, s[60:63], s29 offen offset:3072 sc1 lds\n"
"s_add_u32 m0, s84, 16384\n"
"s_xor_b32 s29, s28, 18432\n"
"s_add_u32 s29, s29, s26\n"
"s_and_b32 s29, s29, 0x7e00\n"
"s_mul_i32 s29, s29, s83\n"
"buffer_load_dwordx4 v20, s[60:63], s29 offen sc1 lds\n"
"buffer_load_dwordx4 v21, s[60:63], s29 offen offset:1024 sc1 lds\n"
"buffer_load_dwordx4 v22, s[60:63], s29 offen offset:2048 sc1 lds\n"
"buffer_load_dwordx4 v23, s[60:63], s29 offen offset:3072 sc1 lds\n"
"s_add_u32 m0, s84, 24576\n"
"s_xor_b32 s29, s28, 19456\n"
"s_add_u32 s29, s29, s26\n"
"s_and_b32 s29, s29, 0x7e00\n"
"s_mul_i32 s29, s29, s83\n"
"buffer_load_dwordx4 v20, s[60:63], s29 offen sc1 lds\n"
"buffer_load_dwordx4 v21, s[60:63], s29 offen offset:1024 sc1 lds\n"
"buffer_load_dwordx4 v22, s[60:63], s29 offen offset:2048 sc1 lds\n"
"buffer_load_dwordx4 v23, s[60:63], s29 offen offset:3072 sc1 lds\n"
"s_add_u32 m0, s84, 32768\n"
"s_xor_b32 s29, s27, 0\n"
"s_add_u32 s29, s29, s26\n"
"s_and_b32 s29, s29, 0x7e00\n"
"s_mul_i32 s29, s29, s83\n"
"buffer_load_dwordx4 v20, s[60:63], s29 offen sc1 lds\n"
"buffer_load_dwordx4 v21, s[60:63], s29 offen offset:1024 sc1 lds\n"
"buffer_load_dwordx4 v22, s[60:63], s29 offen offset:2048 sc1 lds\n"
"buffer_load_dwordx4 v23, s[60:63], s29 offen offset:3072 sc1 lds\n"
"s_add_u32 m0, s84, 40960\n"
"s_xor_b32 s29, s27, 1024\n"
"s_add_u32 s29, s29, s26\n"
"s_and_b32 s29, s29, 0x7e00\n"
"s_mul_i32 s29, s29, s83\n"
"buffer_load_dwordx4 v20, s[60:63], s29 offen sc1 lds\n"
"buffer_load_dwordx4 v21, s[60:63], s29 offen offset:1024 sc1 lds\n"
"buffer_load_dwordx4 v22, s[60:63], s29 offen offset:2048 sc1 lds\n"
"buffer_load_dwordx4 v23, s[60:63], s29 offen offset:3072 sc1 lds\n"
"s_add_u32 m0, s84, 49152\n"
"s_xor_b32 s29, s27, 2048\n"
"s_add_u32 s29, s29, s26\n"
"s_and_b32 s29, s29, 0x7e00\n"
"s_mul_i32 s29, s29, s83\n"
"buffer_load_dwordx4 v20, s[60:63], s29 offen sc1 lds\n"
"buffer_load_dwordx4 v21, s[60:63], s29 offen offset:1024 sc1 lds\n"
"buffer_load_dwordx4 v22, s[60:63], s29 offen offset:2048 sc1 lds\n"
"buffer_load_dwordx4 v23, s[60:63], s29 offen offset:3072 sc1 lds\n"
"s_add_u32 m0, s85, 0\n"
"s_xor_b32 s29, s27, 3072\n"
"s_add_u32 s29, s29, s26\n"
"s_and_b32 s29, s29, 0x7e00\n"
"s_mul_i32 s29, s29, s83\n"
"buffer_load_dwordx4 v20, s[60:63], s29 offen sc1 lds\n"
"buffer_load_dwordx4 v21, s[60:63], s29 offen offset:1024 sc1 lds\n"
"buffer_load_dwordx4 v22, s[60:63], s29 offen offset:2048 sc1 lds\n"
"buffer_load_dwordx4 v23, s[60:63], s29 offen offset:3072 sc1 lds\n"
"s_waitcnt vmcnt(28)\n"
"ds_read_b64_tr_b16 v[192:193], v24 offset:0\n"
"ds_read_b64_tr_b16 v[194:195], v24 offset:2048\n"
"ds_read_b64_tr_b16 v[196:197], v25 offset:0\n"
"ds_read_b64_tr_b16 v[198:199], v25 offset:2048\n"
"ds_read_b64_tr_b16 v[200:201], v26 offset:0\n"
"ds_read_b64_tr_b16 v[202:203], v26 offset:2048\n"
"ds_read_b64_tr_b16 v[204:205], v27 offset:0\n"
"ds_read_b64_tr_b16 v[206:207], v27 offset:2048\n"
"s_waitcnt lgkmcnt(0)\n"
"s_add_u32 m0, s84, 0\n"
"s_xor_b32 s29, s27, 4096\n"
"s_add_u32 s29, s29, s26\n"
"s_and_b32 s29, s29, 0x7e00\n"
"s_mul_i32 s29, s29, s83\n"
"buffer_load_dwordx4 v20, s[60:63], s29 offen sc1 lds\n"
"buffer_load_dwordx4 v21, s[60:63], s29 offen offset:1024 sc1 lds\n"
"buffer_load_dwordx4 v22, s[60:63], s29 offen offset:2048 sc1 lds\n"
"buffer_load_dwordx4 v23, s[60:63], s29 offen offset:3072 sc1 lds\n"
"s_waitcnt vmcnt(28)\n"
"ds_read_b64_tr_b16 v[208:209], v24 offset:8192\n"
"ds_read_b64_tr_b16 v[210:211], v24 offset:10240\n"
"ds_read_b64_tr_b16 v[212:213], v25 offset:8192\n"
"ds_read_b64_tr_b16 v[214:215], v25 offset:10240\n"
"ds_read_b64_tr_b16 v[216:217], v26 offset:8192\n"
"ds_read_b64_tr_b16 v[218:219], v26 offset:10240\n"
"ds_read_b64_tr_b16 v[220:221], v27 offset:8192\n"
"ds_read_b64_tr_b16 v[222:223], v27 offset:10240\n"
"s_waitcnt lgkmcnt(0)\n"
"s_add_u32 m0, s84, 8192\n"
"s_xor_b32 s29, s27, 5120\n"
"s_add_u32 s29, s29, s26\n"
"s_and_b32 s29, s29, 0x7e00\n"
"s_mul_i32 s29, s29, s83\n"
"buffer_load_dwordx4 v20, s[60:63], s29 offen sc1 lds\n"
"buffer_load_dwordx4 v21, s[60:63], s29 offen offset:1024 sc1 lds\n"
"buffer_load_dwordx4 v22, s[60:63], s29 offen offset:2048 sc1 lds\n"
"buffer_load_dwordx4 v23, s[60:63], s29 offen offset:3072 sc1 lds\n"
"s_waitcnt vmcnt(28)\n"
"ds_read_b64_tr_b16 v[224:225], v24 offset:16384\n"
"ds_read_b64_tr_b16 v[226:227], v24 offset:18432\n"
"ds_read_b64_tr_b16 v[228:229], v25 offset:16384\n"
"ds_read_b64_tr_b16 v[230:231], v25 offset:18432\n"
"ds_read_b64_tr_b16 v[232:233], v26 offset:16384\n"
"ds_read_b64_tr_b16 v[234:235], v26 offset:18432\n"
"ds_read_b64_tr_b16 v[236:237], v27 offset:16384\n"
"ds_read_b64_tr_b16 v[238:239], v27 offset:18432\n"
"s_waitcnt lgkmcnt(0)\n"
"s_add_u32 m0, s84, 16384\n"
"s_xor_b32 s29, s27, 6144\n"
"s_add_u32 s29, s29, s26\n"
"s_and_b32 s29, s29, 0x7e00\n"
"s_mul_i32 s29, s29, s83\n"
"buffer_load_dwordx4 v20, s[60:63], s29 offen sc1 lds\n"
"buffer_load_dwordx4 v21, s[60:63], s29 offen offset:1024 sc1 lds\n"
"buffer_load_dwordx4 v22, s[60:63], s29 offen offset:2048 sc1 lds\n"
"buffer_load_dwordx4 v23, s[60:63], s29 offen offset:3072 sc1 lds\n"
"s_waitcnt vmcnt(28)\n"
"ds_read_b64_tr_b16 v[240:241], v24 offset:24576\n"
"ds_read_b64_tr_b16 v[242:243], v24 offset:26624\n"
"ds_read_b64_tr_b16 v[244:245], v25 offset:24576\n"
"ds_read_b64_tr_b16 v[246:247], v25 offset:26624\n"
"ds_read_b64_tr_b16 v[248:249], v26 offset:24576\n"
"ds_read_b64_tr_b16 v[250:251], v26 offset:26624\n"
"ds_read_b64_tr_b16 v[252:253], v27 offset:24576\n"
"ds_read_b64_tr_b16 v[254:255], v27 offset:26624\n"
"s_waitcnt lgkmcnt(0)\n"
"s_add_u32 m0, s84, 24576\n"
"s_xor_b32 s29, s27, 7168\n"
"s_add_u32 s29, s29, s26\n"
"s_and_b32 s29, s29, 0x7e00\n"
"s_mul_i32 s29, s29, s83\n"
"buffer_load_dwordx4 v20, s[60:63], s29 offen sc1 lds\n"
"buffer_load_dwordx4 v21, s[60:63], s29 offen offset:1024 sc1 lds\n"
"buffer_load_dwordx4 v22, s[60:63], s29 offen offset:2048 sc1 lds\n"
"buffer_load_dwordx4 v23, s[60:63], s29 offen offset:3072 sc1 lds\n"
"s_mov_b32 s86, 0x40000\n"
".LkLCF:\n"
"ds_read_b32 v78, v77\n"
"s_waitcnt lgkmcnt(0)\n"
"v_cmp_gt_i32 vcc, 0xb0000037, v78\n"
"s_cbranch_vccz .LkLCFX\n"
"s_sub_u32 s86, s86, 1\n"
"s_cbranch_scc0 .LkLCF\n"
".LkLCFX:\n"
"v_mov_b32 v79, v20\n"
"s_lshl_b32 s78, s83, 8\n"
"s_sub_u32 s78, s78, 1024\n"
"v_mov_b32 v78, 0xb0000039\n"
"s_xor_b32 s29, s27, 8192\n"
"s_add_u32 s29, s29, s26\n"
"s_and_b32 s29, s29, 0x7e00\n"
"s_mul_i32 s29, s29, 3\n"
"buffer_load_dwordx4 v[48:51], v76, s[52:55], s29 offen offset:0 sc1\n"
"buffer_load_dwordx4 v[52:55], v76, s[52:55], s29 offen offset:1024 sc1\n"
"buffer_load_dwordx4 v[56:59], v76, s[52:55], s29 offen offset:2048 sc1\n"
"s_xor_b32 s29, s27, 9216\n"
"s_add_u32 s29, s29, s26\n"
"s_and_b32 s29, s29, 0x7e00\n"
"s_mul_i32 s29, s29, 3\n"
"buffer_load_dwordx4 v[60:63], v76, s[52:55], s29 offen offset:0 sc1\n"
"s_waitcnt vmcnt(3)\n"
"v_mfma_f32_16x16x32_bf16 v[0:3], v[48:51], v[32:35], 0\n"
"v_mfma_f32_16x16x32_bf16 v[4:7], v[48:51], v[36:39], 0\n"
"v_mfma_f32_16x16x32_bf16 v[8:11], v[48:51], v[40:43], 0\n"
"v_mfma_f32_16x16x32_bf16 v[12:15], v[48:51], v[44:47], 0\n"
"buffer_load_dwordx4 v[48:51], v76, s[52:55], s29 offen offset:1024 sc1\n"
"s_waitcnt vmcnt(3)\n"
"v_mfma_f32_16x16x32_bf16 v[28:31], v[52:55], v[44:47], 0\n"
"v_mfma_f32_16x16x32_bf16 v[24:27], v[52:55], v[40:43], 0\n"
"v_mfma_f32_16x16x32_bf16 v[20:23], v[52:55], v[36:39], 0\n"
"v_mfma_f32_16x16x32_bf16 v[16:19], v[52:55], v[32:35], 0\n"
"buffer_load_dwordx4 v[52:55], v76, s[52:55], s29 offen offset:2048 sc1\n"
"s_waitcnt vmcnt(3)\n"
"v_mfma_f32_16x16x32_bf16 v[32:35], v[56:59], v[32:35], 0\n"
"v_mfma_f32_16x16x32_bf16 v[36:39], v[56:59], v[36:39], 0\n"
"v_mfma_f32_16x16x32_bf16 v[40:43], v[56:59], v[40:43], 0\n"
"v_mfma_f32_16x16x32_bf16 v[44:47], v[56:59], v[44:47], 0\n"
"s_xor_b32 s29, s27, 10240\n"
"s_add_u32 s29, s29, s26\n"
"s_and_b32 s29, s29, 0x7e00\n"
"s_mul_i32 s29, s29, 3\n"
"buffer_load_dwordx4 v[56:59], v76, s[52:55], s29 offen offset:0 sc1\n"
"s_waitcnt vmcnt(7)\n"
"s_lshl_b32 m0, s38, 8\n"
"s_nop 0\n"
"ds_write_addtid_b32 v78 offset:0\n"
"s_waitcnt vmcnt(3)\n"
"v_mfma_f32_16x16x32_bf16 v[12:15], v[60:63], v[92:95], v[12:15]\n"
"v_mfma_f32_16x16x32_bf16 v[8:11], v[60:63], v[88:91], v[8:11]\n"
"v_mfma_f32_16x16x32_bf16 v[4:7], v[60:63], v[84:87], v[4:7]\n"
"v_mfma_f32_16x16x32_bf16 v[0:3], v[60:63], v[80:83], v[0:3]\n"
"buffer_load_dwordx4 v[60:63], v76, s[52:55], s29 offen offset:1024 sc1\n"
"s_waitcnt vmcnt(3)\n"
"v_mfma_f32_16x16x32_bf16 v[16:19], v[48:51], v[80:83], v[16:19]\n"
"v_mfma_f32_16x16x32_bf16 v[20:23], v[48:51], v[84:87], v[20:23]\n"
"v_mfma_f32_16x16x32_bf16 v[24:27], v[48:51], v[88:91], v[24:27]\n"
"v_mfma_f32_16x16x32_bf16 v[28:31], v[48:51], v[92:95], v[28:31]\n"
"buffer_load_dwordx4 v[48:51], v76, s[52:55], s29 offen offset:2048 sc1\n"
"s_waitcnt vmcnt(3)\n"
"v_mfma_f32_16x16x32_bf16 v[44:47], v[52:55], v[92:95], v[44:47]\n"
"v_mfma_f32_16x16x32_bf16 v[40:43], v[52:55], v[88:91], v[40:43]\n"
"v_mfma_f32_16x16x32_bf16 v[36:39], v[52:55], v[84:87], v[36:39]\n"
"v_mfma_f32_16x16x32_bf16 v[32:35], v[52:55], v[80:83], v[32:35]\n"
"s_xor_b32 s29, s27, 11264\n"
"s_add_u32 s29, s29, s26\n"
"s_and_b32 s29, s29, 0x7e00\n"
"s_mul_i32 s29, s29, 3\n"
"buffer_load_dwordx4 v[52:55], v76, s[52:55], s29 offen offset:0 sc1\n"
"s_waitcnt vmcnt(3)\n"
"v_mfma_f32_16x16x32_bf16 v[0:3], v[56:59], v[96:99], v[0:3]\n"
"v_mfma_f32_16x16x32_bf16 v[4:7], v[56:59], v[100:103], v[4:7]\n"
"v_mfma_f32_16x16x32_bf16 v[8:11], v[56:59], v[104:107], v[8:11]\n"
"v_mfma_f32_16x16x32_bf16 v[12:15], v[56:59], v[108:111], v[12:15]\n"
"buffer_load_dwordx4 v[56:59], v76, s[52:55], s29 offen offset:1024 sc1\n"
"s_waitcnt vmcnt(3)\n"
"v_mfma_f32_16x16x32_bf16 v[28:31], v[60:63], v[108:111], v[28:31]\n"
"v_mfma_f32_16x16x32_bf16 v[24:27], v[60:63], v[104:107], v[24:27]\n"
"v_mfma_f32_16x16x32_bf16 v[20:23], v[60:63], v[100:103], v[20:23]\n"
"v_mfma_f32_16x16x32_bf16 v[16:19], v[60:63], v[96:99], v[16:19]\n"
"buffer_load_dwordx4 v[60:63], v76, s[52:55], s29 offen offset:2048 sc1\n"
"s_waitcnt vmcnt(3)\n"
"v_mfma_f32_16x16x32_bf16 v[32:35], v[48:51], v[96:99], v[32:35]\n"
"v_mfma_f32_16x16x32_bf16 v[36:39], v[48:51], v[100:103], v[36:39]\n"
"v_mfma_f32_16x16x32_bf16 v[40:43], v[48:51], v[104:107], v[40:43]\n"
"v_mfma_f32_16x16x32_bf16 v[44:47], v[48:51], v[108:111], v[44:47]\n"
"s_xor_b32 s29, s27, 12288\n"
"s_add_u32 s29, s29, s26\n"
"s_and_b32 s29, s29, 0x7e00\n"
"s_mul_i32 s29, s29, 3\n"
"buffer_load_dwordx4 v[48:51], v76, s[52:55], s29 offen offset:0 sc1\n"
"s_waitcnt vmcnt(3)\n"
"v_mfma_f32_16x16x32_bf16 v[12:15], v[52:55], v[124:127], v[12:15]\n"
"v_mfma_f32_16x16x32_bf16 v[8:11], v[52:55], v[120:123], v[8:11]\n"
"v_mfma_f32_16x16x32_bf16 v[4:7], v[52:55], v[116:119], v[4:7]\n"
"v_mfma_f32_16x16x32_bf16 v[0:3], v[52:55], v[112:115], v[0:3]\n"
"buffer_load_dwordx4 v[52:55], v76, s[52:55], s29 offen offset:1024 sc1\n"
"s_waitcnt vmcnt(3)\n"
"v_mfma_f32_16x16x32_bf16 v[16:19], v[56:59], v[112:115], v[16:19]\n"
"v_mfma_f32_16x16x32_bf16 v[20:23], v[56:59], v[116:119], v[20:23]\n"
"v_mfma_f32_16x16x32_bf16 v[24:27], v[56:59], v[120:123], v[24:27]\n"
"v_mfma_f32_16x16x32_bf16 v[28:31], v[56:59], v[124:127], v[28:31]\n"
"buffer_load_dwordx4 v[56:59], v76, s[52:55], s29 offen offset:2048 sc1\n"
"s_waitcnt vmcnt(3)\n"
"v_mfma_f32_16x16x32_bf16 v[44:47], v[60:63], v[124:127], v[44:47]\n"
"v_mfma_f32_16x16x32_bf16 v[40:43], v[60:63], v[120:123], v[40:43]\n"
"v_mfma_f32_16x16x32_bf16 v[36:39], v[60:63], v[116:119], v[36:39]\n"
"v_mfma_f32_16x16x32_bf16 v[32:35], v[60:63], v[112:115], v[32:35]\n"
"s_xor_b32 s29, s27, 13312\n"
"s_add_u32 s29, s29, s26\n"
"s_and_b32 s29, s29, 0x7e00\n"
"s_mul_i32 s29, s29, 3\n"
"buffer_load_dwordx4 v[60:63], v76, s[52:55], s29 offen offset:0 sc1\n"
"s_waitcnt vmcnt(3)\n"
"v_mfma_f32_16x16x32_bf16 v[0:3], v[48:51], v[128:131], v[0:3]\n"
"v_mfma_f32_16x16x32_bf16 v[4:7], v[48:51], v[132:135], v[4:7]\n"
"v_mfma_f32_16x16x32_bf16 v[8:11], v[48:51], v[136:139], v[8:11]\n"
"v_mfma_f32_16x16x32_bf16 v[12:15], v[48:51], v[140:143], v[12:15]\n"
"buffer_load_dwordx4 v[48:51], v76, s[52:55], s29 offen offset:1024 sc1\n"
"s_waitcnt vmcnt(3)\n"
"v_mfma_f32_16x16x32_bf16 v[28:31], v[52:55], v[140:143], v[28:31]\n"
"v_mfma_f32_16x16x32_bf16 v[24:27], v[52:55], v[136:139], v[24:27]\n"
"v_mfma_f32_16x16x32_bf16 v[20:23], v[52:55], v[132:135], v[20:23]\n"
"v_mfma_f32_16x16x32_bf16 v[16:19], v[52:55], v[128:131], v[16:19]\n"
"buffer_load_dwordx4 v[52:55], v76, s[52:55], s29 offen offset:2048 sc1\n"
"s_waitcnt vmcnt(3)\n"
"v_mfma_f32_16x16x32_bf16 v[32:35], v[56:59], v[128:131], v[32:35]\n"
"v_mfma_f32_16x16x32_bf16 v[36:39], v[56:59], v[132:135], v[36:39]\n"
"v_mfma_f32_16x16x32_bf16 v[40:43], v[56:59], v[136:139], v[40:43]\n"
"v_mfma_f32_16x16x32_bf16 v[44:47], v[56:59], v[140:143], v[44:47]\n"
"s_xor_b32 s29, s27, 14336\n"
"s_add_u32 s29, s29, s26\n"
"s_and_b32 s29, s29, 0x7e00\n"
"s_mul_i32 s29, s29, 3\n"
"buffer_load_dwordx4 v[56:59], v76, s[52:55], s29 offen offset:0 sc1\n"
"s_waitcnt vmcnt(3)\n"
"v_mfma_f32_16x16x32_bf16 v[12:15], v[60:63], v[156:159], v[12:15]\n"
"v_mfma_f32_16x16x32_bf16 v[8:11], v[60:63], v[152:155], v[8:11]\n"
"v_mfma_f32_16x16x32_bf16 v[4:7], v[60:63], v[148:151], v[4:7]\n"
"v_mfma_f32_16x16x32_bf16 v[0:3], v[60:63], v[144:147], v[0:3]\n"
"buffer_load_dwordx4 v[60:63], v76, s[52:55], s29 offen offset:1024 sc1\n"
"s_waitcnt vmcnt(3)\n"
"v_mfma_f32_16x16x32_bf16 v[16:19], v[48:51], v[144:147], v[16:19]\n"
"v_mfma_f32_16x16x32_bf16 v[20:23], v[48:51], v[148:151], v[20:23]\n"
"v_mfma_f32_16x16x32_bf16 v[24:27], v[48:51], v[152:155], v[24:27]\n"
"v_mfma_f32_16x16x32_bf16 v[28:31], v[48:51], v[156:159], v[28:31]\n"
"buffer_load_dwordx4 v[48:51], v76, s[52:55], s29 offen offset:2048 sc1\n"
"s_waitcnt vmcnt(3)\n"
"v_mfma_f32_16x16x32_bf16 v[44:47], v[52:55], v[156:159], v[44:47]\n"
"v_mfma_f32_16x16x32_bf16 v[40:43], v[52:55], v[152:155], v[40:43]\n"
"v_mfma_f32_16x16x32_bf16 v[36:39], v[52:55], v[148:151], v[36:39]\n"
"v_mfma_f32_16x16x32_bf16 v[32:35], v[52:55], v[144:147], v[32:35]\n"
"s_xor_b32 s29, s27, 15360\n"
"s_add_u32 s29, s29, s26\n"
"s_and_b32 s29, s29, 0x7e00\n"
"s_mul_i32 s29, s29, 3\n"
"buffer_load_dwordx4 v[52:55], v76, s[52:55], s29 offen offset:0 sc1\n"
"s_waitcnt vmcnt(3)\n"
"v_mfma_f32_16x16x32_bf16 v[0:3], v[56:59], v[160:163], v[0:3]\n"
"v_mfma_f32_16x16x32_bf16 v[4:7], v[56:59], v[164:167], v[4:7]\n"
"v_mfma_f32_16x16x32_bf16 v[8:11], v[56:59], v[168:171], v[8:11]\n"
"v_mfma_f32_16x16x32_bf16 v[12:15], v[56:59], v[172:175], v[12:15]\n"
"buffer_load_dwordx4 v[56:59], v76, s[52:55], s29 offen offset:1024 sc1\n"
"s_waitcnt vmcnt(3)\n"
"v_mfma_f32_16x16x32_bf16 v[28:31], v[60:63], v[172:175], v[28:31]\n"
"v_mfma_f32_16x16x32_bf16 v[24:27], v[60:63], v[168:171], v[24:27]\n"
"v_mfma_f32_16x16x32_bf16 v[20:23], v[60:63], v[164:167], v[20:23]\n"
"v_mfma_f32_16x16x32_bf16 v[16:19], v[60:63], v[160:163], v[16:19]\n"
"buffer_load_dwordx4 v[60:63], v76, s[52:55], s29 offen offset:2048 sc1\n"
"s_waitcnt vmcnt(3)\n"
"v_mfma_f32_16x16x32_bf16 v[32:35], v[48:51], v[160:163], v[32:35]\n"
"v_mfma_f32_16x16x32_bf16 v[36:39], v[48:51], v[164:167], v[36:39]\n"
"v_mfma_f32_16x16x32_bf16 v[40:43], v[48:51], v[168:171], v[40:43]\n"
"v_mfma_f32_16x16x32_bf16 v[44:47], v[48:51], v[172:175], v[44:47]\n"
"s_xor_b32 s29, s28, 16384\n"
"s_add_u32 s29, s29, s26\n"
"s_and_b32 s29, s29, 0x7e00\n"
"s_mul_i32 s29, s29, 3\n"
"buffer_load_dwordx4 v[48:51], v76, s[52:55], s29 offen offset:0 sc1\n"
"s_waitcnt vmcnt(3)\n"
"v_mfma_f32_16x16x32_bf16 v[12:15], v[52:55], v[188:191], v[12:15]\n"
"v_mfma_f32_16x16x32_bf16 v[8:11], v[52:55], v[184:187], v[8:11]\n"
"v_mfma_f32_16x16x32_bf16 v[4:7], v[52:55], v[180:183], v[4:7]\n"
"v_mfma_f32_16x16x32_bf16 v[0:3], v[52:55], v[176:179], v[0:3]\n"
"buffer_load_dwordx4 v[52:55], v76, s[52:55], s29 offen offset:1024 sc1\n"
"s_waitcnt vmcnt(3)\n"
"v_mfma_f32_16x16x32_bf16 v[16:19], v[56:59], v[176:179], v[16:19]\n"
"v_mfma_f32_16x16x32_bf16 v[20:23], v[56:59], v[180:183], v[20:23]\n"
"v_mfma_f32_16x16x32_bf16 v[24:27], v[56:59], v[184:187], v[24:27]\n"
"v_mfma_f32_16x16x32_bf16 v[28:31], v[56:59], v[188:191], v[28:31]\n"
"buffer_load_dwordx4 v[56:59], v76, s[52:55], s29 offen offset:2048 sc1\n"
"s_waitcnt vmcnt(3)\n"
"v_mfma_f32_16x16x32_bf16 v[44:47], v[60:63], v[188:191], v[44:47]\n"
"v_mfma_f32_16x16x32_bf16 v[40:43], v[60:63], v[184:187], v[40:43]\n"
"v_mfma_f32_16x16x32_bf16 v[36:39], v[60:63], v[180:183], v[36:39]\n"
"v_mfma_f32_16x16x32_bf16 v[32:35], v[60:63], v[176:179], v[32:35]\n"
"s_xor_b32 s29, s28, 17408\n"
"s_add_u32 s29, s29, s26\n"
"s_and_b32 s29, s29, 0x7e00\n"
"s_mul_i32 s29, s29, 3\n"
"buffer_load_dwordx4 v[60:63], v76, s[52:55], s29 offen offset:0 sc1\n"
"s_waitcnt vmcnt(3)\n"
"v_mfma_f32_16x16x32_bf16 v[0:3], v[48:51], v[192:195], v[0:3]\n"
"v_mfma_f32_16x16x32_bf16 v[4:7], v[48:51], v[196:199], v[4:7]\n"
"v_mfma_f32_16x16x32_bf16 v[8:11], v[48:51], v[200:203], v[8:11]\n"
"v_mfma_f32_16x16x32_bf16 v[12:15], v[48:51], v[204:207], v[12:15]\n"
"s_xor_b32 s29, s28, 18432\n"
"s_add_u32 s29, s29, s26\n"
"s_and_b32 s29, s29, 0x7e00\n"
"s_mul_i32 s29, s29, 3\n"
"buffer_load_dwordx4 v[48:51], v76, s[52:55], s29 offen offset:0 sc1\n"
"s_waitcnt vmcnt(3)\n"
"v_mfma_f32_16x16x32_bf16 v[28:31], v[52:55], v[204:207], v[28:31]\n"
"v_mfma_f32_16x16x32_bf16 v[24:27], v[52:55], v[200:203], v[24:27]\n"
"v_mfma_f32_16x16x32_bf16 v[20:23], v[52:55], v[196:199], v[20:23]\n"
"v_mfma_f32_16x16x32_bf16 v[16:19], v[52:55], v[192:195], v[16:19]\n"
"s_xor_b32 s29, s28, 19456\n"
"s_add_u32 s29, s29, s26\n"
"s_and_b32 s29, s29, 0x7e00\n"
"s_mul_i32 s29, s29, 3\n"
"buffer_load_dwordx4 v[52:55], v76, s[52:55], s29 offen offset:0 sc1\n"
"s_waitcnt vmcnt(3)\n"
"v_mfma_f32_16x16x32_bf16 v[32:35], v[56:59], v[192:195], v[32:35]\n"
"v_mfma_f32_16x16x32_bf16 v[36:39], v[56:59], v[196:199], v[36:39]\n"
"v_mfma_f32_16x16x32_bf16 v[40:43], v[56:59], v[200:203], v[40:43]\n"
"v_mfma_f32_16x16x32_bf16 v[44:47], v[56:59], v[204:207], v[44:47]\n"
"s_xor_b32 s29, s28, 17408\n"
"s_add_u32 s29, s29, s26\n"
"s_and_b32 s29, s29, 0x7e00\n"
"s_mul_i32 s29, s29, 3\n"
"buffer_load_dwordx4 v[56:59], v76, s[52:55], s29 offen offset:1024 sc1\n"
"s_waitcnt vmcnt(3)\n"
"v_mfma_f32_16x16x32_bf16 v[0:3], v[60:63], v[208:211], v[0:3]\n"
"v_mfma_f32_16x16x32_bf16 v[4:7], v[60:63], v[212:215], v[4:7]\n"
"v_mfma_f32_16x16x32_bf16 v[8:11], v[60:63], v[216:219], v[8:11]\n"
"v_mfma_f32_16x16x32_bf16 v[12:15], v[60:63], v[220:223], v[12:15]\n"
"s_xor_b32 s29, s28, 18432\n"
"s_add_u32 s29, s29, s26\n"
"s_and_b32 s29, s29, 0x7e00\n"
"s_mul_i32 s29, s29, 3\n"
"buffer_load_dwordx4 v[60:63], v76, s[52:55], s29 offen offset:1024 sc1\n"
"s_waitcnt vmcnt(3)\n"
"v_mfma_f32_16x16x32_bf16 v[0:3], v[48:51], v[224:227], v[0:3]\n"
"v_mfma_f32_16x16x32_bf16 v[4:7], v[48:51], v[228:231], v[4:7]\n"
"v_mfma_f32_16x16x32_bf16 v[8:11], v[48:51], v[232:235], v[8:11]\n"
"v_mfma_f32_16x16x32_bf16 v[12:15], v[48:51], v[236:239], v[12:15]\n"
"s_xor_b32 s29, s28, 19456\n"
"s_add_u32 s29, s29, s26\n"
"s_and_b32 s29, s29, 0x7e00\n"
"s_mul_i32 s29, s29, 3\n"
"buffer_load_dwordx4 v[48:51], v76, s[52:55], s29 offen offset:1024 sc1\n"
"s_waitcnt vmcnt(3)\n"
"v_mfma_f32_16x16x32_bf16 v[0:3], v[52:55], v[240:243], v[0:3]\n"
"v_mfma_f32_16x16x32_bf16 v[4:7], v[52:55], v[244:247], v[4:7]\n"
"v_mfma_f32_16x16x32_bf16 v[8:11], v[52:55], v[248:251], v[8:11]\n"
"v_mfma_f32_16x16x32_bf16 v[12:15], v[52:55], v[252:255], v[12:15]\n"
"s_xor_b32 s29, s28, 17408\n"
"s_add_u32 s29, s29, s26\n"
"s_and_b32 s29, s29, 0x7e00\n"
"s_mul_i32 s29, s29, 3\n"
"buffer_load_dwordx4 v[52:55], v76, s[52:55], s29 offen offset:2048 sc1\n"
"s_mov_b32 s86, 0x40000\n"
".LkLCX:\n"
"ds_read_b32 v78, v77\n"
"s_waitcnt lgkmcnt(0)\n"
"v_cmp_gt_i32 vcc, 0xb000003c, v78\n"
"s_cbranch_vccz .LkLCXX\n"
"s_sub_u32 s86, s86, 1\n"
"s_cbranch_scc0 .LkLCX\n"
".LkLCXX:\n"
"s_lshr_b32 s81, s38, 1\n"
"s_mul_i32 s81, s81, 49152\n"
"s_lshl_b32 s82, s38, 12\n"
"s_add_u32 m0, s81, s82\n"
"s_waitcnt vmcnt(3)\n"
"v_mfma_f32_16x16x32_bf16 v[16:19], v[56:59], v[208:211], v[16:19]\n"
"ds_write_addtid_b32 v0 offset:10240\n"
"v_mfma_f32_16x16x32_bf16 v[20:23], v[56:59], v[212:215], v[20:23]\n"
"ds_write_addtid_b32 v1 offset:10496\n"
"ds_write_addtid_b32 v2 offset:10752\n"
"v_mfma_f32_16x16x32_bf16 v[24:27], v[56:59], v[216:219], v[24:27]\n"
"ds_write_addtid_b32 v3 offset:11008\n"
"ds_write_addtid_b32 v4 offset:11264\n"
"v_mfma_f32_16x16x32_bf16 v[28:31], v[56:59], v[220:223], v[28:31]\n"
"ds_write_addtid_b32 v5 offset:11520\n"
"ds_write_addtid_b32 v6 offset:11776\n"
"s_xor_b32 s29, s28, 18432\n"
"s_add_u32 s29, s29, s26\n"
"s_and_b32 s29, s29, 0x7e00\n"
"s_mul_i32 s29, s29, 3\n"
"buffer_load_dwordx4 v[56:59], v76, s[52:55], s29 offen offset:2048 sc1\n"
"s_waitcnt vmcnt(3)\n"
"v_mfma_f32_16x16x32_bf16 v[16:19], v[60:63], v[224:227], v[16:19]\n"
"ds_write_addtid_b32 v7 offset:12032\n"
"ds_write_addtid_b32 v8 offset:12288\n"
"v_mfma_f32_16x16x32_bf16 v[20:23], v[60:63], v[228:231], v[20:23]\n"
"ds_write_addtid_b32 v9 offset:12544\n"
"ds_write_addtid_b32 v10 offset:12800\n"
"v_mfma_f32_16x16x32_bf16 v[24:27], v[60:63], v[232:235], v[24:27]\n"
"ds_write_addtid_b32 v11 offset:13056\n"
"ds_write_addtid_b32 v12 offset:13312\n"
"v_mfma_f32_16x16x32_bf16 v[28:31], v[60:63], v[236:239], v[28:31]\n"
"ds_write_addtid_b32 v13 offset:13568\n"
"ds_write_addtid_b32 v14 offset:13824\n"
"s_xor_b32 s29, s28, 19456\n"
"s_add_u32 s29, s29, s26\n"
"s_and_b32 s29, s29, 0x7e00\n"
"s_mul_i32 s29, s29, 3\n"
"buffer_load_dwordx4 v[60:63], v76, s[52:55], s29 offen offset:2048 sc1\n"
"s_waitcnt vmcnt(3)\n"
"v_mfma_f32_16x16x32_bf16 v[16:19], v[48:51], v[240:243], v[16:19]\n"
"ds_write_addtid_b32 v15 offset:14080\n"
"v_mfma_f32_16x16x32_bf16 v[20:23], v[48:51], v[244:247], v[20:23]\n"
"v_mfma_f32_16x16x32_bf16 v[24:27], v[48:51], v[248:251], v[24:27]\n"
"v_mfma_f32_16x16x32_bf16 v[28:31], v[48:51], v[252:255], v[28:31]\n"
"s_waitcnt vmcnt(2)\n"
"v_mfma_f32_16x16x32_bf16 v[32:35], v[52:55], v[208:211], v[32:35]\n"
"s_nop 1\n"
"ds_write_addtid_b32 v16 offset:18432\n"
"v_mfma_f32_16x16x32_bf16 v[36:39], v[52:55], v[212:215], v[36:39]\n"
"ds_write_addtid_b32 v17 offset:18688\n"
"ds_write_addtid_b32 v18 offset:18944\n"
"v_mfma_f32_16x16x32_bf16 v[40:43], v[52:55], v[216:219], v[40:43]\n"
"ds_write_addtid_b32 v19 offset:19200\n"
"ds_write_addtid_b32 v20 offset:19456\n"
"v_mfma_f32_16x16x32_bf16 v[44:47], v[52:55], v[220:223], v[44:47]\n"
"ds_write_addtid_b32 v21 offset:19712\n"
"ds_write_addtid_b32 v22 offset:19968\n"
"s_waitcnt vmcnt(1)\n"
"v_mfma_f32_16x16x32_bf16 v[32:35], v[56:59], v[224:227], v[32:35]\n"
"ds_write_addtid_b32 v23 offset:20224\n"
"ds_write_addtid_b32 v24 offset:20480\n"
"v_mfma_f32_16x16x32_bf16 v[36:39], v[56:59], v[228:231], v[36:39]\n"
"ds_write_addtid_b32 v25 offset:20736\n"
"ds_write_addtid_b32 v26 offset:20992\n"
"v_mfma_f32_16x16x32_bf16 v[40:43], v[56:59], v[232:235], v[40:43]\n"
"ds_write_addtid_b32 v27 offset:21248\n"
"ds_write_addtid_b32 v28 offset:21504\n"
"v_mfma_f32_16x16x32_bf16 v[44:47], v[56:59], v[236:239], v[44:47]\n"
"ds_write_addtid_b32 v29 offset:21760\n"
"ds_write_addtid_b32 v30 offset:22016\n"
"s_waitcnt vmcnt(0)\n"
"v_mfma_f32_16x16x32_bf16 v[32:35], v[60:63], v[240:243], v[32:35]\n"
"ds_write_addtid_b32 v31 offset:22272\n"
"v_mfma_f32_16x16x32_bf16 v[36:39], v[60:63], v[244:247], v[36:39]\n"
"v_mfma_f32_16x16x32_bf16 v[40:43], v[60:63], v[248:251], v[40:43]\n"
"v_mfma_f32_16x16x32_bf16 v[44:47], v[60:63], v[252:255], v[44:47]\n"
"s_nop 3\n"
"ds_write_addtid_b32 v32 offset:26624\n"
"ds_write_addtid_b32 v33 offset:26880\n"
"ds_write_addtid_b32 v34 offset:27136\n"
"ds_write_addtid_b32 v35 offset:27392\n"
"ds_write_addtid_b32 v36 offset:27648\n"
"ds_write_addtid_b32 v37 offset:27904\n"
"ds_write_addtid_b32 v38 offset:28160\n"
"ds_write_addtid_b32 v39 offset:28416\n"
"ds_write_addtid_b32 v40 offset:28672\n"
"ds_write_addtid_b32 v41 offset:28928\n"
"ds_write_addtid_b32 v42 offset:29184\n"
"ds_write_addtid_b32 v43 offset:29440\n"
"ds_write_addtid_b32 v44 offset:29696\n"
"ds_write_addtid_b32 v45 offset:29952\n"
"ds_write_addtid_b32 v46 offset:30208\n"
"ds_write_addtid_b32 v47 offset:30464\n"
"v_mov_b32 v79, 0xb000003c\n"
"s_lshl_b32 m0, s38, 8\n"
"s_cmp_eq_u32 s38, 3\n"
"ds_write_addtid_b32 v79 offset:0\n"
".LkLRED:\n"
"v_mbcnt_lo_u32_b32 v48, -1, 0\n"
"v_mbcnt_hi_u32_b32 v48, -1, v48\n"
"v_and_b32 v56, 3, v48\n"
"v_mul_u32_u24 v49, 0x104, v56\n"
"v_lshlrev_b32 v57, 8, v56\n"
"v_add_u32 v54, 0x400, v57\n"
"v_and_b32 v58, 15, v48\n"
"v_lshrrev_b32 v59, 4, v48\n"
"v_and_b32 v60, 1, v48\n"
"v_xor_b32 v61, v59, v60\n"
"v_lshl_add_u32 v62, v61, 2, s38\n"
"v_lshlrev_b32 v63, 8, v62\n"
"v_lshl_add_u32 v64, v58, 4, v63\n"
"v_add_u32 v50, 0x2800, v64\n"
"v_add_u32 v51, 0xe000, v50\n"
"v_lshrrev_b32 v65, 5, v48\n"
"v_bfe_u32 v66, v48, 2, 2\n"
"v_and_b32 v67, 1, v59\n"
"v_lshlrev_b32 v68, 4, v67\n"
"v_lshl_add_u32 v69, v66, 2, v68\n"
"v_add_u32 v70, s38, v69\n"
"v_lshlrev_b32 v71, 10, v70\n"
"v_lshl_add_u32 v72, v56, 5, v71\n"
"v_lshl_add_u32 v52, v65, 4, v72\n"
"s_mov_b32 s81, 0x8000\n"
"s_mov_b32 s86, 0x40000\n"
".LkLCC:\n"
"ds_read_b32 v53, v49\n"
"s_waitcnt lgkmcnt(0)\n"
"v_cmp_gt_i32 vcc, 0xb000003c, v53\n"
"s_cbranch_vccz .LkLCCX\n"
"s_sub_u32 s86, s86, 1\n"
"s_cbranch_scc0 .LkLCC\n"
".LkLCCX:\n"
"ds_read_b128 v[80:83], v50 offset:0\n"
"ds_read_b128 v[84:87], v50 offset:4096\n"
"ds_read_b128 v[88:91], v51 offset:0\n"
"ds_read_b128 v[92:95], v51 offset:4096\n"
"ds_read_b128 v[96:99], v50 offset:8192\n"
"ds_read_b128 v[100:103], v50 offset:12288\n"
"ds_read_b128 v[104:107], v51 offset:8192\n"
"ds_read_b128 v[108:111], v51 offset:12288\n"
"ds_read_b128 v[112:115], v50 offset:16384\n"
"ds_read_b128 v[116:119], v50 offset:20480\n"
"ds_read_b128 v[120:123], v51 offset:16384\n"
"ds_read_b128 v[124:127], v51 offset:20480\n"
"s_waitcnt lgkmcnt(10)\n"
"v_pk_add_f32 v[80:81], v[80:81], v[84:85]\n"
"v_pk_add_f32 v[82:83], v[82:83], v[86:87]\n"
"s_waitcnt lgkmcnt(8)\n"
"v_pk_add_f32 v[88:89], v[88:89], v[92:93]\n"
"v_pk_add_f32 v[90:91], v[90:91], v[94:95]\n"
"v_pk_add_f32 v[80:81], v[80:81], v[88:89]\n"
"v_pk_add_f32 v[82:83], v[82:83], v[90:91]\n"
"s_waitcnt lgkmcnt(6)\n"
"v_pk_add_f32 v[96:97], v[96:97], v[100:101]\n"
"v_pk_add_f32 v[98:99], v[98:99], v[102:103]\n"
"s_waitcnt lgkmcnt(4)\n"
"v_pk_add_f32 v[104:105], v[104:105], v[108:109]\n"
"v_pk_add_f32 v[106:107], v[106:107], v[110:111]\n"
"v_pk_add_f32 v[96:97], v[96:97], v[104:105]\n"
"v_pk_add_f32 v[98:99], v[98:99], v[106:107]\n"
"s_waitcnt lgkmcnt(2)\n"
"v_pk_add_f32 v[112:113], v[112:113], v[116:117]\n"
"v_pk_add_f32 v[114:115], v[114:115], v[118:119]\n"
"s_waitcnt lgkmcnt(0)\n"
"v_pk_add_f32 v[120:121], v[120:121], v[124:125]\n"
"v_pk_add_f32 v[122:123], v[122:123], v[126:127]\n"
"v_pk_add_f32 v[112:113], v[112:113], v[120:121]\n"
"v_pk_add_f32 v[114:115], v[114:115], v[122:123]\n"
"s_barrier\n"
"ds_read_b128 v[128:131], v50 offset:24576\n"
"ds_read_b128 v[132:135], v50 offset:28672\n"
"ds_read_b128 v[136:139], v51 offset:24576\n"
"ds_read_b128 v[140:143], v51 offset:28672\n"
"ds_read_b128 v[144:147], v50 offset:32768\n"
"ds_read_b128 v[148:151], v50 offset:36864\n"
"ds_read_b128 v[152:155], v51 offset:32768\n"
"ds_read_b128 v[156:159], v51 offset:36864\n"
"ds_read_b128 v[160:163], v50 offset:40960\n"
"ds_read_b128 v[164:167], v50 offset:45056\n"
"ds_read_b128 v[168:171], v51 offset:40960\n"
"ds_read_b128 v[172:175], v51 offset:45056\n"
"s_waitcnt lgkmcnt(11)\n"
"v_pk_add_f32 v[80:81], v[80:81], v[128:129]\n"
"v_pk_add_f32 v[82:83], v[82:83], v[130:131]\n"
"s_waitcnt lgkmcnt(10)\n"
"v_pk_add_f32 v[80:81], v[80:81], v[132:133]\n"
"v_pk_add_f32 v[82:83], v[82:83], v[134:135]\n"
"s_waitcnt lgkmcnt(8)\n"
"v_pk_add_f32 v[136:137], v[136:137], v[140:141]\n"
"v_pk_add_f32 v[138:139], v[138:139], v[142:143]\n"
"v_pk_add_f32 v[80:81], v[80:81], v[136:137]\n"
"v_pk_add_f32 v[82:83], v[82:83], v[138:139]\n"
"v_cvt_pk_bf16_f32 v56, v80, v81\n"
"v_cvt_pk_bf16_f32 v57, v82, v83\n"
"s_waitcnt lgkmcnt(7)\n"
"v_pk_add_f32 v[96:97], v[96:97], v[144:145]\n"
"v_pk_add_f32 v[98:99], v[98:99], v[146:147]\n"
"s_waitcnt lgkmcnt(6)\n"
"v_pk_add_f32 v[96:97], v[96:97], v[148:149]\n"
"v_pk_add_f32 v[98:99], v[98:99], v[150:151]\n"
"s_waitcnt lgkmcnt(4)\n"
"v_pk_add_f32 v[152:153], v[152:153], v[156:157]\n"
"v_pk_add_f32 v[154:155], v[154:155], v[158:159]\n"
"v_pk_add_f32 v[96:97], v[96:97], v[152:153]\n"
"v_pk_add_f32 v[98:99], v[98:99], v[154:155]\n"
"v_cvt_pk_bf16_f32 v58, v96, v97\n"
"v_cvt_pk_bf16_f32 v59, v98, v99\n"
"s_nop 0\n"
"v_permlane16_swap_b32 v56, v58\n"
"v_permlane16_swap_b32 v57, v59\n"
"buffer_store_dwordx4 v[56:59], v52, s[72:75], 0 offen sc1\n"
"s_waitcnt lgkmcnt(3)\n"
"v_pk_add_f32 v[112:113], v[112:113], v[160:161]\n"
"v_pk_add_f32 v[114:115], v[114:115], v[162:163]\n"
"s_waitcnt lgkmcnt(2)\n"
"v_pk_add_f32 v[112:113], v[112:113], v[164:165]\n"
"v_pk_add_f32 v[114:115], v[114:115], v[166:167]\n"
"s_waitcnt lgkmcnt(0)\n"
"v_pk_add_f32 v[168:169], v[168:169], v[172:173]\n"
"v_pk_add_f32 v[170:171], v[170:171], v[174:175]\n"
"v_pk_add_f32 v[112:113], v[112:113], v[168:169]\n"
"v_pk_add_f32 v[114:115], v[114:115], v[170:171]\n"
"v_cvt_pk_bf16_f32 v60, v112, v113\n"
"v_cvt_pk_bf16_f32 v61, v114, v115\n"
"v_cvt_pk_bf16_f32 v62, v112, v113\n"
"v_cvt_pk_bf16_f32 v63, v114, v115\n"
"s_nop 0\n"
"v_permlane16_swap_b32 v60, v62\n"
"v_permlane16_swap_b32 v61, v63\n"
"s_mov_b32 exec_lo, 0xffff\n"
"s_mov_b32 exec_hi, 0xffff\n"
"buffer_store_dwordx4 v[60:63], v52, s[72:75], s81 offen sc1\n"
"s_mov_b64 exec, -1\n"
"s_branch .LkLEND\n"
".LkLBUMP:\n"
"s_cmp_lg_u32 s43, 0\n"
"s_cbranch_scc1 .LkLEND\n"
"s_cmp_lg_u32 s39, 7\n"
"s_cbranch_scc1 .LkLEND\n"
"s_mov_b32 s86, 0x8000\n"
".LkLBP:\n"
"s_waitcnt vmcnt(0)\n"
"v_cmp_ne_u64 s[78:79], s[20:21], v[96:97]\n"
"v_cmp_ne_u64 s[80:81], s[20:21], v[98:99]\n"
"v_cmp_ne_u64 s[82:83], s[20:21], v[100:101]\n"
"v_cmp_ne_u64 s[84:85], s[20:21], v[102:103]\n"
"s_or_b64 s[78:79], s[78:79], s[80:81]\n"
"s_or_b64 s[82:83], s[82:83], s[84:85]\n"
"s_or_b64 s[78:79], s[78:79], s[82:83]\n"
"s_cbranch_scc0 .LkLBD\n"
"buffer_load_dwordx2 v[96:97], v104, s[68:71], 0 offen sc1\n"
"buffer_load_dwordx2 v[98:99], v105, s[68:71], 0 offen sc1\n"
"buffer_load_dwordx2 v[100:101], v106, s[68:71], 0 offen sc1\n"
"buffer_load_dwordx2 v[102:103], v107, s[68:71], 0 offen sc1\n"
"s_sub_u32 s86, s86, 1\n"
"s_cbranch_scc0 .LkLBP\n"
"s_waitcnt vmcnt(0)\n"
".LkLBD:\n"
"v_mov_b32 v0, s20\n"
"v_mov_b32 v1, s21\n"
"v_mov_b32 v2, 0x40000\n"
"s_mov_b64 exec, 1\n"
"buffer_store_dwordx2 v[0:1], v2, s[68:71], 0 offen sc1\n"
"s_mov_b64 exec, -1\n"
".LkLEND:\n"
"s_endpgm\n"
".p2align 7\n"
".LkLTAB:\n"
"s_movk_i32 s0, 0\n"
"s_movk_i32 s0, 1\n"
"s_movk_i32 s0, 2\n"
"s_movk_i32 s0, 3\n"
"s_movk_i32 s0, 4\n"
"s_movk_i32 s0, 5\n"
"s_movk_i32 s0, 6\n"
"s_movk_i32 s0, 7\n"
"s_movk_i32 s0, 8\n"
"s_movk_i32 s0, 9\n"
"s_movk_i32 s0, 10\n"
"s_movk_i32 s0, 11\n"
"s_movk_i32 s0, 12\n"
"s_movk_i32 s0, 13\n"
"s_movk_i32 s0, 14\n"
"s_movk_i32 s0, 15\n"
"s_movk_i32 s0, 16\n"
"s_movk_i32 s0, 17\n"
"s_movk_i32 s0, 18\n"
"s_movk_i32 s0, 19\n"
"s_movk_i32 s0, 20\n"
"s_movk_i32 s0, 21\n"
"s_movk_i32 s0, 22\n"
"s_movk_i32 s0, 23\n"
"s_movk_i32 s0, 24\n"
"s_movk_i32 s0, 25\n"
"s_movk_i32 s0, 26\n"
"s_movk_i32 s0, 27\n"
"s_movk_i32 s0, 28\n"
"s_movk_i32 s0, 29\n"
"s_movk_i32 s0, 30\n"
"s_movk_i32 s0, 31\n"
"s_movk_i32 s0, 32\n"
"s_movk_i32 s0, 33\n"
"s_movk_i32 s0, 34\n"
"s_movk_i32 s0, 35\n"
"s_movk_i32 s0, 36\n"
"s_movk_i32 s0, 37\n"
"s_movk_i32 s0, 38\n"
"s_movk_i32 s0, 39\n"
"s_movk_i32 s0, 40\n"
"s_movk_i32 s0, 41\n"
"s_movk_i32 s0, 42\n"
"s_movk_i32 s0, 43\n"
"s_movk_i32 s0, 44\n"
"s_movk_i32 s0, 45\n"
"s_movk_i32 s0, 46\n"
"s_movk_i32 s0, 47\n"
"s_movk_i32 s0, 48\n"
"s_movk_i32 s0, 49\n"
"s_movk_i32 s0, 50\n"
"s_movk_i32 s0, 51\n"
"s_movk_i32 s0, 52\n"
"s_movk_i32 s0, 53\n"
"s_movk_i32 s0, 54\n"
"s_movk_i32 s0, 55\n"
"s_movk_i32 s0, 56\n"
"s_movk_i32 s0, 57\n"
"s_movk_i32 s0, 58\n"
"s_movk_i32 s0, 59\n"
"s_movk_i32 s0, 60\n"
"s_movk_i32 s0, 61\n"
"s_movk_i32 s0, 62\n"
"s_movk_i32 s0, 63\n"
".LkLSLOW:\n"
"s_waitcnt vmcnt(0) lgkmcnt(0)\n"
"s_lshl_b32 s83, s42, 18\n"
"s_add_u32 s44, s22, s83\n"
"s_addc_u32 s45, s23, 0\n"
"s_mov_b32 s46, 0x40000\n"
"s_mov_b32 s47, 0x20000\n"
"s_add_u32 s48, s24, s83\n"
"s_addc_u32 s49, s25, 0\n"
"s_mov_b32 s50, 0x40000\n"
"s_mov_b32 s51, 0x20000\n"
"v_lshrrev_b32 v0, 4, v255\n"
"v_and_b32 v1, 15, v255\n"
"v_lshlrev_b32 v2, 8, v1\n"
"v_lshl_add_u32 v246, v0, 4, v2\n"
"v_lshlrev_b32 v3, 10, v0\n"
"v_lshl_add_u32 v247, v1, 1, v3\n"
"v_add_u32 v248, 0x1000, v247\n"
"v_mov_b32 v208, 0xff800000\n"
"v_mov_b32 v209, 0xff800000\n"
"v_mov_b32 v210, 0xff800000\n"
"s_mov_b32 s83, 0\n"
".LkLSA1:\n"
"buffer_load_dwordx4 v[220:223], v246, s[44:47], s83 offen offset:0\n"
"buffer_load_dwordx4 v[224:227], v246, s[44:47], s83 offen offset:64\n"
"buffer_load_dwordx4 v[228:231], v246, s[44:47], s83 offen offset:128\n"
"buffer_load_dwordx4 v[232:235], v246, s[44:47], s83 offen offset:192\n"
"s_add_u32 s83, s83, 4096\n"
"s_waitcnt vmcnt(0)\n"
"v_mfma_f32_16x16x32_bf16 v[172:175], v[220:223], v[96:99], 0\n"
"v_mfma_f32_16x16x32_bf16 v[176:179], v[220:223], v[112:115], 0\n"
"v_mfma_f32_16x16x32_bf16 v[180:183], v[220:223], v[128:131], 0\n"
"v_mfma_f32_16x16x32_bf16 v[172:175], v[224:227], v[100:103], v[172:175]\n"
"v_mfma_f32_16x16x32_bf16 v[176:179], v[224:227], v[116:119], v[176:179]\n"
"v_mfma_f32_16x16x32_bf16 v[180:183], v[224:227], v[132:135], v[180:183]\n"
"v_mfma_f32_16x16x32_bf16 v[172:175], v[228:231], v[104:107], v[172:175]\n"
"v_mfma_f32_16x16x32_bf16 v[176:179], v[228:231], v[120:123], v[176:179]\n"
"v_mfma_f32_16x16x32_bf16 v[180:183], v[228:231], v[136:139], v[180:183]\n"
"v_mfma_f32_16x16x32_bf16 v[172:175], v[232:235], v[108:111], v[172:175]\n"
"v_mfma_f32_16x16x32_bf16 v[176:179], v[232:235], v[124:127], v[176:179]\n"
"v_mfma_f32_16x16x32_bf16 v[180:183], v[232:235], v[140:143], v[180:183]\n"
"s_nop 5\n"
"v_max3_f32 v208, v208, v172, v173\n"
"v_max3_f32 v208, v208, v174, v175\n"
"v_max3_f32 v209, v209, v176, v177\n"
"v_max3_f32 v209, v209, v178, v179\n"
"v_max3_f32 v210, v210, v180, v181\n"
"v_max3_f32 v210, v210, v182, v183\n"
"s_cmp_lt_u32 s83, 262144\n"
"s_cbranch_scc1 .LkLSA1\n"
"v_mov_b32 v212, v208\n"
"v_mov_b32 v213, v209\n"
"v_mov_b32 v214, v210\n"
"v_permlane32_swap_b32 v208, v212\n"
"v_permlane32_swap_b32 v209, v213\n"
"v_permlane32_swap_b32 v210, v214\n"
"v_max_f32 v208, v208, v212\n"
"v_max_f32 v209, v209, v213\n"
"v_max_f32 v210, v210, v214\n"
"v_mov_b32 v212, v208\n"
"v_mov_b32 v213, v209\n"
"v_mov_b32 v214, v210\n"
"v_permlane16_swap_b32 v208, v212\n"
"v_permlane16_swap_b32 v209, v213\n"
"v_permlane16_swap_b32 v210, v214\n"
"v_min_f32_e64 v156, -v208, -v212\n"
"v_min_f32_e64 v157, -v208, -v212\n"
"v_min_f32_e64 v158, -v208, -v212\n"
"v_min_f32_e64 v159, -v208, -v212\n"
"v_min_f32_e64 v160, -v209, -v213\n"
"v_min_f32_e64 v161, -v209, -v213\n"
"v_min_f32_e64 v162, -v209, -v213\n"
"v_min_f32_e64 v163, -v209, -v213\n"
"v_min_f32_e64 v164, -v210, -v214\n"
"v_min_f32_e64 v165, -v210, -v214\n"
"v_min_f32_e64 v166, -v210, -v214\n"
"v_min_f32_e64 v167, -v210, -v214\n"
"v_mov_b32 v0, 0\n"
"v_mov_b32 v1, 0\n"
"v_mov_b32 v2, 0\n"
"v_mov_b32 v3, 0\n"
"v_mov_b32 v4, 0\n"
"v_mov_b32 v5, 0\n"
"v_mov_b32 v6, 0\n"
"v_mov_b32 v7, 0\n"
"v_mov_b32 v8, 0\n"
"v_mov_b32 v9, 0\n"
"v_mov_b32 v10, 0\n"
"v_mov_b32 v11, 0\n"
"v_mov_b32 v12, 0\n"
"v_mov_b32 v13, 0\n"
"v_mov_b32 v14, 0\n"
"v_mov_b32 v15, 0\n"
"v_mov_b32 v16, 0\n"
"v_mov_b32 v17, 0\n"
"v_mov_b32 v18, 0\n"
"v_mov_b32 v19, 0\n"
"v_mov_b32 v20, 0\n"
"v_mov_b32 v21, 0\n"
"v_mov_b32 v22, 0\n"
"v_mov_b32 v23, 0\n"
"v_mov_b32 v24, 0\n"
"v_mov_b32 v25, 0\n"
"v_mov_b32 v26, 0\n"
"v_mov_b32 v27, 0\n"
"v_mov_b32 v28, 0\n"
"v_mov_b32 v29, 0\n"
"v_mov_b32 v30, 0\n"
"v_mov_b32 v31, 0\n"
"v_mov_b32 v32, 0\n"
"v_mov_b32 v33, 0\n"
"v_mov_b32 v34, 0\n"
"v_mov_b32 v35, 0\n"
"v_mov_b32 v36, 0\n"
"v_mov_b32 v37, 0\n"
"v_mov_b32 v38, 0\n"
"v_mov_b32 v39, 0\n"
"v_mov_b32 v40, 0\n"
"v_mov_b32 v41, 0\n"
"v_mov_b32 v42, 0\n"
"v_mov_b32 v43, 0\n"
"v_mov_b32 v44, 0\n"
"v_mov_b32 v45, 0\n"
"v_mov_b32 v46, 0\n"
"v_mov_b32 v47, 0\n"
"v_mov_b32 v48, 0\n"
"v_mov_b32 v49, 0\n"
"v_mov_b32 v50, 0\n"
"v_mov_b32 v51, 0\n"
"v_mov_b32 v52, 0\n"
"v_mov_b32 v53, 0\n"
"v_mov_b32 v54, 0\n"
"v_mov_b32 v55, 0\n"
"v_mov_b32 v56, 0\n"
"v_mov_b32 v57, 0\n"
"v_mov_b32 v58, 0\n"
"v_mov_b32 v59, 0\n"
"v_mov_b32 v60, 0\n"
"v_mov_b32 v61, 0\n"
"v_mov_b32 v62, 0\n"
"v_mov_b32 v63, 0\n"
"v_mov_b32 v64, 0\n"
"v_mov_b32 v65, 0\n"
"v_mov_b32 v66, 0\n"
"v_mov_b32 v67, 0\n"
"v_mov_b32 v68, 0\n"
"v_mov_b32 v69, 0\n"
"v_mov_b32 v70, 0\n"
"v_mov_b32 v71, 0\n"
"v_mov_b32 v72, 0\n"
"v_mov_b32 v73, 0\n"
"v_mov_b32 v74, 0\n"
"v_mov_b32 v75, 0\n"
"v_mov_b32 v76, 0\n"
"v_mov_b32 v77, 0\n"
"v_mov_b32 v78, 0\n"
"v_mov_b32 v79, 0\n"
"v_mov_b32 v80, 0\n"
"v_mov_b32 v81, 0\n"
"v_mov_b32 v82, 0\n"
"v_mov_b32 v83, 0\n"
"v_mov_b32 v84, 0\n"
"v_mov_b32 v85, 0\n"
"v_mov_b32 v86, 0\n"
"v_mov_b32 v87, 0\n"
"v_mov_b32 v88, 0\n"
"v_mov_b32 v89, 0\n"
"v_mov_b32 v90, 0\n"
"v_mov_b32 v91, 0\n"
"v_mov_b32 v92, 0\n"
"v_mov_b32 v93, 0\n"
"v_mov_b32 v94, 0\n"
"v_mov_b32 v95, 0\n"
"v_mov_b32 v144, 0\n"
"v_mov_b32 v145, 0\n"
"v_mov_b32 v146, 0\n"
"v_mov_b32 v147, 0\n"
"v_mov_b32 v148, 0\n"
"v_mov_b32 v149, 0\n"
"v_mov_b32 v150, 0\n"
"v_mov_b32 v151, 0\n"
"v_mov_b32 v152, 0\n"
"v_mov_b32 v153, 0\n"
"v_mov_b32 v154, 0\n"
"v_mov_b32 v155, 0\n"
"s_mov_b32 s83, 0\n"
"s_mov_b32 s84, 0\n"
".LkLSA3:\n"
"buffer_load_dwordx4 v[220:223], v246, s[44:47], s83 offen offset:0\n"
"buffer_load_dwordx4 v[224:227], v246, s[44:47], s83 offen offset:64\n"
"buffer_load_dwordx4 v[228:231], v246, s[44:47], s83 offen offset:128\n"
"buffer_load_dwordx4 v[232:235], v246, s[44:47], s83 offen offset:192\n"
"s_add_u32 s83, s83, 4096\n"
"s_waitcnt vmcnt(0)\n"
"v_mfma_f32_16x16x32_bf16 v[172:175], v[220:223], v[96:99], v[156:159]\n"
"v_mfma_f32_16x16x32_bf16 v[176:179], v[220:223], v[112:115], v[160:163]\n"
"v_mfma_f32_16x16x32_bf16 v[180:183], v[220:223], v[128:131], v[164:167]\n"
"v_mfma_f32_16x16x32_bf16 v[172:175], v[224:227], v[100:103], v[172:175]\n"
"v_mfma_f32_16x16x32_bf16 v[176:179], v[224:227], v[116:119], v[176:179]\n"
"v_mfma_f32_16x16x32_bf16 v[180:183], v[224:227], v[132:135], v[180:183]\n"
"v_mfma_f32_16x16x32_bf16 v[172:175], v[228:231], v[104:107], v[172:175]\n"
"v_mfma_f32_16x16x32_bf16 v[176:179], v[228:231], v[120:123], v[176:179]\n"
"v_mfma_f32_16x16x32_bf16 v[180:183], v[228:231], v[136:139], v[180:183]\n"
"v_mfma_f32_16x16x32_bf16 v[172:175], v[232:235], v[108:111], v[172:175]\n"
"v_mfma_f32_16x16x32_bf16 v[176:179], v[232:235], v[124:127], v[176:179]\n"
"v_mfma_f32_16x16x32_bf16 v[180:183], v[232:235], v[140:143], v[180:183]\n"
"buffer_load_dwordx4 v[220:223], v246, s[44:47], s83 offen offset:0\n"
"buffer_load_dwordx4 v[224:227], v246, s[44:47], s83 offen offset:64\n"
"buffer_load_dwordx4 v[228:231], v246, s[44:47], s83 offen offset:128\n"
"buffer_load_dwordx4 v[232:235], v246, s[44:47], s83 offen offset:192\n"
"s_add_u32 s83, s83, 4096\n"
"s_waitcnt vmcnt(0)\n"
"v_mfma_f32_16x16x32_bf16 v[184:187], v[220:223], v[96:99], v[156:159]\n"
"v_mfma_f32_16x16x32_bf16 v[188:191], v[220:223], v[112:115], v[160:163]\n"
"v_mfma_f32_16x16x32_bf16 v[192:195], v[220:223], v[128:131], v[164:167]\n"
"v_mfma_f32_16x16x32_bf16 v[184:187], v[224:227], v[100:103], v[184:187]\n"
"v_mfma_f32_16x16x32_bf16 v[188:191], v[224:227], v[116:119], v[188:191]\n"
"v_mfma_f32_16x16x32_bf16 v[192:195], v[224:227], v[132:135], v[192:195]\n"
"v_mfma_f32_16x16x32_bf16 v[184:187], v[228:231], v[104:107], v[184:187]\n"
"v_mfma_f32_16x16x32_bf16 v[188:191], v[228:231], v[120:123], v[188:191]\n"
"v_mfma_f32_16x16x32_bf16 v[192:195], v[228:231], v[136:139], v[192:195]\n"
"v_mfma_f32_16x16x32_bf16 v[184:187], v[232:235], v[108:111], v[184:187]\n"
"v_mfma_f32_16x16x32_bf16 v[188:191], v[232:235], v[124:127], v[188:191]\n"
"v_mfma_f32_16x16x32_bf16 v[192:195], v[232:235], v[140:143], v[192:195]\n"
"v_exp_f32 v172, v172\n"
"v_exp_f32 v173, v173\n"
"v_exp_f32 v174, v174\n"
"v_exp_f32 v175, v175\n"
"v_exp_f32 v176, v176\n"
"v_exp_f32 v177, v177\n"
"v_exp_f32 v178, v178\n"
"v_exp_f32 v179, v179\n"
"v_exp_f32 v180, v180\n"
"v_exp_f32 v181, v181\n"
"v_exp_f32 v182, v182\n"
"v_exp_f32 v183, v183\n"
"v_cvt_pk_bf16_f32 v196, v172, v173\n"
"v_cvt_pk_bf16_f32 v197, v174, v175\n"
"v_cvt_pk_bf16_f32 v200, v176, v177\n"
"v_cvt_pk_bf16_f32 v201, v178, v179\n"
"v_cvt_pk_bf16_f32 v204, v180, v181\n"
"v_cvt_pk_bf16_f32 v205, v182, v183\n"
"v_exp_f32 v184, v184\n"
"v_exp_f32 v185, v185\n"
"v_exp_f32 v186, v186\n"
"v_exp_f32 v187, v187\n"
"v_exp_f32 v188, v188\n"
"v_exp_f32 v189, v189\n"
"v_exp_f32 v190, v190\n"
"v_exp_f32 v191, v191\n"
"v_exp_f32 v192, v192\n"
"v_exp_f32 v193, v193\n"
"v_exp_f32 v194, v194\n"
"v_exp_f32 v195, v195\n"
"v_cvt_pk_bf16_f32 v198, v184, v185\n"
"v_cvt_pk_bf16_f32 v199, v186, v187\n"
"v_cvt_pk_bf16_f32 v202, v188, v189\n"
"v_cvt_pk_bf16_f32 v203, v190, v191\n"
"v_cvt_pk_bf16_f32 v206, v192, v193\n"
"v_cvt_pk_bf16_f32 v207, v194, v195\n"
"v_mfma_f32_16x16x32_bf16 v[144:147], v[168:171], v[196:199], v[144:147]\n"
"v_mfma_f32_16x16x32_bf16 v[148:151], v[168:171], v[200:203], v[148:151]\n"
"v_mfma_f32_16x16x32_bf16 v[152:155], v[168:171], v[204:207], v[152:155]\n"
"buffer_load_ushort v220, v247, s[48:51], s84 offen offset:0\n"
"buffer_load_ushort v221, v247, s[48:51], s84 offen offset:256\n"
"buffer_load_ushort v222, v247, s[48:51], s84 offen offset:512\n"
"buffer_load_ushort v223, v247, s[48:51], s84 offen offset:768\n"
"buffer_load_ushort v224, v248, s[48:51], s84 offen offset:0\n"
"buffer_load_ushort v225, v248, s[48:51], s84 offen offset:256\n"
"buffer_load_ushort v226, v248, s[48:51], s84 offen offset:512\n"
"buffer_load_ushort v227, v248, s[48:51], s84 offen offset:768\n"
"s_waitcnt vmcnt(0)\n"
"v_lshl_or_b32 v228, v221, 16, v220\n"
"v_lshl_or_b32 v229, v223, 16, v222\n"
"v_lshl_or_b32 v230, v225, 16, v224\n"
"v_lshl_or_b32 v231, v227, 16, v226\n"
"s_nop 1\n"
"v_mfma_f32_16x16x32_bf16 v[0:3], v[228:231], v[196:199], v[0:3]\n"
"v_mfma_f32_16x16x32_bf16 v[32:35], v[228:231], v[200:203], v[32:35]\n"
"v_mfma_f32_16x16x32_bf16 v[64:67], v[228:231], v[204:207], v[64:67]\n"
"buffer_load_ushort v220, v247, s[48:51], s84 offen offset:32\n"
"buffer_load_ushort v221, v247, s[48:51], s84 offen offset:288\n"
"buffer_load_ushort v222, v247, s[48:51], s84 offen offset:544\n"
"buffer_load_ushort v223, v247, s[48:51], s84 offen offset:800\n"
"buffer_load_ushort v224, v248, s[48:51], s84 offen offset:32\n"
"buffer_load_ushort v225, v248, s[48:51], s84 offen offset:288\n"
"buffer_load_ushort v226, v248, s[48:51], s84 offen offset:544\n"
"buffer_load_ushort v227, v248, s[48:51], s84 offen offset:800\n"
"s_waitcnt vmcnt(0)\n"
"v_lshl_or_b32 v228, v221, 16, v220\n"
"v_lshl_or_b32 v229, v223, 16, v222\n"
"v_lshl_or_b32 v230, v225, 16, v224\n"
"v_lshl_or_b32 v231, v227, 16, v226\n"
"s_nop 1\n"
"v_mfma_f32_16x16x32_bf16 v[4:7], v[228:231], v[196:199], v[4:7]\n"
"v_mfma_f32_16x16x32_bf16 v[36:39], v[228:231], v[200:203], v[36:39]\n"
"v_mfma_f32_16x16x32_bf16 v[68:71], v[228:231], v[204:207], v[68:71]\n"
"buffer_load_ushort v220, v247, s[48:51], s84 offen offset:64\n"
"buffer_load_ushort v221, v247, s[48:51], s84 offen offset:320\n"
"buffer_load_ushort v222, v247, s[48:51], s84 offen offset:576\n"
"buffer_load_ushort v223, v247, s[48:51], s84 offen offset:832\n"
"buffer_load_ushort v224, v248, s[48:51], s84 offen offset:64\n"
"buffer_load_ushort v225, v248, s[48:51], s84 offen offset:320\n"
"buffer_load_ushort v226, v248, s[48:51], s84 offen offset:576\n"
"buffer_load_ushort v227, v248, s[48:51], s84 offen offset:832\n"
"s_waitcnt vmcnt(0)\n"
"v_lshl_or_b32 v228, v221, 16, v220\n"
"v_lshl_or_b32 v229, v223, 16, v222\n"
"v_lshl_or_b32 v230, v225, 16, v224\n"
"v_lshl_or_b32 v231, v227, 16, v226\n"
"s_nop 1\n"
"v_mfma_f32_16x16x32_bf16 v[8:11], v[228:231], v[196:199], v[8:11]\n"
"v_mfma_f32_16x16x32_bf16 v[40:43], v[228:231], v[200:203], v[40:43]\n"
"v_mfma_f32_16x16x32_bf16 v[72:75], v[228:231], v[204:207], v[72:75]\n"
"buffer_load_ushort v220, v247, s[48:51], s84 offen offset:96\n"
"buffer_load_ushort v221, v247, s[48:51], s84 offen offset:352\n"
"buffer_load_ushort v222, v247, s[48:51], s84 offen offset:608\n"
"buffer_load_ushort v223, v247, s[48:51], s84 offen offset:864\n"
"buffer_load_ushort v224, v248, s[48:51], s84 offen offset:96\n"
"buffer_load_ushort v225, v248, s[48:51], s84 offen offset:352\n"
"buffer_load_ushort v226, v248, s[48:51], s84 offen offset:608\n"
"buffer_load_ushort v227, v248, s[48:51], s84 offen offset:864\n"
"s_waitcnt vmcnt(0)\n"
"v_lshl_or_b32 v228, v221, 16, v220\n"
"v_lshl_or_b32 v229, v223, 16, v222\n"
"v_lshl_or_b32 v230, v225, 16, v224\n"
"v_lshl_or_b32 v231, v227, 16, v226\n"
"s_nop 1\n"
"v_mfma_f32_16x16x32_bf16 v[12:15], v[228:231], v[196:199], v[12:15]\n"
"v_mfma_f32_16x16x32_bf16 v[44:47], v[228:231], v[200:203], v[44:47]\n"
"v_mfma_f32_16x16x32_bf16 v[76:79], v[228:231], v[204:207], v[76:79]\n"
"buffer_load_ushort v220, v247, s[48:51], s84 offen offset:128\n"
"buffer_load_ushort v221, v247, s[48:51], s84 offen offset:384\n"
"buffer_load_ushort v222, v247, s[48:51], s84 offen offset:640\n"
"buffer_load_ushort v223, v247, s[48:51], s84 offen offset:896\n"
"buffer_load_ushort v224, v248, s[48:51], s84 offen offset:128\n"
"buffer_load_ushort v225, v248, s[48:51], s84 offen offset:384\n"
"buffer_load_ushort v226, v248, s[48:51], s84 offen offset:640\n"
"buffer_load_ushort v227, v248, s[48:51], s84 offen offset:896\n"
"s_waitcnt vmcnt(0)\n"
"v_lshl_or_b32 v228, v221, 16, v220\n"
"v_lshl_or_b32 v229, v223, 16, v222\n"
"v_lshl_or_b32 v230, v225, 16, v224\n"
"v_lshl_or_b32 v231, v227, 16, v226\n"
"s_nop 1\n"
"v_mfma_f32_16x16x32_bf16 v[16:19], v[228:231], v[196:199], v[16:19]\n"
"v_mfma_f32_16x16x32_bf16 v[48:51], v[228:231], v[200:203], v[48:51]\n"
"v_mfma_f32_16x16x32_bf16 v[80:83], v[228:231], v[204:207], v[80:83]\n"
"buffer_load_ushort v220, v247, s[48:51], s84 offen offset:160\n"
"buffer_load_ushort v221, v247, s[48:51], s84 offen offset:416\n"
"buffer_load_ushort v222, v247, s[48:51], s84 offen offset:672\n"
"buffer_load_ushort v223, v247, s[48:51], s84 offen offset:928\n"
"buffer_load_ushort v224, v248, s[48:51], s84 offen offset:160\n"
"buffer_load_ushort v225, v248, s[48:51], s84 offen offset:416\n"
"buffer_load_ushort v226, v248, s[48:51], s84 offen offset:672\n"
"buffer_load_ushort v227, v248, s[48:51], s84 offen offset:928\n"
"s_waitcnt vmcnt(0)\n"
"v_lshl_or_b32 v228, v221, 16, v220\n"
"v_lshl_or_b32 v229, v223, 16, v222\n"
"v_lshl_or_b32 v230, v225, 16, v224\n"
"v_lshl_or_b32 v231, v227, 16, v226\n"
"s_nop 1\n"
"v_mfma_f32_16x16x32_bf16 v[20:23], v[228:231], v[196:199], v[20:23]\n"
"v_mfma_f32_16x16x32_bf16 v[52:55], v[228:231], v[200:203], v[52:55]\n"
"v_mfma_f32_16x16x32_bf16 v[84:87], v[228:231], v[204:207], v[84:87]\n"
"buffer_load_ushort v220, v247, s[48:51], s84 offen offset:192\n"
"buffer_load_ushort v221, v247, s[48:51], s84 offen offset:448\n"
"buffer_load_ushort v222, v247, s[48:51], s84 offen offset:704\n"
"buffer_load_ushort v223, v247, s[48:51], s84 offen offset:960\n"
"buffer_load_ushort v224, v248, s[48:51], s84 offen offset:192\n"
"buffer_load_ushort v225, v248, s[48:51], s84 offen offset:448\n"
"buffer_load_ushort v226, v248, s[48:51], s84 offen offset:704\n"
"buffer_load_ushort v227, v248, s[48:51], s84 offen offset:960\n"
"s_waitcnt vmcnt(0)\n"
"v_lshl_or_b32 v228, v221, 16, v220\n"
"v_lshl_or_b32 v229, v223, 16, v222\n"
"v_lshl_or_b32 v230, v225, 16, v224\n"
"v_lshl_or_b32 v231, v227, 16, v226\n"
"s_nop 1\n"
"v_mfma_f32_16x16x32_bf16 v[24:27], v[228:231], v[196:199], v[24:27]\n"
"v_mfma_f32_16x16x32_bf16 v[56:59], v[228:231], v[200:203], v[56:59]\n"
"v_mfma_f32_16x16x32_bf16 v[88:91], v[228:231], v[204:207], v[88:91]\n"
"buffer_load_ushort v220, v247, s[48:51], s84 offen offset:224\n"
"buffer_load_ushort v221, v247, s[48:51], s84 offen offset:480\n"
"buffer_load_ushort v222, v247, s[48:51], s84 offen offset:736\n"
"buffer_load_ushort v223, v247, s[48:51], s84 offen offset:992\n"
"buffer_load_ushort v224, v248, s[48:51], s84 offen offset:224\n"
"buffer_load_ushort v225, v248, s[48:51], s84 offen offset:480\n"
"buffer_load_ushort v226, v248, s[48:51], s84 offen offset:736\n"
"buffer_load_ushort v227, v248, s[48:51], s84 offen offset:992\n"
"s_waitcnt vmcnt(0)\n"
"v_lshl_or_b32 v228, v221, 16, v220\n"
"v_lshl_or_b32 v229, v223, 16, v222\n"
"v_lshl_or_b32 v230, v225, 16, v224\n"
"v_lshl_or_b32 v231, v227, 16, v226\n"
"s_nop 1\n"
"v_mfma_f32_16x16x32_bf16 v[28:31], v[228:231], v[196:199], v[28:31]\n"
"v_mfma_f32_16x16x32_bf16 v[60:63], v[228:231], v[200:203], v[60:63]\n"
"v_mfma_f32_16x16x32_bf16 v[92:95], v[228:231], v[204:207], v[92:95]\n"
"s_add_u32 s84, s84, 8192\n"
"s_cmp_lt_u32 s84, 262144\n"
"s_cbranch_scc1 .LkLSA3\n"
"v_rcp_f32 v146, v144\n"
"s_nop 0\n"
"v_pk_mul_f32 v[0:1], v[0:1], v[146:147] op_sel_hi:[1,0]\n"
"v_pk_mul_f32 v[2:3], v[2:3], v[146:147] op_sel_hi:[1,0]\n"
"v_pk_mul_f32 v[4:5], v[4:5], v[146:147] op_sel_hi:[1,0]\n"
"v_pk_mul_f32 v[6:7], v[6:7], v[146:147] op_sel_hi:[1,0]\n"
"v_cvt_pk_bf16_f32 v0, v0, v1\n"
"v_cvt_pk_bf16_f32 v1, v2, v3\n"
"v_cvt_pk_bf16_f32 v2, v4, v5\n"
"v_cvt_pk_bf16_f32 v3, v6, v7\n"
"buffer_store_dwordx4 v[0:3], v252, s[64:67], 0 offen offset:0 sc1\n"
"v_pk_mul_f32 v[8:9], v[8:9], v[146:147] op_sel_hi:[1,0]\n"
"v_pk_mul_f32 v[10:11], v[10:11], v[146:147] op_sel_hi:[1,0]\n"
"v_pk_mul_f32 v[12:13], v[12:13], v[146:147] op_sel_hi:[1,0]\n"
"v_pk_mul_f32 v[14:15], v[14:15], v[146:147] op_sel_hi:[1,0]\n"
"v_cvt_pk_bf16_f32 v8, v8, v9\n"
"v_cvt_pk_bf16_f32 v9, v10, v11\n"
"v_cvt_pk_bf16_f32 v10, v12, v13\n"
"v_cvt_pk_bf16_f32 v11, v14, v15\n"
"buffer_store_dwordx4 v[8:11], v252, s[64:67], 0 offen offset:3072 sc1\n"
"v_pk_mul_f32 v[16:17], v[16:17], v[146:147] op_sel_hi:[1,0]\n"
"v_pk_mul_f32 v[18:19], v[18:19], v[146:147] op_sel_hi:[1,0]\n"
"v_pk_mul_f32 v[20:21], v[20:21], v[146:147] op_sel_hi:[1,0]\n"
"v_pk_mul_f32 v[22:23], v[22:23], v[146:147] op_sel_hi:[1,0]\n"
"v_cvt_pk_bf16_f32 v16, v16, v17\n"
"v_cvt_pk_bf16_f32 v17, v18, v19\n"
"v_cvt_pk_bf16_f32 v18, v20, v21\n"
"v_cvt_pk_bf16_f32 v19, v22, v23\n"
"buffer_store_dwordx4 v[16:19], v252, s[64:67], s81 offen offset:2048 sc1\n"
"v_pk_mul_f32 v[24:25], v[24:25], v[146:147] op_sel_hi:[1,0]\n"
"v_pk_mul_f32 v[26:27], v[26:27], v[146:147] op_sel_hi:[1,0]\n"
"v_pk_mul_f32 v[28:29], v[28:29], v[146:147] op_sel_hi:[1,0]\n"
"v_pk_mul_f32 v[30:31], v[30:31], v[146:147] op_sel_hi:[1,0]\n"
"v_cvt_pk_bf16_f32 v24, v24, v25\n"
"v_cvt_pk_bf16_f32 v25, v26, v27\n"
"v_cvt_pk_bf16_f32 v26, v28, v29\n"
"v_cvt_pk_bf16_f32 v27, v30, v31\n"
"buffer_store_dwordx4 v[24:27], v252, s[64:67], s82 offen offset:1024 sc1\n"
"v_rcp_f32 v150, v148\n"
"s_nop 0\n"
"v_pk_mul_f32 v[32:33], v[32:33], v[150:151] op_sel_hi:[1,0]\n"
"v_pk_mul_f32 v[34:35], v[34:35], v[150:151] op_sel_hi:[1,0]\n"
"v_pk_mul_f32 v[36:37], v[36:37], v[150:151] op_sel_hi:[1,0]\n"
"v_pk_mul_f32 v[38:39], v[38:39], v[150:151] op_sel_hi:[1,0]\n"
"v_cvt_pk_bf16_f32 v32, v32, v33\n"
"v_cvt_pk_bf16_f32 v33, v34, v35\n"
"v_cvt_pk_bf16_f32 v34, v36, v37\n"
"v_cvt_pk_bf16_f32 v35, v38, v39\n"
"buffer_store_dwordx4 v[32:35], v252, s[64:67], 0 offen offset:1024 sc1\n"
"v_pk_mul_f32 v[40:41], v[40:41], v[150:151] op_sel_hi:[1,0]\n"
"v_pk_mul_f32 v[42:43], v[42:43], v[150:151] op_sel_hi:[1,0]\n"
"v_pk_mul_f32 v[44:45], v[44:45], v[150:151] op_sel_hi:[1,0]\n"
"v_pk_mul_f32 v[46:47], v[46:47], v[150:151] op_sel_hi:[1,0]\n"
"v_cvt_pk_bf16_f32 v40, v40, v41\n"
"v_cvt_pk_bf16_f32 v41, v42, v43\n"
"v_cvt_pk_bf16_f32 v42, v44, v45\n"
"v_cvt_pk_bf16_f32 v43, v46, v47\n"
"buffer_store_dwordx4 v[40:43], v252, s[64:67], s81 offen offset:0 sc1\n"
"v_pk_mul_f32 v[48:49], v[48:49], v[150:151] op_sel_hi:[1,0]\n"
"v_pk_mul_f32 v[50:51], v[50:51], v[150:151] op_sel_hi:[1,0]\n"
"v_pk_mul_f32 v[52:53], v[52:53], v[150:151] op_sel_hi:[1,0]\n"
"v_pk_mul_f32 v[54:55], v[54:55], v[150:151] op_sel_hi:[1,0]\n"
"v_cvt_pk_bf16_f32 v48, v48, v49\n"
"v_cvt_pk_bf16_f32 v49, v50, v51\n"
"v_cvt_pk_bf16_f32 v50, v52, v53\n"
"v_cvt_pk_bf16_f32 v51, v54, v55\n"
"buffer_store_dwordx4 v[48:51], v252, s[64:67], s81 offen offset:3072 sc1\n"
"v_pk_mul_f32 v[56:57], v[56:57], v[150:151] op_sel_hi:[1,0]\n"
"v_pk_mul_f32 v[58:59], v[58:59], v[150:151] op_sel_hi:[1,0]\n"
"v_pk_mul_f32 v[60:61], v[60:61], v[150:151] op_sel_hi:[1,0]\n"
"v_pk_mul_f32 v[62:63], v[62:63], v[150:151] op_sel_hi:[1,0]\n"
"v_cvt_pk_bf16_f32 v56, v56, v57\n"
"v_cvt_pk_bf16_f32 v57, v58, v59\n"
"v_cvt_pk_bf16_f32 v58, v60, v61\n"
"v_cvt_pk_bf16_f32 v59, v62, v63\n"
"buffer_store_dwordx4 v[56:59], v252, s[64:67], s82 offen offset:2048 sc1\n"
"v_rcp_f32 v154, v152\n"
"s_nop 0\n"
"v_pk_mul_f32 v[64:65], v[64:65], v[154:155] op_sel_hi:[1,0]\n"
"v_pk_mul_f32 v[66:67], v[66:67], v[154:155] op_sel_hi:[1,0]\n"
"v_pk_mul_f32 v[68:69], v[68:69], v[154:155] op_sel_hi:[1,0]\n"
"v_pk_mul_f32 v[70:71], v[70:71], v[154:155] op_sel_hi:[1,0]\n"
"v_cvt_pk_bf16_f32 v64, v64, v65\n"
"v_cvt_pk_bf16_f32 v65, v66, v67\n"
"v_cvt_pk_bf16_f32 v66, v68, v69\n"
"v_cvt_pk_bf16_f32 v67, v70, v71\n"
"buffer_store_dwordx4 v[64:67], v252, s[64:67], 0 offen offset:2048 sc1\n"
"v_pk_mul_f32 v[72:73], v[72:73], v[154:155] op_sel_hi:[1,0]\n"
"v_pk_mul_f32 v[74:75], v[74:75], v[154:155] op_sel_hi:[1,0]\n"
"v_pk_mul_f32 v[76:77], v[76:77], v[154:155] op_sel_hi:[1,0]\n"
"v_pk_mul_f32 v[78:79], v[78:79], v[154:155] op_sel_hi:[1,0]\n"
"v_cvt_pk_bf16_f32 v72, v72, v73\n"
"v_cvt_pk_bf16_f32 v73, v74, v75\n"
"v_cvt_pk_bf16_f32 v74, v76, v77\n"
"v_cvt_pk_bf16_f32 v75, v78, v79\n"
"buffer_store_dwordx4 v[72:75], v252, s[64:67], s81 offen offset:1024 sc1\n"
"v_pk_mul_f32 v[80:81], v[80:81], v[154:155] op_sel_hi:[1,0]\n"
"v_pk_mul_f32 v[82:83], v[82:83], v[154:155] op_sel_hi:[1,0]\n"
"v_pk_mul_f32 v[84:85], v[84:85], v[154:155] op_sel_hi:[1,0]\n"
"v_pk_mul_f32 v[86:87], v[86:87], v[154:155] op_sel_hi:[1,0]\n"
"v_cvt_pk_bf16_f32 v80, v80, v81\n"
"v_cvt_pk_bf16_f32 v81, v82, v83\n"
"v_cvt_pk_bf16_f32 v82, v84, v85\n"
"v_cvt_pk_bf16_f32 v83, v86, v87\n"
"buffer_store_dwordx4 v[80:83], v252, s[64:67], s82 offen offset:0 sc1\n"
"v_pk_mul_f32 v[88:89], v[88:89], v[154:155] op_sel_hi:[1,0]\n"
"v_pk_mul_f32 v[90:91], v[90:91], v[154:155] op_sel_hi:[1,0]\n"
"v_pk_mul_f32 v[92:93], v[92:93], v[154:155] op_sel_hi:[1,0]\n"
"v_pk_mul_f32 v[94:95], v[94:95], v[154:155] op_sel_hi:[1,0]\n"
"v_cvt_pk_bf16_f32 v88, v88, v89\n"
"v_cvt_pk_bf16_f32 v89, v90, v91\n"
"v_cvt_pk_bf16_f32 v90, v92, v93\n"
"v_cvt_pk_bf16_f32 v91, v94, v95\n"
"buffer_store_dwordx4 v[88:91], v252, s[64:67], s82 offen offset:3072 sc1\n"
"s_branch .LkLPUB\n"
  :: "s"(__builtin_amdgcn_kernarg_segment_ptr()), "s"(blockIdx.x), "s"(wid)
  : "v0", "v1", "v2", "v3", "v4", "v5", "v6", "v7", "v8", "v9", "v10", "v11", "v12", "v13", "v14", "v15", "v16", "v17", "v18", "v19", "v20", "v21", "v22", "v23", "v24", "v25", "v26", "v27", "v28", "v29", "v30", "v31", "v32", "v33", "v34", "v35", "v36", "v37", "v38", "v39", "v40", "v41", "v42", "v43", "v44", "v45", "v46", "v47", "v48", "v49", "v50", "v51", "v52", "v53", "v54", "v55", "v56", "v57", "v58", "v59", "v60", "v61", "v62", "v63", "v64", "v65", "v66", "v67", "v68", "v69", "v70", "v71", "v72", "v73", "v74", "v75", "v76", "v77", "v78", "v79", "v80", "v81", "v82", "v83", "v84", "v85", "v86", "v87", "v88", "v89", "v90", "v91", "v92", "v93", "v94", "v95", "v96", "v97", "v98", "v99", "v100", "v101", "v102", "v103", "v104", "v105", "v106", "v107", "v108", "v109", "v110", "v111", "v112", "v113", "v114", "v115", "v116", "v117", "v118", "v119", "v120", "v121", "v122", "v123", "v124", "v125", "v126", "v127", "v128", "v129", "v130", "v131", "v132", "v133", "v134", "v135", "v136", "v137", "v138", "v139", "v140", "v141", "v142", "v143", "v144", "v145", "v146", "v147", "v148", "v149", "v150", "v151", "v152", "v153", "v154", "v155", "v156", "v157", "v158", "v159", "v160", "v161", "v162", "v163", "v164", "v165", "v166", "v167", "v168", "v169", "v170", "v171", "v172", "v173", "v174", "v175", "v176", "v177", "v178", "v179", "v180", "v181", "v182", "v183", "v184", "v185", "v186", "v187", "v188", "v189", "v190", "v191", "v192", "v193", "v194", "v195", "v196", "v197", "v198", "v199", "v200", "v201", "v202", "v203", "v204", "v205", "v206", "v207", "v208", "v209", "v210", "v211", "v212", "v213", "v214", "v215", "v216", "v217", "v218", "v219", "v220", "v221", "v222", "v223", "v224", "v225", "v226", "v227", "v228", "v229", "v230", "v231", "v232", "v233", "v234", "v235", "v236", "v237", "v238", "v239", "v240", "v241", "v242", "v243", "v244", "v245", "v246", "v247", "v248", "v249", "v250", "v251", "v252", "v253", "v254", "v255", "s20", "s21", "s22", "s23", "s24", "s25", "s26", "s27", "s28", "s29", "s30", "s31", "s32", "s33", "s34", "s35", "s36", "s37", "s38", "s39", "s40", "s41", "s42", "s43", "s44", "s45", "s46", "s47", "s48", "s49", "s50", "s51", "s52", "s53", "s54", "s55", "s56", "s57", "s58", "s59", "s60", "s61", "s62", "s63", "s64", "s65", "s66", "s67", "s68", "s69", "s70", "s71", "s72", "s73", "s74", "s75", "s76", "s77", "s78", "s79", "s80", "s81", "s82", "s83", "s84", "s85", "s86", "s87", "s88", "s89", "s90", "s91", "s92", "s93", "s94", "s95", "s96", "s97", "s98", "s99", "s100", "s101", "vcc", "scc", "m0", "memory");
}
extern "C" void kernel_launch(void* const* d_in, const int* in_sizes, int n_in, void* d_out, int out_size, void* d_ws, size_t ws_size, hipStream_t stream) {
  hipFuncSetAttribute((const void*)fused_k, hipFuncAttributeMaxDynamicSharedMemorySize, 163840);
  fused_k<<<256, 512, 163840, stream>>>(d_in[0], d_in[1], d_in[2], d_in[3], d_in[4], d_out, d_ws);
}
```
